```python
import torch

def _softmax(x, axis=-1):
    return torch.softmax(x, dim=axis)
def _gelu(x):
    return torch.nn.functional.gelu(x, approximate="tanh")
def _silu(x):
    return torch.nn.functional.silu(x)
def _sigmoid(x):
    return torch.sigmoid(x)
def _layernorm(x, axis=-1, eps=1e-5):
    m = x.mean(dim=axis, keepdim=True)
    v = x.var(dim=axis, keepdim=True, unbiased=False)
    return (x - m) / torch.sqrt(v + eps)
def _scatter_add(tgt, idx, src, axis=0):
    return tgt.clone().index_add_(axis, idx, src)
def _segment_sum(src, seg, n):
    out = torch.zeros((n,) + src.shape[1:], dtype=src.dtype)
    return out.index_add_(0, seg, src)
def _pad(x, pw):
    flat = tuple(v for pair in reversed(pw) for v in pair)
    return torch.nn.functional.pad(x, flat)

kernel_name = 'concat_heads__compressed_kv_cla__sliding_block_cached__decode'

def setup_inputs():

    _w = (lambda a: (lambda t: t.to(torch.bfloat16) if t.is_floating_point() else t)(torch.as_tensor(a)))
    def _A_setup():
        B, H, Sq, Sk, D, M = 2, 32, 1, 8192, 128, 512
        Dc = 4 * D
        return {
            "x": _w((lambda *s: torch.randn(*s, dtype=torch.bfloat16) * 3.2)(B, Sq, M)),
            "c_kv": _w((lambda *s: torch.randn(*s, dtype=torch.bfloat16) * 0.1)(B, Sk, Dc)),
            "w_q": _w((lambda *s: torch.randn(*s, dtype=torch.bfloat16) * 6.4)(M, H, D) * (M ** -0.5)),
            "w_uk": _w((lambda *s: torch.randn(*s, dtype=torch.bfloat16) * 6.4)(Dc, H, D) * (Dc ** -0.5)),
            "w_uv": _w((lambda *s: torch.randn(*s, dtype=torch.bfloat16) * 0.1)(Dc, H, D) * (Dc ** -0.5)),
            "w_o": _w((lambda *s: torch.randn(*s, dtype=torch.bfloat16) * 0.1)(H, D, M) * ((H * D) ** -0.5)),
        }
    _A_in = _A_setup()

    def _B_setup():
        B, Sq, D = 2, 1, 512
        Hq, Hkv, Sk, Dh = 32, 8, 8192, 128
        assert Hq % Hkv == 0, (Hq, Hkv)
        return {
            "x": _w((lambda *s: torch.randn(*s, dtype=torch.bfloat16) * 1.6)(B, Sq, D)),
            "k_cache": _w((lambda *s: torch.randn(*s, dtype=torch.bfloat16) * 1.6)(B, Hkv, Sk, Dh)),
            "v_cache": _w((lambda *s: torch.randn(*s, dtype=torch.bfloat16) * 0.05)(B, Hkv, Sk, Dh)),
            "wq": _w((lambda *s: torch.randn(*s, dtype=torch.bfloat16) * 1.6)(D, Hq * Dh) * (D ** -0.5)),
            "wo": _w((lambda *s: torch.randn(*s, dtype=torch.bfloat16) * 0.05)(Hq * Dh, D) * ((Hq * Dh) ** -0.5)),
        }
    _B_in = _B_setup()

    return {
        "A_x": _A_in["x"],
        "A_c_kv": _A_in["c_kv"],
        "A_w_q": _A_in["w_q"],
        "A_w_uk": _A_in["w_uk"],
        "A_w_uv": _A_in["w_uv"],
        "A_w_o": _A_in["w_o"],
        "B_x": _B_in["x"],
        "B_k_cache": _B_in["k_cache"],
        "B_v_cache": _B_in["v_cache"],
        "B_wq": _B_in["wq"],
        "B_wo": _B_in["wo"],
    }

def reference(A_x, A_c_kv, A_w_q, A_w_uk, A_w_uv, A_w_o, B_x, B_k_cache, B_v_cache, B_wq, B_wo):
    def _A_ref(x, c_kv, w_q, w_uk, w_uv, w_o):
        B, Sq, M = x.shape[0], x.shape[1], x.shape[2]
        Sk = c_kv.shape[1]
        H, D = w_q.shape[1], w_q.shape[2]
        q = torch.einsum("bqm,mhd->bhqd", x, w_q)
        k = torch.einsum("bkc,chd->bhkd", c_kv, w_uk)
        v = torch.einsum("bkc,chd->bhkd", c_kv, w_uv)
        scores = torch.einsum("bhqd,bhkd->bhqk", q, k) * (D ** -0.5)
        off = Sk - Sq
        qi = torch.arange(Sq)[:, None]
        kj = torch.arange(Sk)[None, :]
        cmask = kj <= (qi + off)
        scores = torch.where(cmask[None, None], scores, -1e9)
        p = _softmax(scores, axis=-1)
        ctx = torch.einsum("bhqk,bhkd->bhqd", p, v)
        return torch.einsum("bhqd,hdm->bqm", ctx, w_o)

    def _B_ref(x, k_cache, v_cache, wq, wo):
        B, Sq, D = x.shape
        Hq, Dh = 32, 128
        Hkv, Sk = k_cache.shape[1], k_cache.shape[2]
        W = min(4096, max(256, Sk // 4))
        rep = Hq // Hkv
        q = (lambda x, *d: x.permute(*d))((x @ wq).reshape(B, Sq, Hq, Dh), 0, 2, 1, 3)
        k = (lambda x, n, axis: torch.repeat_interleave(x, n, dim=axis))(k_cache[:, :, Sk - W:, :], rep, axis=1)
        v = (lambda x, n, axis: torch.repeat_interleave(x, n, dim=axis))(v_cache[:, :, Sk - W:, :], rep, axis=1)
        scores = torch.einsum("bhqd,bhkd->bhqk", q, k) * (Dh ** -0.5)
        p = _softmax(scores, axis=-1)
        o = torch.einsum("bhqk,bhkd->bhqd", p, v)
        return (lambda x, *d: x.permute(*d))(o, 0, 2, 1, 3).reshape(B, Sq, Hq * Dh) @ wo

    a = _A_ref(A_x, A_c_kv, A_w_q, A_w_uk, A_w_uv, A_w_o)
    b = _B_ref(B_x, B_k_cache, B_v_cache, B_wq, B_wo)
    return torch.concatenate([a, b], axis=-1)
```

```cpp
#include <hip/hip_runtime.h>
#include <hip/hip_bf16.h>
#include <stdint.h>

typedef unsigned short u16;
__device__ __forceinline__ float bf2f(u16 b){ return __uint_as_float(((unsigned)b)<<16); }
__device__ __forceinline__ u16 f2bf(float f){ unsigned u=__float_as_uint(f); u += 0x7FFFu + ((u>>16)&1u); return (u16)(u>>16); }
__device__ __forceinline__ float rbf(float f){ return bf2f(f2bf(f)); }

constexpr int NS_A = 64;
constexpr int NS_B = 8;

__global__ void __launch_bounds__(128) k_q(const u16* xA, const u16* wqA, const u16* xB, const u16* wqB, float* q){
  int which = blockIdx.y; int col = blockIdx.x*128 + threadIdx.x;
  const u16* x = which? xB: xA; const u16* w = which? wqB: wqA;
  float a0=0,a1=0;
  for(int m=0;m<512;m++){ float wv=bf2f(w[(size_t)m*4096+col]); a0+=bf2f(x[m])*wv; a1+=bf2f(x[512+m])*wv; }
  q[which*8192+col]=rbf(a0); q[which*8192+4096+col]=rbf(a1);
}
__global__ void __launch_bounds__(64) k_qabs(const float* q, const u16* wuk, float* qabs){
  int h=blockIdx.x; int c=blockIdx.y*64+threadIdx.x;
  float a0=0,a1=0;
  for(int d=0;d<128;d++){ float wv=bf2f(wuk[(size_t)c*4096+h*128+d]); a0+=q[h*128+d]*wv; a1+=q[4096+h*128+d]*wv; }
  qabs[(0*32+h)*512+c]=a0; qabs[(1*32+h)*512+c]=a1;
}
__global__ void __launch_bounds__(128) k_attnA(const float* qabs, const u16* ckv, float* pO, float* pML){
  int s=blockIdx.x, h=blockIdx.y, b=blockIdx.z; int t=threadIdx.x;
  __shared__ float sq[512]; __shared__ float sp[128]; __shared__ float red[128];
  for(int i=t;i<512;i+=128) sq[i]=qabs[(b*32+h)*512+i];
  __syncthreads();
  const u16* kp = ckv + ((size_t)b*8192 + s*128 + t)*512;
  float a=0; for(int c=0;c<512;c++) a+=sq[c]*bf2f(kp[c]);
  a*=0.08838834764831845f;
  red[t]=a; __syncthreads();
  float m=-INFINITY; for(int i=0;i<128;i++) m=fmaxf(m,red[i]);
  float p=__expf(a-m); sp[t]=p; __syncthreads();
  float l=0; for(int i=0;i<128;i++) l+=sp[i];
  const u16* vb = ckv + ((size_t)b*8192 + s*128)*512;
  for(int c=t;c<512;c+=128){ float o=0; for(int k=0;k<128;k++) o+=sp[k]*bf2f(vb[(size_t)k*512+c]); pO[(((size_t)b*32+h)*NS_A+s)*512+c]=o; }
  if(t==0){ pML[((b*32+h)*NS_A+s)*2]=m; pML[((b*32+h)*NS_A+s)*2+1]=l; }
}
__global__ void __launch_bounds__(256) k_attnB(const float* q, const u16* K, const u16* V, float* pO, float* pML){
  int s=blockIdx.x, h=blockIdx.y, b=blockIdx.z; int t=threadIdx.x; int kvh=h/4;
  __shared__ float sq[128]; __shared__ float sp[256]; __shared__ float red[256];
  if(t<128) sq[t]=q[8192+b*4096+h*128+t];
  __syncthreads();
  size_t base=(((size_t)b*8+kvh)*8192 + 6144 + s*256)*128;
  const u16* kp=K+base+(size_t)t*128;
  float a=0; for(int d=0;d<128;d++) a+=sq[d]*bf2f(kp[d]);
  a*=0.08838834764831845f;
  red[t]=a; __syncthreads();
  float m=-INFINITY; for(int i=0;i<256;i++) m=fmaxf(m,red[i]);
  float p=__expf(a-m); sp[t]=p; __syncthreads();
  float l=0; for(int i=0;i<256;i++) l+=sp[i];
  if(t<128){ float o=0; for(int k=0;k<256;k++) o+=sp[k]*bf2f(V[base+(size_t)k*128+t]); pO[(((size_t)b*32+h)*NS_B+s)*128+t]=o; }
  if(t==0){ pML[((b*32+h)*NS_B+s)*2]=m; pML[((b*32+h)*NS_B+s)*2+1]=l; }
}
__global__ void __launch_bounds__(128) k_combA(const float* pO, const float* pML, const u16* wuv, float* ctx){
  int h=blockIdx.x,b=blockIdx.y,t=threadIdx.x;
  __shared__ float lat[512]; __shared__ float w[NS_A];
  float M=-INFINITY; for(int s=0;s<NS_A;s++) M=fmaxf(M,pML[((b*32+h)*NS_A+s)*2]);
  float L=0; for(int s=0;s<NS_A;s++) L+=__expf(pML[((b*32+h)*NS_A+s)*2]-M)*pML[((b*32+h)*NS_A+s)*2+1];
  if(t<NS_A) w[t]=__expf(pML[((b*32+h)*NS_A+t)*2]-M)/L;
  __syncthreads();
  for(int c=t;c<512;c+=128){ float o=0; for(int s=0;s<NS_A;s++) o+=w[s]*pO[(((size_t)b*32+h)*NS_A+s)*512+c]; lat[c]=o; }
  __syncthreads();
  float a=0; for(int c=0;c<512;c++) a+=lat[c]*bf2f(wuv[(size_t)c*4096+h*128+t]);
  ctx[b*4096+h*128+t]=rbf(a);
}
__global__ void __launch_bounds__(128) k_combB(const float* pO, const float* pML, float* ctx){
  int h=blockIdx.x,b=blockIdx.y,t=threadIdx.x;
  float M=-INFINITY; for(int s=0;s<NS_B;s++) M=fmaxf(M,pML[((b*32+h)*NS_B+s)*2]);
  float L=0; for(int s=0;s<NS_B;s++) L+=__expf(pML[((b*32+h)*NS_B+s)*2]-M)*pML[((b*32+h)*NS_B+s)*2+1];
  float o=0; for(int s=0;s<NS_B;s++) o+=__expf(pML[((b*32+h)*NS_B+s)*2]-M)/L*pO[(((size_t)b*32+h)*NS_B+s)*128+t];
  ctx[8192+b*4096+h*128+t]=rbf(o);
}
__global__ void k_out(const float* ctx, const u16* woA, const u16* woB, u16* out){
  int which=blockIdx.y; int m=blockIdx.x*64+(threadIdx.x&63); int part=threadIdx.x>>6;
  const u16* w= which? woB: woA; const float* c=ctx+which*8192;
  float a0=0,a1=0;
  for(int k=part*256;k<part*256+256;k++){ float wv=bf2f(w[(size_t)k*512+m]); a0+=c[k]*wv; a1+=c[4096+k]*wv; }
  __shared__ float r[2][16][64];
  r[0][part][threadIdx.x&63]=a0; r[1][part][threadIdx.x&63]=a1; __syncthreads();
  if(part<2){ float s=0; for(int i=0;i<16;i++) s+=r[part][i][threadIdx.x&63]; out[part*1024+which*512+m]=f2bf(s); }
}

extern "C" void kernel_launch(void* const* d_in, const int* in_sizes, int n_in,
                              void* d_out, int out_size, void* d_ws, size_t ws_size,
                              hipStream_t stream) {
  const u16* xA=(const u16*)d_in[0]; const u16* ckv=(const u16*)d_in[1]; const u16* wq=(const u16*)d_in[2];
  const u16* wuk=(const u16*)d_in[3]; const u16* wuv=(const u16*)d_in[4]; const u16* wo=(const u16*)d_in[5];
  const u16* xB=(const u16*)d_in[6]; const u16* K=(const u16*)d_in[7]; const u16* V=(const u16*)d_in[8];
  const u16* wqB=(const u16*)d_in[9]; const u16* woB=(const u16*)d_in[10];
  float* ws=(float*)d_ws;
  float* q=ws; float* qabs=q+16384; float* ctx=qabs+32768; float* mlA=ctx+16384; float* mlB=mlA+64*NS_A*2;
  float* oB=mlB+64*NS_B*2; float* oA=oB+64*NS_B*128;
  k_q<<<dim3(32,2),128,0,stream>>>(xA,wq,xB,wqB,q);
  k_qabs<<<dim3(32,8),64,0,stream>>>(q,wuk,qabs);
  k_attnA<<<dim3(NS_A,32,2),128,0,stream>>>(qabs,ckv,oA,mlA);
  k_attnB<<<dim3(NS_B,32,2),256,0,stream>>>(q,K,V,oB,mlB);
  k_combA<<<dim3(32,2),128,0,stream>>>(oA,mlA,wuv,ctx);
  k_combB<<<dim3(32,2),128,0,stream>>>(oB,mlB,ctx);
  k_out<<<dim3(8,2),1024,0,stream>>>(ctx,wo,woB,(u16*)d_out);
}
```

```cpp
#include <hip/hip_runtime.h>
#include <stdint.h>

typedef unsigned u32;
typedef unsigned long long u64;
typedef unsigned short u16;
typedef u32 v4u __attribute__((ext_vector_type(4)));
typedef u32 v2u __attribute__((ext_vector_type(2)));
typedef float f32x2 __attribute__((ext_vector_type(2)));
typedef float f32x4 __attribute__((ext_vector_type(4)));
typedef float f32x16 __attribute__((ext_vector_type(16)));
typedef __bf16 bf16x8 __attribute__((ext_vector_type(8)));
typedef __bf16 bf16x2 __attribute__((ext_vector_type(2)));
typedef short s16x4 __attribute__((ext_vector_type(4)));
#define GAS __attribute__((address_space(1)))
#define LAS __attribute__((address_space(3)))
#define DI __device__ __forceinline__

constexpr u32 WS_E = 0;
constexpr u32 WS_QA = 4096;
constexpr u32 WS_QB = WS_QA + 262144;
constexpr u32 WS_QABS = WS_QB + 262144;
constexpr u32 WS_MLA = WS_QABS + 131072;
constexpr u32 WS_CTX = WS_MLA + 65536;
constexpr u32 WS_OB = WS_CTX + 262144;
constexpr u32 WS_MLB = WS_OB + 524288;
constexpr u32 WS_OUTP = WS_MLB + 8192;
constexpr u32 WS_OA = 2u << 20;
constexpr u32 WS_DBG = 8u << 20;
static_assert(WS_OUTP + 524288 <= WS_OA, "ws map");

constexpr float SC2 = 0.08838834764831845f * 1.4426950408889634f;
constexpr int SPIN_CAP = 1 << 17;

constexpr u32 L_S0 = 0, L_S1 = 32768, L_S2 = 65536, L_S3 = 98304, L_S4 = 131072;

#define STAMP(k)
#define STAMPV(k, v)

#define WAITVM(n) asm volatile("s_waitcnt vmcnt(%0)" ::"i"(n) : "memory")
#define BAR() asm volatile("s_waitcnt lgkmcnt(0)\n\ts_barrier" ::: "memory")

template <int CTRL> DI float dppf(float x) {
  return __builtin_bit_cast(float, __builtin_amdgcn_mov_dpp(__builtin_bit_cast(int, x), CTRL, 0xf, 0xf, true));
}
constexpr int XOR1 = 0xB1, XOR2 = 0x4E, HMIR = 0x141, ROR4 = 0x124, ROR8 = 0x128;
DI float sw32_add(float a, float b) { auto s = __builtin_amdgcn_permlane32_swap(__float_as_uint(a), __float_as_uint(b), false, false); return __uint_as_float(s[0]) + __uint_as_float(s[1]); }
DI float sw16_add(float a, float b) { auto s = __builtin_amdgcn_permlane16_swap(__float_as_uint(a), __float_as_uint(b), false, false); return __uint_as_float(s[0]) + __uint_as_float(s[1]); }
DI float sw32_max(float a) { auto s = __builtin_amdgcn_permlane32_swap(__float_as_uint(a), __float_as_uint(a), false, false); return fmaxf(__uint_as_float(s[0]), __uint_as_float(s[1])); }
DI float sw16_max(float a) { auto s = __builtin_amdgcn_permlane16_swap(__float_as_uint(a), __float_as_uint(a), false, false); return fmaxf(__uint_as_float(s[0]), __uint_as_float(s[1])); }
DI u32 pk2(float lo, float hi) { f32x2 p = {lo, hi}; return __builtin_bit_cast(u32, __builtin_convertvector(p, bf16x2)); }
DI float bflo(u32 x) { return __uint_as_float(x << 16); }
DI float bfhi(u32 x) { return __uint_as_float(x & 0xffff0000u); }
DI f32x4 mfma16(v4u a, v4u b, f32x4 c) { return __builtin_amdgcn_mfma_f32_16x16x32_bf16(__builtin_bit_cast(bf16x8, a), __builtin_bit_cast(bf16x8, b), c, 0, 0, 0); }
DI f32x16 mfma32(v4u a, v4u b, f32x16 c) { return __builtin_amdgcn_mfma_f32_32x32x16_bf16(__builtin_bit_cast(bf16x8, a), __builtin_bit_cast(bf16x8, b), c, 0, 0, 0); }

DI v4u lds128(LAS char* L, u32 off) { return *(LAS v4u*)(L + off); }
DI v2u tr64(LAS char* L, u32 off) { return __builtin_bit_cast(v2u, __builtin_amdgcn_ds_read_tr16_b64_v4i16((LAS s16x4*)(L + off))); }
DI v4u tr128(LAS char* L, u32 o0, u32 o1) { v2u a = tr64(L, o0), b = tr64(L, o1); return v4u{a.x, a.y, b.x, b.y}; }

DI void dma4(const void* sbase, u32 v0, u32 v1, u32 v2, u32 v3, u32 lds) {
  asm volatile("s_mov_b32 m0, %5\n\ts_nop 0\n\t"
               "global_load_lds_dwordx4 %0, %4 nt\n\t"
               "global_load_lds_dwordx4 %1, %4 offset:1024 nt\n\t"
               "global_load_lds_dwordx4 %2, %4 offset:2048 nt\n\t"
               "global_load_lds_dwordx4 %3, %4 offset:3072 nt"
               ::"v"(v0), "v"(v1), "v"(v2), "v"(v3), "s"(sbase), "s"(lds) : "memory");
}
DI void dma2a(const void* sbase, u32 v0, u32 v1, u32 lds) {
  asm volatile("s_mov_b32 m0, %3\n\ts_nop 0\n\t"
               "global_load_lds_dwordx4 %0, %2 nt\n\t"
               "global_load_lds_dwordx4 %1, %2 offset:1024 nt"
               ::"v"(v0), "v"(v1), "s"(sbase), "s"(lds) : "memory");
}
DI void dma2b(const void* sbase, u32 v2, u32 v3, u32 lds) {
  asm volatile("s_mov_b32 m0, %3\n\ts_nop 0\n\t"
               "global_load_lds_dwordx4 %0, %2 offset:2048 nt\n\t"
               "global_load_lds_dwordx4 %1, %2 offset:3072 nt"
               ::"v"(v2), "v"(v3), "s"(sbase), "s"(lds) : "memory");
}
template <int OFF> DI void ld_sc1(v4u& d, const void* sbase, u32 voff) {
  asm volatile("global_load_dwordx4 %0, %1, %2 offset:%3 sc1" : "=v"(d) : "v"(voff), "s"(sbase), "i"(OFF) : "memory");
}
template <int OFF> DI void ld_plain(v4u& d, const void* sbase, u32 voff) {
  asm volatile("global_load_dwordx4 %0, %1, %2 offset:%3" : "=v"(d) : "v"(voff), "s"(sbase), "i"(OFF) : "memory");
}
DI void st_sc1(void* sbase, u32 voff, v4u d) {
  asm volatile("global_store_dwordx4 %0, %1, %2 sc1\n\ts_nop 1" ::"v"(voff), "v"(d), "s"(sbase) : "memory");
}
#define PIN1(a) asm volatile("" : "+v"(a))
DI u32 tagbad(const v4u& g, u32 ta, u32 tb) { return (g.y ^ ta) | (g.w ^ tb); }
DI bool wave_ok(u32 bad) { return __builtin_amdgcn_ballot_w64(bad != 0u) == 0ull; }

struct Ctx { LAS char* L; char* ws; int lane, w; u32 ta, tb; };

DI void dma_wblock(const char* blk, u32 rstride, u32 lds, int lane, int w) {
  const u32 r = (u32)lane >> 4, c = (u32)lane & 15u;
  const u32 b = (16u * w + r) * rstride;
  const u32 x = c ^ (r << 2);
  dma4(blk, b + 16u * x, b + 4u * rstride - 1024u + 16u * (x ^ 1u), b + 8u * rstride - 2048u + 16u * (x ^ 2u), b + 12u * rstride - 3072u + 16u * (x ^ 3u),
       lds + 4096u * w);
}
DI void read_wfrags(LAS char* L, u32 lds, int lane, int w, v4u (&a)[4]) {
  const u32 g = (u32)lane >> 4, q = ((u32)lane & 15u) >> 2, p = (u32)lane & 3u;
  const u32 ch = 2u * w + (p >> 1);
  const u32 o0 = lds + 256u * (8u * g + q) + 16u * (ch ^ ((q << 2) | ((2u * g) & 3u))) + 8u * (p & 1u);
  const u32 o1 = lds + 256u * (8u * g + 4u + q) + 16u * (ch ^ ((q << 2) | ((2u * g + 1u) & 3u))) + 8u * (p & 1u);
#pragma unroll
  for (int ks = 0; ks < 4; ++ks) a[ks] = tr128(L, o0 + 8192u * ks, o1 + 8192u * ks);
}
template <int MASK> DI void read_bfrags(LAS char* L, u32 img, int lane, v4u (&b)[4]) {
  const u32 o = img + 272u * ((u32)lane & MASK) + 16u * ((u32)lane >> 4);
#pragma unroll
  for (int s = 0; s < 4; ++s) b[s] = lds128(L, o + 64u * s);
}
DI void publish_d(void* dst, f32x4 d, int lane, int w, u32 ta, u32 tb) {
  const u32 col = (u32)lane & 15u, g = (u32)lane >> 4, pr = (col >> 1) & 1u;
  const float v0 = pr ? d[2] : d[0], v1 = pr ? d[3] : d[1];
  if (col < 4u) st_sc1(dst, (col & 1u) * 1024u + (8u * w + 2u * g + pr) * 16u, v4u{__float_as_uint(v0), ta, __float_as_uint(v1), tb});
}
DI f32x4 mv4(const v4u (&a)[4], const v4u (&b)[4]) {
  f32x4 d = {0.f, 0.f, 0.f, 0.f};
#pragma unroll
  for (int s = 0; s < 4; ++s) d = mfma16(a[s], b[s], d);
  return d;
}
DI void gather4(const char* src, LAS char* L, u32 img, int lane, int w, u32 ta, u32 tb) {
  const u32 gi = (16u * w + ((u32)lane >> 2)) & 63u, b = (u32)w >> 2;
  const u32 off = ((u32)lane & 3u) * 2048u + b * 1024u + gi * 16u;
  v4u g;
  for (int it = 0;; ++it) {
    ld_sc1<0>(g, src, off);
    asm volatile("s_waitcnt vmcnt(0)" : "+v"(g)::"memory");
    if (wave_ok(tagbad(g, ta, tb)) || it > SPIN_CAP) break;
    __builtin_amdgcn_s_sleep(1);
  }
  float v0 = __uint_as_float(g.x), v1 = __uint_as_float(g.z);
  v0 += dppf<XOR1>(v0); v1 += dppf<XOR1>(v1);
  v0 += dppf<XOR2>(v0); v1 += dppf<XOR2>(v1);
  *(LAS u32*)(L + img + 272u * b + 4u * gi) = pk2(v0, v1);
}
DI void final_reduce(const char* ws, u16* out, int which, int idx, int lane, int w, u32 ta, u32 tb) {
  const u32 mq = (u32)idx >> 5, sub = (u32)idx & 31u;
  const char* src = ws + WS_OUTP + (which * 4u + mq) * 65536u;
  const u32 off = ((u32)lane >> 1) * 2048u + (u32)w * 1024u + (2u * sub + ((u32)lane & 1u)) * 16u;
  v4u g;
  for (int it = 0;; ++it) {
    ld_sc1<0>(g, src, off);
    asm volatile("s_waitcnt vmcnt(0)" : "+v"(g)::"memory");
    if (wave_ok(tagbad(g, ta, tb)) || it > SPIN_CAP) break;
    __builtin_amdgcn_s_sleep(1);
  }
  float v0 = __uint_as_float(g.x), v1 = __uint_as_float(g.z);
  v0 += dppf<XOR2>(v0); v1 += dppf<XOR2>(v1);
  v0 += dppf<ROR4>(v0); v1 += dppf<ROR4>(v1);
  v0 += dppf<ROR8>(v0); v1 += dppf<ROR8>(v1);
  v0 = sw16_add(v0, v0); v1 = sw16_add(v1, v1);
  v0 = sw32_add(v0, v0); v1 = sw32_add(v1, v1);
  if (lane < 2) *(GAS u32*)((GAS char*)out + ((u32)w * 1024u + which * 512u + 128u * mq + 4u * sub + 2u * lane) * 2u) = pk2(v0, v1);
}

__global__ void __launch_bounds__(512, 2)
fused_decode(const u16* __restrict__ xA, const u16* __restrict__ ckv, const u16* __restrict__ wq, const u16* __restrict__ wuk,
             const u16* __restrict__ wuv, const u16* __restrict__ wo, const u16* __restrict__ xB, const u16* __restrict__ Kc,
             const u16* __restrict__ Vc, const u16* __restrict__ wqB, const u16* __restrict__ woB, u16* __restrict__ out, char* __restrict__ ws) {
  __shared__ __attribute__((aligned(16))) char smem[163840];
  LAS char* L = (LAS char*)smem;
  const int lane = threadIdx.x & 63, w = __builtin_amdgcn_readfirstlane(threadIdx.x >> 6);
  const int bid = blockIdx.x, roleB = (bid >> 3) & 1, idx = ((bid >> 4) << 3) | (bid & 7);
  const int hh_ = (idx & 7) + 8 * (idx >> 5), qq_ = (idx >> 3) & 3;
  STAMP(0);
  {
    u64 pc; asm volatile("s_getpc_b64 %0" : "=s"(pc));
    pc &= ~(u64)0x7f;
    if (w >= 5) {
      asm volatile("s_mov_b32 m0, %2\n\ts_nop 0\n\tglobal_load_lds_dword %0, %1"
                   ::"v"((u32)lane * 128u + (u32)(w - 5) * 8192u), "s"(pc), "s"(L_S3 + 4096u * (u32)w) : "memory");
    } else {
      asm volatile("s_mov_b32 m0, %2\n\ts_nop 0\n\tglobal_load_lds_dword %0, %1" ::"v"(0u), "s"(pc), "s"(L_S3 + 4096u * (u32)w) : "memory");
    }
  }
  const u32 xo = ((u32)lane & 1u) * 1024u + (u32)qq_ * 256u + ((u32)lane >> 4) * 16u;
#define LOAD_EX(x) \
  u32 ev; v4u xf[4]; \
  asm volatile("global_load_dword %0, %1, %2 sc1" : "=v"(ev) : "v"(0u), "s"(ws) : "memory"); \
  ld_plain<0>(xf[0], x, xo); ld_plain<64>(xf[1], x, xo); ld_plain<128>(xf[2], x, xo); ld_plain<192>(xf[3], x, xo);
  asm volatile("" ::"s"(xA), "s"(ckv), "s"(wq), "s"(wuk), "s"(wuv), "s"(wo), "s"(xB), "s"(Kc), "s"(Vc), "s"(wqB), "s"(woB), "s"(out), "s"(ws));
  u32 ta, tb, e0;

  if (!roleB) {
    const int b = (idx & 7) >> 2, sp = (idx >> 3) * 4 + (idx & 3);
    const char* kv = (const char*)ckv + ((size_t)b * 8192 + (size_t)sp * 128 + 4u * w) * 1024u;
    const u32 kx = (16u * lane) ^ (16u * (w & 3));
    const u32 k0 = kx, k1 = kx ^ 64u, k2 = kx ^ 128u, k3 = kx ^ 192u;
    const u32 lw = 4096u * w;
    LOAD_EX(xA)
    dma_wblock((const char*)wq + (size_t)qq_ * 128 * 8192 + hh_ * 256, 8192u, L_S4, lane, w);
    dma2a(kv, k0, k1, L_S0 + lw);
    asm volatile("s_waitcnt vmcnt(2)" : "+v"(ev), "+v"(xf[0]), "+v"(xf[1]), "+v"(xf[2]), "+v"(xf[3])::"memory");
    STAMP(1);
    dma2b(kv, k2, k3, L_S0 + lw);
    e0 = __builtin_amdgcn_readfirstlane(ev);
    ta = e0 * 0x9E3779B1u + 0x7F4A7C15u; tb = (e0 ^ (e0 >> 15)) * 0x85EBCA77u + 0x3C6EF372u;
    BAR();
    {
      v4u a[4]; read_wfrags(L, L_S4, lane, w, a);
      f32x4 d = mv4(a, xf);
      publish_d(ws + WS_QA + ((u32)hh_ * 4u + qq_) * 2048u, d, lane, w, ta, tb);
    }
    STAMP(2);
    dma4(kv + 32768, k0, k1, k2, k3, L_S1 + lw);
    dma4(kv + 65536, k0, k1, k2, k3, L_S2 + lw);
    const int kt = w & 3, ch = w >> 2;
    v4u ka[16], vt[8][2];
    const u32 r31 = (u32)lane & 31u, hf = (u32)lane >> 5;
    const u32 fr = ((r31 & 3u) << 2) | ((r31 >> 2) & 3u);
    const u32 rowo = 1024u * r31 + 16u * (hf ^ fr) + 512u * ch;
    const u32 blk = ((u32)lane >> 4) & 1u, q4 = ((u32)lane & 15u) >> 2, p4 = (u32)lane & 3u;
    const u32 chx = 8u * w + 2u * blk + (p4 >> 1);
    const u32 tro0 = 1024u * (8u * hf + q4) + 16u * (chx ^ ((q4 << 2) | (2u * hf))) + 8u * (p4 & 1u);
    const u32 tro1 = 1024u * (8u * hf + 4u + q4) + 16u * (chx ^ ((q4 << 2) | (2u * hf + 1u))) + 8u * (p4 & 1u);
#define READ_TILE(t)                                                                                          \
    {                                                                                                         \
      if (kt == (t)) {                                                                                        \
        _Pragma("unroll") for (int s = 0; s < 16; ++s) ka[s] = lds128(L, 32768u * (t) + ((rowo ^ (32u * (s & 7))) + 256u * (s >> 3))); \
      }                                                                                                       \
      _Pragma("unroll") for (int ks = 0; ks < 2; ++ks)                                                        \
        _Pragma("unroll") for (int u = 0; u < 2; ++u)                                                         \
          vt[2 * (t) + ks][u] = tr128(L, 32768u * (t) + 16384u * ks + (tro0 ^ (64u * u)), 32768u * (t) + 16384u * ks + (tro1 ^ (64u * u))); \
    }
#pragma unroll
    for (int s = 0; s < 16; ++s) ka[s] = v4u{0u, 0u, 0u, 0u};
    WAITVM(8); BAR(); STAMP(3);
    READ_TILE(0)
    dma4(kv + 98304, k0, k1, k2, k3, L_S3 + lw);
    WAITVM(8); BAR(); STAMP(4);
    READ_TILE(1)
    v4u g[8];
    const char* qsrc = ws + WS_QABS + (u32)b * 65536u + (u32)w * 8192u;
    const u32 go = 16u * lane;
#define GATHER_Q() { ld_sc1<0>(g[0], qsrc, go); ld_sc1<1024>(g[1], qsrc, go); ld_sc1<2048>(g[2], qsrc, go); ld_sc1<3072>(g[3], qsrc, go); \
                     ld_sc1<0>(g[4], qsrc + 4096, go); ld_sc1<1024>(g[5], qsrc + 4096, go); ld_sc1<2048>(g[6], qsrc + 4096, go); ld_sc1<3072>(g[7], qsrc + 4096, go); }
    GATHER_Q()
    WAITVM(12); BAR(); STAMP(5);
    READ_TILE(2)
    WAITVM(8); BAR(); STAMP(6);
    READ_TILE(3)
#define WAIT_Q() asm volatile("s_waitcnt vmcnt(0)" : "+v"(g[0]), "+v"(g[1]), "+v"(g[2]), "+v"(g[3]), "+v"(g[4]), "+v"(g[5]), "+v"(g[6]), "+v"(g[7])::"memory")
#define BAD_Q() (tagbad(g[0], ta, tb) | tagbad(g[1], ta, tb) | tagbad(g[2], ta, tb) | tagbad(g[3], ta, tb) | tagbad(g[4], ta, tb) | tagbad(g[5], ta, tb) | tagbad(g[6], ta, tb) | tagbad(g[7], ta, tb))
    WAIT_Q();
    if (!wave_ok(BAD_Q())) {
      for (int it = 0; it < SPIN_CAP; ++it) {
        __builtin_amdgcn_s_sleep(2);
        GATHER_Q()
        WAIT_Q();
        STAMPV(30, it + 1);
        if (wave_ok(BAD_Q())) break;
      }
    }
    STAMP(7);
#pragma unroll
    for (int i = 0; i < 8; ++i) {
      const u32 gi = 64u * (i & 1) + lane, f = ((u32)(i >> 1) << 2) | (u32)(w & 3);
      *(LAS v2u*)(L + L_S4 + 1024u * (4u * w + (i >> 1)) + 16u * ((gi >> 1) ^ f) + 8u * (gi & 1u)) = v2u{g[i].x, g[i].z};
    }
    BAR();
    STAMP(8);
    dma_wblock((const char*)wuv + (size_t)qq_ * 128 * 8192 + hh_ * 256, 8192u, L_S2, lane, w);
    {
      f32x16 acc;
#pragma unroll
      for (int i = 0; i < 16; ++i) acc[i] = 0.f;
#pragma unroll
      for (int s = 0; s < 16; ++s) acc = mfma32(ka[s], lds128(L, L_S4 + ((rowo ^ (32u * (s & 7))) + 256u * (s >> 3))), acc);
      const u32 so = L_S0 + 16896u * ch + 528u * r31 + 128u * kt + 16u * hf;
#pragma unroll
      for (int a = 0; a < 4; ++a) *(LAS f32x4*)(L + so + 32u * a) = f32x4{acc[4 * a], acc[4 * a + 1], acc[4 * a + 2], acc[4 * a + 3]};
    }
    BAR();
    STAMP(9);
    constexpr u32 L_P = L_S1 + 16384;
    float m2, lsum;
    {
      const u32 hd = 4u * w + ((u32)lane >> 4), j = (u32)lane & 15u;
      const u32 so = L_S0 + 528u * hd + 32u * j;
      const f32x4 s0 = *(LAS f32x4*)(L + so) + *(LAS f32x4*)(L + so + 16896u);
      const f32x4 s1 = *(LAS f32x4*)(L + so + 16u) + *(LAS f32x4*)(L + so + 16896u + 16u);
      float m = fmaxf(fmaxf(fmaxf(s0[0], s0[1]), fmaxf(s0[2], s0[3])), fmaxf(fmaxf(s1[0], s1[1]), fmaxf(s1[2], s1[3])));
      m = fmaxf(m, dppf<XOR1>(m)); m = fmaxf(m, dppf<XOR2>(m)); m = fmaxf(m, dppf<ROR4>(m)); m = fmaxf(m, dppf<ROR8>(m));
      m2 = m * SC2;
      float p[8];
#pragma unroll
      for (int i = 0; i < 4; ++i) { p[i] = __builtin_amdgcn_exp2f(fmaf(s0[i], SC2, -m2)); p[4 + i] = __builtin_amdgcn_exp2f(fmaf(s1[i], SC2, -m2)); }
      *(LAS v4u*)(L + L_P + 256u * hd + 16u * (j ^ (hd & 15u))) = v4u{pk2(p[0], p[1]), pk2(p[2], p[3]), pk2(p[4], p[5]), pk2(p[6], p[7])};
      float l = ((p[0] + p[1]) + (p[2] + p[3])) + ((p[4] + p[5]) + (p[6] + p[7]));
      l += dppf<XOR1>(l); l += dppf<XOR2>(l); l += dppf<ROR4>(l); l += dppf<ROR8>(l);
      lsum = l;
    }
    BAR();
    STAMP(10);
    {
      f32x16 o0, o1;
#pragma unroll
      for (int i = 0; i < 16; ++i) { o0[i] = 0.f; o1[i] = 0.f; }
      const u32 po = L_P + 256u * r31 + 16u * (hf ^ (r31 & 15u));
#pragma unroll
      for (int s = 0; s < 8; ++s) {
        const v4u pf = lds128(L, po ^ (32u * s));
        o0 = mfma32(vt[s][0], pf, o0);
        o1 = mfma32(vt[s][1], pf, o1);
      }
      const u32 sb = L_S3 + lw + 128u * r31 + 8u * hf, sx = 16u * (r31 & 7u);
#pragma unroll
      for (int a = 0; a < 4; ++a) {
        *(LAS v2u*)(L + sb + ((16u * a) ^ sx)) = v2u{pk2(o0[4 * a], o0[4 * a + 1]), pk2(o0[4 * a + 2], o0[4 * a + 3])};
        *(LAS v2u*)(L + sb + ((64u + 16u * a) ^ sx)) = v2u{pk2(o1[4 * a], o1[4 * a + 1]), pk2(o1[4 * a + 2], o1[4 * a + 3])};
      }
      const u32 rr = (u32)lane >> 3, kk = (u32)lane & 7u;
      char* od = ws + WS_OA + (((u32)b * 32u * 4u + (u32)(w >> 1)) * 64u + sp) * 256u + (u32)(w & 1) * 128u;
      v4u t[4];
#pragma unroll
      for (int i = 0; i < 4; ++i) t[i] = lds128(L, L_S3 + lw + 1024u * i + 128u * rr + 16u * (kk ^ rr));
#pragma unroll
      for (int i = 0; i < 4; ++i) st_sc1(od, (8u * i + rr) * 65536u + 16u * kk, t[i]);
    }
    STAMP(11);
    WAITVM(0); BAR();
    STAMP(12);
    if ((lane & 15) == 0)
      st_sc1(ws + WS_MLA + (u32)b * 32768u + (u32)sp * 16u, (4u * w + ((u32)lane >> 4)) * 1024u, v4u{__float_as_uint(m2), ta, __float_as_uint(lsum), tb});
    v4u wa[4];
    read_wfrags(L, L_S2, lane, w, wa);
    constexpr u32 L_IMG = L_S4;
    {
      const u32 bb = (u32)w >> 2, cs = (u32)w & 3u;
      const u32 rec = ((u32)lane & 3u) | (((u32)lane >> 4) << 2), chk = ((u32)lane >> 2) & 3u;
      const char* fsrc = ws + WS_MLA + (bb * 32u + hh_) * 1024u;
      v4u f[4];
      for (int it = 0;; ++it) {
        ld_sc1<0>(f[0], fsrc, rec * 16u); ld_sc1<256>(f[1], fsrc, rec * 16u); ld_sc1<512>(f[2], fsrc, rec * 16u); ld_sc1<768>(f[3], fsrc, rec * 16u);
        asm volatile("s_waitcnt vmcnt(0)" : "+v"(f[0]), "+v"(f[1]), "+v"(f[2]), "+v"(f[3])::"memory");
        if (wave_ok(tagbad(f[0], ta, tb) | tagbad(f[1], ta, tb) | tagbad(f[2], ta, tb) | tagbad(f[3], ta, tb)) || it > SPIN_CAP) break;
        __builtin_amdgcn_s_sleep(1);
      }
      STAMP(13);
      const char* psrc = ws + WS_OA + ((bb * 32u + hh_) * 4u + qq_) * 16384u + cs * 64u;
      const u32 po = rec * 256u + chk * 16u;
      v4u d[4];
      ld_sc1<0>(d[0], psrc, po); ld_sc1<0>(d[1], psrc + 4096, po); ld_sc1<0>(d[2], psrc + 8192, po); ld_sc1<0>(d[3], psrc + 12288, po);
      dma_wblock((const char*)wo + (size_t)hh_ * 128 * 1024 + qq_ * 256, 1024u, L_S0, lane, w);
      float M = fmaxf(fmaxf(__uint_as_float(f[0].x), __uint_as_float(f[1].x)), fmaxf(__uint_as_float(f[2].x), __uint_as_float(f[3].x)));
      M = fmaxf(M, dppf<XOR1>(M)); M = fmaxf(M, dppf<XOR2>(M)); M = sw16_max(M); M = sw32_max(M);
      float e[4], Ls = 0.f;
#pragma unroll
      for (int i = 0; i < 4; ++i) { e[i] = __builtin_amdgcn_exp2f(__uint_as_float(f[i].x) - M); Ls = fmaf(e[i], __uint_as_float(f[i].z), Ls); }
      Ls += dppf<XOR1>(Ls); Ls += dppf<XOR2>(Ls); Ls = sw16_add(Ls, Ls); Ls = sw32_add(Ls, Ls);
      const float inv = __builtin_amdgcn_rcpf(Ls);
      asm volatile("s_waitcnt vmcnt(4)" : "+v"(d[0]), "+v"(d[1]), "+v"(d[2]), "+v"(d[3])::"memory");
      STAMP(14);
      float a[8];
#pragma unroll
      for (int k = 0; k < 8; ++k) a[k] = 0.f;
#pragma unroll
      for (int i = 0; i < 4; ++i) {
        const float wi = e[i] * inv;
#pragma unroll
        for (int k = 0; k < 4; ++k) { a[2 * k] = fmaf(wi, bflo(d[i][k]), a[2 * k]); a[2 * k + 1] = fmaf(wi, bfhi(d[i][k]), a[2 * k + 1]); }
      }
      const float r0 = sw32_add(a[0], a[4]), r1 = sw32_add(a[1], a[5]), r2 = sw32_add(a[2], a[6]), r3 = sw32_add(a[3], a[7]);
      float u0 = sw16_add(r0, r2), u1 = sw16_add(r1, r3);
      u0 += dppf<XOR1>(u0); u1 += dppf<XOR1>(u1); u0 += dppf<XOR2>(u0); u1 += dppf<XOR2>(u1);
      const u32 hi = pk2(u0, u1), lo = pk2(u0 - bflo(hi), u1 - bfhi(hi));
      const u32 cl = 32u * cs + 8u * chk + 4u * ((u32)lane >> 5) + 2u * (((u32)lane >> 4) & 1u);
      *(LAS u32*)(L + L_IMG + 272u * (bb + 2u * ((u32)lane & 1u)) + 2u * cl) = (lane & 1) ? lo : hi;
    }
    BAR();
    {
      v4u bf[4]; read_bfrags<3>(L, L_IMG, lane, bf);
      f32x4 d = mv4(wa, bf);
#pragma unroll
      for (int i = 0; i < 4; ++i) d[i] += dppf<XOR2>(d[i]);
      publish_d(ws + WS_CTX + ((u32)hh_ * 4u + qq_) * 2048u, d, lane, w, ta, tb);
    }
    STAMP(15);
    gather4(ws + WS_CTX + (u32)hh_ * 8192u, L, L_IMG + 2048u, lane, w, ta, tb);
    STAMP(16);
    BAR();
    {
      v4u a[4], bf[4];
      read_wfrags(L, L_S0, lane, w, a);
      read_bfrags<1>(L, L_IMG + 2048u, lane, bf);
      publish_d(ws + WS_OUTP + ((u32)qq_ * 32u + hh_) * 2048u, mv4(a, bf), lane, w, ta, tb);
    }
    STAMP(17);
    if (w < 2) {
      final_reduce(ws, out, 0, idx, lane, w, ta, tb);
      if (idx == 0 && w == 0 && lane == 0) __hip_atomic_store((GAS u32*)(ws + WS_E), e0 + 1u, __ATOMIC_RELAXED, __HIP_MEMORY_SCOPE_AGENT);
    }
    STAMP(18);
  } else {
    const u32 r4 = (u32)lane >> 4, c16 = (u32)lane & 15u;
    LOAD_EX(xB)
    {
      const u32 bs = (16u * w + r4) * 8192u, x = c16 ^ r4;
      dma4((const char*)wuk + (size_t)qq_ * 128 * 8192 + hh_ * 256, bs + 16u * x, bs + 32768u - 1024u + 16u * (x ^ 4u), bs + 65536u - 2048u + 16u * (x ^ 8u),
           bs + 98304u - 3072u + 16u * (x ^ 12u), L_S0 + 4096u * w);
    }
    asm volatile("s_waitcnt vmcnt(0)" : "+v"(ev), "+v"(xf[0]), "+v"(xf[1]), "+v"(xf[2]), "+v"(xf[3])::"memory");
    STAMP(1);
    e0 = __builtin_amdgcn_readfirstlane(ev);
    ta = e0 * 0x9E3779B1u + 0x7F4A7C15u; tb = (e0 ^ (e0 >> 15)) * 0x85EBCA77u + 0x3C6EF372u;
    constexpr u32 L_IMG = L_S1;
    v4u uk[4];
    {
      const u32 o = L_S0 + 4096u * w + 256u * c16 + 16u * (r4 ^ c16);
#pragma unroll
      for (int s = 0; s < 4; ++s) uk[s] = lds128(L, o ^ (64u * s));
    }
    gather4(ws + WS_QA + (u32)hh_ * 8192u, L, L_IMG, lane, w, ta, tb);
    STAMP(2);
    dma_wblock((const char*)wqB + (size_t)qq_ * 128 * 8192 + hh_ * 256, 8192u, L_S4, lane, w);
    BAR();
    {
      v4u bf[4]; read_bfrags<1>(L, L_IMG, lane, bf);
      const f32x4 d = mv4(uk, bf);
      if (c16 < 2u)
        st_sc1(ws + WS_QABS + (u32)hh_ * 2048u + (32u * qq_ + 4u * w) * 16u, c16 * 65536u + r4 * 16u, v4u{pk2(d[0], d[1]), ta, pk2(d[2], d[3]), tb});
    }
    STAMP(3);
    WAITVM(0); BAR();
    STAMP(4);
    {
      v4u a[4]; read_wfrags(L, L_S4, lane, w, a);
      publish_d(ws + WS_QB + ((u32)hh_ * 4u + qq_) * 2048u, mv4(a, xf), lane, w, ta, tb);
    }
    STAMP(5);
    const int b = idx >> 6, kvh = (idx >> 3) & 7, j = idx & 7;
    const size_t kvo = (((size_t)b * 8 + kvh) * 8192 + 6144 + 256u * j + 32u * w) * 256u;
    const u32 kb = L_S0 + 8192u * w, vb = L_S2 + 8192u * w;
    {
      const u32 x = c16 ^ r4, bs = 256u * r4;
      dma4((const char*)Kc + kvo, bs + 16u * x, bs + 16u * (x ^ 4u), bs + 16u * (x ^ 8u), bs + 16u * (x ^ 12u), kb);
      dma4((const char*)Kc + kvo + 4096, bs + 16u * x, bs + 16u * (x ^ 4u), bs + 16u * (x ^ 8u), bs + 16u * (x ^ 12u), kb + 4096u);
    }
    v4u g[2];
    const char* qsrc = ws + WS_QB + (4u * kvh + (u32)(w >> 1)) * 8192u + (u32)b * 1024u + (u32)(w & 1) * 512u;
    const u32 go = ((u32)lane & 3u) * 2048u + ((u32)lane >> 2) * 16u;
    ld_sc1<0>(g[0], qsrc, go); ld_sc1<256>(g[1], qsrc, go);
    {
      const u32 bs = 256u * r4 + 16u * (c16 & 1u), x = (c16 >> 1) ^ r4;
      dma4((const char*)Vc + kvo, bs + 32u * x, bs + 32u * (x ^ 4u), bs + 32u * x, bs + 32u * (x ^ 4u), vb);
      dma4((const char*)Vc + kvo + 4096, bs + 32u * x, bs + 32u * (x ^ 4u), bs + 32u * x, bs + 32u * (x ^ 4u), vb + 4096u);
    }
    asm volatile("s_waitcnt vmcnt(8)" : "+v"(g[0]), "+v"(g[1])::"memory");
    STAMP(6);
    v4u kf[2][4];
    {
      const u32 o = kb + 256u * c16 + 16u * (r4 ^ c16);
#pragma unroll
      for (int t = 0; t < 2; ++t)
#pragma unroll
        for (int s = 0; s < 4; ++s) kf[t][s] = lds128(L, (o ^ (64u * s)) + 4096u * t);
    }
    for (int it = 0;; ++it) {
      if (wave_ok(tagbad(g[0], ta, tb) | tagbad(g[1], ta, tb)) || it > SPIN_CAP) { STAMPV(30, it); break; }
      __builtin_amdgcn_s_sleep(1);
      ld_sc1<0>(g[0], qsrc, go); ld_sc1<256>(g[1], qsrc, go);
      asm volatile("s_waitcnt vmcnt(0)" : "+v"(g[0]), "+v"(g[1])::"memory");
    }
    constexpr u32 L_IMG2 = L_S4;
    BAR();
#pragma unroll
    for (int i = 0; i < 2; ++i) {
      float v0 = __uint_as_float(g[i].x), v1 = __uint_as_float(g[i].z);
      v0 += dppf<XOR1>(v0); v1 += dppf<XOR1>(v1); v0 += dppf<XOR2>(v0); v1 += dppf<XOR2>(v1);
      *(LAS u32*)(L + L_IMG2 + 272u * (u32)(w >> 1) + 4u * ((u32)(w & 1) * 32u + 16u * i + ((u32)lane >> 2))) = pk2(v0, v1);
    }
    BAR();
    STAMP(7);
    v4u pb; float m2, lsum;
    {
      v4u bf[4]; read_bfrags<3>(L, L_IMG2, lane, bf);
      const f32x4 s0 = mv4(kf[0], bf), s1 = mv4(kf[1], bf);
      float m = fmaxf(fmaxf(fmaxf(s0[0], s0[1]), fmaxf(s0[2], s0[3])), fmaxf(fmaxf(s1[0], s1[1]), fmaxf(s1[2], s1[3])));
      m = sw16_max(m); m = sw32_max(m);
      m2 = m * SC2;
      float p[8];
#pragma unroll
      for (int i = 0; i < 4; ++i) { p[i] = __builtin_amdgcn_exp2f(fmaf(s0[i], SC2, -m2)); p[4 + i] = __builtin_amdgcn_exp2f(fmaf(s1[i], SC2, -m2)); }
      pb = v4u{pk2(p[0], p[1]), pk2(p[2], p[3]), pk2(p[4], p[5]), pk2(p[6], p[7])};
      float l = ((p[0] + p[1]) + (p[2] + p[3])) + ((p[4] + p[5]) + (p[6] + p[7]));
      l = sw16_add(l, l); lsum = sw32_add(l, l);
    }
    BAR();
    dma_wblock((const char*)woB + (size_t)hh_ * 128 * 1024 + qq_ * 256, 1024u, L_S4, lane, w);
    WAITVM(4);
    STAMP(8);
    {
      const u32 q4 = c16 >> 2, p4 = (u32)lane & 3u, rw = 4u * r4 + q4;
      const u32 o = vb + 256u * rw + 32u * (rw & 7u) + 8u * p4;
#pragma unroll
      for (int c = 0; c < 8; ++c) {
        const v4u a = tr128(L, o ^ (32u * c), (o ^ (32u * c)) + 4096u);
        const f32x4 d = mfma16(a, pb, f32x4{0.f, 0.f, 0.f, 0.f});
        if (c16 < 4u) *(LAS f32x4*)(L + kb + 528u * c16 + 64u * c + 16u * r4) = d;
      }
      if (lane < 4) *(LAS f32x2*)(L + kb + 4096u + 8u * lane) = f32x2{m2, lsum};
    }
    BAR();
    STAMP(9);
    if (w < 4) {
      const f32x2 ml = *(LAS f32x2*)(L + L_S0 + 8192u * ((u32)lane & 7u) + 4096u + 8u * w);
      float M = ml[0];
      M = fmaxf(M, dppf<XOR1>(M)); M = fmaxf(M, dppf<XOR2>(M)); M = fmaxf(M, dppf<HMIR>(M));
      const float e = __builtin_amdgcn_exp2f(ml[0] - M);
      float Ls = e * ml[1];
      Ls += dppf<XOR1>(Ls); Ls += dppf<XOR2>(Ls); Ls += dppf<HMIR>(Ls);
      float o0 = 0.f, o1 = 0.f;
#pragma unroll
      for (int v = 0; v < 8; ++v) {
        const float ev_ = __builtin_bit_cast(float, __builtin_amdgcn_readlane(__builtin_bit_cast(int, e), v));
        const f32x2 ov = *(LAS f32x2*)(L + L_S0 + 8192u * v + 528u * w + 8u * lane);
        o0 = fmaf(ev_, ov[0], o0); o1 = fmaf(ev_, ov[1], o1);
      }
      const u32 rec = ((u32)b * 32u + 4u * kvh + w) * 8u + j;
      st_sc1(ws + WS_OB + rec * 1024u, 16u * lane, v4u{__float_as_uint(o0), ta, __float_as_uint(o1), tb});
      if (lane == 0) st_sc1(ws + WS_MLB + rec * 16u, 0u, v4u{__float_as_uint(M), ta, __float_as_uint(Ls), tb});
    }
    STAMP(10);
    constexpr u32 L_IMG3 = L_S2;
    {
      const u32 bb = (u32)w >> 2, jj = ((u32)lane & 3u) | (((u32)lane >> 5) << 2), gl = ((u32)lane >> 2) & 7u;
      const char* osrc = ws + WS_OB + (bb * 32u + hh_) * 8192u + (u32)(w & 3) * 256u;
      const char* msrc = ws + WS_MLB + (bb * 32u + hh_) * 128u;
      v4u d0, d1, f;
      for (int it = 0;; ++it) {
        ld_sc1<0>(d0, osrc, jj * 1024u + gl * 16u); ld_sc1<128>(d1, osrc, jj * 1024u + gl * 16u); ld_sc1<0>(f, msrc, jj * 16u);
        asm volatile("s_waitcnt vmcnt(0)" : "+v"(d0), "+v"(d1), "+v"(f)::"memory");
        if (wave_ok(tagbad(d0, ta, tb) | tagbad(d1, ta, tb) | tagbad(f, ta, tb)) || it > SPIN_CAP) break;
        __builtin_amdgcn_s_sleep(1);
      }
      STAMP(11);
      float M = __uint_as_float(f.x);
      M = fmaxf(M, dppf<XOR1>(M)); M = fmaxf(M, dppf<XOR2>(M)); M = sw32_max(M);
      const float e = __builtin_amdgcn_exp2f(__uint_as_float(f.x) - M);
      float Ls = e * __uint_as_float(f.z);
      Ls += dppf<XOR1>(Ls); Ls += dppf<XOR2>(Ls); Ls = sw32_add(Ls, Ls);
      const float wi = e * __builtin_amdgcn_rcpf(Ls);
      float v0 = sw32_add(wi * __uint_as_float(d0.x), wi * __uint_as_float(d1.x));
      float v1 = sw32_add(wi * __uint_as_float(d0.z), wi * __uint_as_float(d1.z));
      v0 += dppf<XOR1>(v0); v1 += dppf<XOR1>(v1); v0 += dppf<XOR2>(v0); v1 += dppf<XOR2>(v1);
      *(LAS u32*)(L + L_IMG3 + 272u * bb + 4u * ((u32)(w & 3) * 16u + ((u32)lane >> 5) * 8u + gl)) = pk2(v0, v1);
    }
    BAR();
    {
      v4u a[4], bf[4];
      read_wfrags(L, L_S4, lane, w, a);
      read_bfrags<1>(L, L_IMG3, lane, bf);
      publish_d(ws + WS_OUTP + ((4u + qq_) * 32u + hh_) * 2048u, mv4(a, bf), lane, w, ta, tb);
    }
    STAMP(17);
    if (w < 2) final_reduce(ws, out, 1, idx, lane, w, ta, tb);
    STAMP(18);
  }
}

extern "C" void kernel_launch(void* const* d_in, const int* in_sizes, int n_in, void* d_out, int out_size, void* d_ws, size_t ws_size,
                              hipStream_t stream) {
  fused_decode<<<256, 512, 0, stream>>>((const u16*)d_in[0], (const u16*)d_in[1], (const u16*)d_in[2], (const u16*)d_in[3], (const u16*)d_in[4],
                                        (const u16*)d_in[5], (const u16*)d_in[6], (const u16*)d_in[7], (const u16*)d_in[8], (const u16*)d_in[9],
                                        (const u16*)d_in[10], (u16*)d_out, (char*)d_ws);
}
```

```cpp
#define CAL_STAMP 98
#define CAL_ROLE 0
#include <hip/hip_runtime.h>
#include <stdint.h>

typedef unsigned u32;
typedef unsigned long long u64;
typedef unsigned short u16;
typedef u32 v4u __attribute__((ext_vector_type(4)));
typedef u32 v2u __attribute__((ext_vector_type(2)));
typedef float f32x2 __attribute__((ext_vector_type(2)));
typedef float f32x4 __attribute__((ext_vector_type(4)));
typedef float f32x16 __attribute__((ext_vector_type(16)));
typedef __bf16 bf16x8 __attribute__((ext_vector_type(8)));
typedef __bf16 bf16x2 __attribute__((ext_vector_type(2)));
typedef short s16x4 __attribute__((ext_vector_type(4)));
#define GAS __attribute__((address_space(1)))
#define LAS __attribute__((address_space(3)))
#define DI __device__ __forceinline__

constexpr u32 WS_E = 0;
constexpr u32 WS_QA = 4096;
constexpr u32 WS_QB = WS_QA + 262144;
constexpr u32 WS_QABS = WS_QB + 262144;
constexpr u32 WS_MLA = WS_QABS + 131072;
constexpr u32 WS_CTX = WS_MLA + 65536;
constexpr u32 WS_OB = WS_CTX + 262144;
constexpr u32 WS_MLB = WS_OB + 524288;
constexpr u32 WS_OUTP = WS_MLB + 8192;
constexpr u32 WS_OA = 2u << 20;
constexpr u32 WS_DBG = 8u << 20;
static_assert(WS_OUTP + 524288 <= WS_OA, "ws map");

constexpr float SC2 = 0.08838834764831845f * 1.4426950408889634f;
constexpr int SPIN_CAP = 1 << 17;

constexpr u32 L_S0 = 0, L_S1 = 32768, L_S2 = 65536, L_S3 = 98304, L_S4 = 131072;

#define STAMP(k) do { if ((k) == 0 || (k) == CAL_STAMP) { u64 t_; asm volatile("s_memrealtime %0\n\ts_waitcnt lgkmcnt(0)" : "=s"(t_)::"memory"); if ((k) == 0) cal0 = (u32)t_; else if (CAL_ROLE == roleB) calx = (u32)t_ - cal0; } } while (0)
#define STAMPV(k, v)

#define WAITVM(n) asm volatile("s_waitcnt vmcnt(%0)" ::"i"(n) : "memory")
#define BAR() asm volatile("s_waitcnt lgkmcnt(0)\n\ts_barrier" ::: "memory")

template <int CTRL> DI float dppf(float x) {
  return __builtin_bit_cast(float, __builtin_amdgcn_mov_dpp(__builtin_bit_cast(int, x), CTRL, 0xf, 0xf, true));
}
constexpr int XOR1 = 0xB1, XOR2 = 0x4E, HMIR = 0x141, ROR4 = 0x124, ROR8 = 0x128;
DI float sw32_add(float a, float b) { auto s = __builtin_amdgcn_permlane32_swap(__float_as_uint(a), __float_as_uint(b), false, false); return __uint_as_float(s[0]) + __uint_as_float(s[1]); }
DI float sw16_add(float a, float b) { auto s = __builtin_amdgcn_permlane16_swap(__float_as_uint(a), __float_as_uint(b), false, false); return __uint_as_float(s[0]) + __uint_as_float(s[1]); }
DI float sw32_max(float a) { auto s = __builtin_amdgcn_permlane32_swap(__float_as_uint(a), __float_as_uint(a), false, false); return fmaxf(__uint_as_float(s[0]), __uint_as_float(s[1])); }
DI float sw16_max(float a) { auto s = __builtin_amdgcn_permlane16_swap(__float_as_uint(a), __float_as_uint(a), false, false); return fmaxf(__uint_as_float(s[0]), __uint_as_float(s[1])); }
DI u32 pk2(float lo, float hi) { f32x2 p = {lo, hi}; return __builtin_bit_cast(u32, __builtin_convertvector(p, bf16x2)); }
DI float bflo(u32 x) { return __uint_as_float(x << 16); }
DI float bfhi(u32 x) { return __uint_as_float(x & 0xffff0000u); }
DI f32x4 mfma16(v4u a, v4u b, f32x4 c) { return __builtin_amdgcn_mfma_f32_16x16x32_bf16(__builtin_bit_cast(bf16x8, a), __builtin_bit_cast(bf16x8, b), c, 0, 0, 0); }
DI f32x16 mfma32(v4u a, v4u b, f32x16 c) { return __builtin_amdgcn_mfma_f32_32x32x16_bf16(__builtin_bit_cast(bf16x8, a), __builtin_bit_cast(bf16x8, b), c, 0, 0, 0); }

DI v4u lds128(LAS char* L, u32 off) { return *(LAS v4u*)(L + off); }
DI v2u tr64(LAS char* L, u32 off) { return __builtin_bit_cast(v2u, __builtin_amdgcn_ds_read_tr16_b64_v4i16((LAS s16x4*)(L + off))); }
DI v4u tr128(LAS char* L, u32 o0, u32 o1) { v2u a = tr64(L, o0), b = tr64(L, o1); return v4u{a.x, a.y, b.x, b.y}; }

DI void dma4(const void* sbase, u32 v0, u32 v1, u32 v2, u32 v3, u32 lds) {
  asm volatile("s_mov_b32 m0, %5\n\ts_nop 0\n\t"
               "global_load_lds_dwordx4 %0, %4 nt\n\t"
               "global_load_lds_dwordx4 %1, %4 offset:1024 nt\n\t"
               "global_load_lds_dwordx4 %2, %4 offset:2048 nt\n\t"
               "global_load_lds_dwordx4 %3, %4 offset:3072 nt"
               ::"v"(v0), "v"(v1), "v"(v2), "v"(v3), "s"(sbase), "s"(lds) : "memory");
}
DI void dma2a(const void* sbase, u32 v0, u32 v1, u32 lds) {
  asm volatile("s_mov_b32 m0, %3\n\ts_nop 0\n\t"
               "global_load_lds_dwordx4 %0, %2 nt\n\t"
               "global_load_lds_dwordx4 %1, %2 offset:1024 nt"
               ::"v"(v0), "v"(v1), "s"(sbase), "s"(lds) : "memory");
}
DI void dma2b(const void* sbase, u32 v2, u32 v3, u32 lds) {
  asm volatile("s_mov_b32 m0, %3\n\ts_nop 0\n\t"
               "global_load_lds_dwordx4 %0, %2 offset:2048 nt\n\t"
               "global_load_lds_dwordx4 %1, %2 offset:3072 nt"
               ::"v"(v2), "v"(v3), "s"(sbase), "s"(lds) : "memory");
}
template <int OFF> DI void ld_sc1(v4u& d, const void* sbase, u32 voff) {
  asm volatile("global_load_dwordx4 %0, %1, %2 offset:%3 sc1" : "=v"(d) : "v"(voff), "s"(sbase), "i"(OFF) : "memory");
}
template <int OFF> DI void ld_plain(v4u& d, const void* sbase, u32 voff) {
  asm volatile("global_load_dwordx4 %0, %1, %2 offset:%3" : "=v"(d) : "v"(voff), "s"(sbase), "i"(OFF) : "memory");
}
DI void st_sc1(void* sbase, u32 voff, v4u d) {
  asm volatile("global_store_dwordx4 %0, %1, %2 sc1\n\ts_nop 1" ::"v"(voff), "v"(d), "s"(sbase) : "memory");
}
#define PIN1(a) asm volatile("" : "+v"(a))
DI u32 tagbad(const v4u& g, u32 ta, u32 tb) { return (g.y ^ ta) | (g.w ^ tb); }
DI bool wave_ok(u32 bad) { return __builtin_amdgcn_ballot_w64(bad != 0u) == 0ull; }

struct Ctx { LAS char* L; char* ws; int lane, w; u32 ta, tb; };

DI void dma_wblock(const char* blk, u32 rstride, u32 lds, int lane, int w) {
  const u32 r = (u32)lane >> 4, c = (u32)lane & 15u;
  const u32 b = (16u * w + r) * rstride;
  const u32 x = c ^ (r << 2);
  dma4(blk, b + 16u * x, b + 4u * rstride - 1024u + 16u * (x ^ 1u), b + 8u * rstride - 2048u + 16u * (x ^ 2u), b + 12u * rstride - 3072u + 16u * (x ^ 3u),
       lds + 4096u * w);
}
DI void read_wfrags(LAS char* L, u32 lds, int lane, int w, v4u (&a)[4]) {
  const u32 g = (u32)lane >> 4, q = ((u32)lane & 15u) >> 2, p = (u32)lane & 3u;
  const u32 ch = 2u * w + (p >> 1);
  const u32 o0 = lds + 256u * (8u * g + q) + 16u * (ch ^ ((q << 2) | ((2u * g) & 3u))) + 8u * (p & 1u);
  const u32 o1 = lds + 256u * (8u * g + 4u + q) + 16u * (ch ^ ((q << 2) | ((2u * g + 1u) & 3u))) + 8u * (p & 1u);
#pragma unroll
  for (int ks = 0; ks < 4; ++ks) a[ks] = tr128(L, o0 + 8192u * ks, o1 + 8192u * ks);
}
template <int MASK> DI void read_bfrags(LAS char* L, u32 img, int lane, v4u (&b)[4]) {
  const u32 o = img + 272u * ((u32)lane & MASK) + 16u * ((u32)lane >> 4);
#pragma unroll
  for (int s = 0; s < 4; ++s) b[s] = lds128(L, o + 64u * s);
}
DI void publish_d(void* dst, f32x4 d, int lane, int w, u32 ta, u32 tb) {
  const u32 col = (u32)lane & 15u, g = (u32)lane >> 4, pr = (col >> 1) & 1u;
  const float v0 = pr ? d[2] : d[0], v1 = pr ? d[3] : d[1];
  if (col < 4u) st_sc1(dst, (col & 1u) * 1024u + (8u * w + 2u * g + pr) * 16u, v4u{__float_as_uint(v0), ta, __float_as_uint(v1), tb});
}
DI f32x4 mv4(const v4u (&a)[4], const v4u (&b)[4]) {
  f32x4 d = {0.f, 0.f, 0.f, 0.f};
#pragma unroll
  for (int s = 0; s < 4; ++s) d = mfma16(a[s], b[s], d);
  return d;
}
DI void gather4(const char* src, LAS char* L, u32 img, int lane, int w, u32 ta, u32 tb) {
  const u32 gi = (16u * w + ((u32)lane >> 2)) & 63u, b = (u32)w >> 2;
  const u32 off = ((u32)lane & 3u) * 2048u + b * 1024u + gi * 16u;
  v4u g;
  for (int it = 0;; ++it) {
    ld_sc1<0>(g, src, off);
    asm volatile("s_waitcnt vmcnt(0)" : "+v"(g)::"memory");
    if (wave_ok(tagbad(g, ta, tb)) || it > SPIN_CAP) break;
    __builtin_amdgcn_s_sleep(1);
  }
  float v0 = __uint_as_float(g.x), v1 = __uint_as_float(g.z);
  v0 += dppf<XOR1>(v0); v1 += dppf<XOR1>(v1);
  v0 += dppf<XOR2>(v0); v1 += dppf<XOR2>(v1);
  *(LAS u32*)(L + img + 272u * b + 4u * gi) = pk2(v0, v1);
}
DI void final_reduce(const char* ws, u16* out, int which, int idx, int lane, int w, u32 ta, u32 tb) {
  const u32 mq = (u32)idx >> 5, sub = (u32)idx & 31u;
  const char* src = ws + WS_OUTP + (which * 4u + mq) * 65536u;
  const u32 off = ((u32)lane >> 1) * 2048u + (u32)w * 1024u + (2u * sub + ((u32)lane & 1u)) * 16u;
  v4u g;
  for (int it = 0;; ++it) {
    ld_sc1<0>(g, src, off);
    asm volatile("s_waitcnt vmcnt(0)" : "+v"(g)::"memory");
    if (wave_ok(tagbad(g, ta, tb)) || it > SPIN_CAP) break;
    __builtin_amdgcn_s_sleep(1);
  }
  float v0 = __uint_as_float(g.x), v1 = __uint_as_float(g.z);
  v0 += dppf<XOR2>(v0); v1 += dppf<XOR2>(v1);
  v0 += dppf<ROR4>(v0); v1 += dppf<ROR4>(v1);
  v0 += dppf<ROR8>(v0); v1 += dppf<ROR8>(v1);
  v0 = sw16_add(v0, v0); v1 = sw16_add(v1, v1);
  v0 = sw32_add(v0, v0); v1 = sw32_add(v1, v1);
  if (lane < 2) *(GAS u32*)((GAS char*)out + ((u32)w * 1024u + which * 512u + 128u * mq + 4u * sub + 2u * lane) * 2u) = pk2(v0, v1);
}

__global__ void __launch_bounds__(512, 2)
fused_decode(const u16* __restrict__ xA, const u16* __restrict__ ckv, const u16* __restrict__ wq, const u16* __restrict__ wuk,
             const u16* __restrict__ wuv, const u16* __restrict__ wo, const u16* __restrict__ xB, const u16* __restrict__ Kc,
             const u16* __restrict__ Vc, const u16* __restrict__ wqB, const u16* __restrict__ woB, u16* __restrict__ out, char* __restrict__ ws) {
  __shared__ __attribute__((aligned(16))) char smem[163840];
  LAS char* L = (LAS char*)smem;
  const int lane = threadIdx.x & 63, w = __builtin_amdgcn_readfirstlane(threadIdx.x >> 6);
  const int bid = blockIdx.x, roleB = (bid >> 3) & 1, idx = ((bid >> 4) << 3) | (bid & 7);
  const int hh_ = (idx & 7) + 8 * (idx >> 5), qq_ = (idx >> 3) & 3;
  u32 cal0 = 0, calx = 0; u64 c0_; asm volatile("s_memtime %0\n\ts_waitcnt lgkmcnt(0)" : "=s"(c0_)::"memory");
  STAMP(0);
  {
    u64 pc; asm volatile("s_getpc_b64 %0" : "=s"(pc));
    pc &= ~(u64)0x7f;
    if (w >= 5) {
      asm volatile("s_mov_b32 m0, %2\n\ts_nop 0\n\tglobal_load_lds_dword %0, %1"
                   ::"v"((u32)lane * 128u + (u32)(w - 5) * 8192u), "s"(pc), "s"(L_S3 + 4096u * (u32)w) : "memory");
    } else {
      asm volatile("s_mov_b32 m0, %2\n\ts_nop 0\n\tglobal_load_lds_dword %0, %1" ::"v"(0u), "s"(pc), "s"(L_S3 + 4096u * (u32)w) : "memory");
    }
  }
  const u32 xo = ((u32)lane & 1u) * 1024u + (u32)qq_ * 256u + ((u32)lane >> 4) * 16u;
#define LOAD_EX(x) \
  u32 ev; v4u xf[4]; \
  asm volatile("global_load_dword %0, %1, %2 sc1" : "=v"(ev) : "v"(0u), "s"(ws) : "memory"); \
  ld_plain<0>(xf[0], x, xo); ld_plain<64>(xf[1], x, xo); ld_plain<128>(xf[2], x, xo); ld_plain<192>(xf[3], x, xo);
  asm volatile("" ::"s"(xA), "s"(ckv), "s"(wq), "s"(wuk), "s"(wuv), "s"(wo), "s"(xB), "s"(Kc), "s"(Vc), "s"(wqB), "s"(woB), "s"(out), "s"(ws));
  u32 ta, tb, e0;

  if (!roleB) {
    const int b = (idx & 7) >> 2, sp = (idx >> 3) * 4 + (idx & 3);
    const char* kv = (const char*)ckv + ((size_t)b * 8192 + (size_t)sp * 128 + 4u * w) * 1024u;
    const u32 kx = (16u * lane) ^ (16u * (w & 3));
    const u32 k0 = kx, k1 = kx ^ 64u, k2 = kx ^ 128u, k3 = kx ^ 192u;
    const u32 lw = 4096u * w;
    LOAD_EX(xA)
    dma_wblock((const char*)wq + (size_t)qq_ * 128 * 8192 + hh_ * 256, 8192u, L_S4, lane, w);
    dma2a(kv, k0, k1, L_S0 + lw);
    asm volatile("s_waitcnt vmcnt(2)" : "+v"(ev), "+v"(xf[0]), "+v"(xf[1]), "+v"(xf[2]), "+v"(xf[3])::"memory");
    STAMP(1);
    dma2b(kv, k2, k3, L_S0 + lw);
    e0 = __builtin_amdgcn_readfirstlane(ev);
    ta = e0 * 0x9E3779B1u + 0x7F4A7C15u; tb = (e0 ^ (e0 >> 15)) * 0x85EBCA77u + 0x3C6EF372u;
    BAR();
    {
      v4u a[4]; read_wfrags(L, L_S4, lane, w, a);
      f32x4 d = mv4(a, xf);
      publish_d(ws + WS_QA + ((u32)hh_ * 4u + qq_) * 2048u, d, lane, w, ta, tb);
    }
    STAMP(2);
    dma4(kv + 32768, k0, k1, k2, k3, L_S1 + lw);
    dma4(kv + 65536, k0, k1, k2, k3, L_S2 + lw);
    const int kt = w & 3, ch = w >> 2;
    v4u ka[16], vt[8][2];
    const u32 r31 = (u32)lane & 31u, hf = (u32)lane >> 5;
    const u32 fr = ((r31 & 3u) << 2) | ((r31 >> 2) & 3u);
    const u32 rowo = 1024u * r31 + 16u * (hf ^ fr) + 512u * ch;
    const u32 blk = ((u32)lane >> 4) & 1u, q4 = ((u32)lane & 15u) >> 2, p4 = (u32)lane & 3u;
    const u32 chx = 8u * w + 2u * blk + (p4 >> 1);
    const u32 tro0 = 1024u * (8u * hf + q4) + 16u * (chx ^ ((q4 << 2) | (2u * hf))) + 8u * (p4 & 1u);
    const u32 tro1 = 1024u * (8u * hf + 4u + q4) + 16u * (chx ^ ((q4 << 2) | (2u * hf + 1u))) + 8u * (p4 & 1u);
#define READ_TILE(t)                                                                                          \
    {                                                                                                         \
      if (kt == (t)) {                                                                                        \
        _Pragma("unroll") for (int s = 0; s < 16; ++s) ka[s] = lds128(L, 32768u * (t) + ((rowo ^ (32u * (s & 7))) + 256u * (s >> 3))); \
      }                                                                                                       \
      _Pragma("unroll") for (int ks = 0; ks < 2; ++ks)                                                        \
        _Pragma("unroll") for (int u = 0; u < 2; ++u)                                                         \
          vt[2 * (t) + ks][u] = tr128(L, 32768u * (t) + 16384u * ks + (tro0 ^ (64u * u)), 32768u * (t) + 16384u * ks + (tro1 ^ (64u * u))); \
    }
#pragma unroll
    for (int s = 0; s < 16; ++s) ka[s] = v4u{0u, 0u, 0u, 0u};
    WAITVM(8); BAR(); STAMP(3);
    READ_TILE(0)
    dma4(kv + 98304, k0, k1, k2, k3, L_S3 + lw);
    WAITVM(8); BAR(); STAMP(4);
    READ_TILE(1)
    v4u g[8];
    const char* qsrc = ws + WS_QABS + (u32)b * 65536u + (u32)w * 8192u;
    const u32 go = 16u * lane;
#define GATHER_Q() { ld_sc1<0>(g[0], qsrc, go); ld_sc1<1024>(g[1], qsrc, go); ld_sc1<2048>(g[2], qsrc, go); ld_sc1<3072>(g[3], qsrc, go); \
                     ld_sc1<0>(g[4], qsrc + 4096, go); ld_sc1<1024>(g[5], qsrc + 4096, go); ld_sc1<2048>(g[6], qsrc + 4096, go); ld_sc1<3072>(g[7], qsrc + 4096, go); }
    GATHER_Q()
    WAITVM(12); BAR(); STAMP(5);
    READ_TILE(2)
    WAITVM(8); BAR(); STAMP(6);
    READ_TILE(3)
#define WAIT_Q() asm volatile("s_waitcnt vmcnt(0)" : "+v"(g[0]), "+v"(g[1]), "+v"(g[2]), "+v"(g[3]), "+v"(g[4]), "+v"(g[5]), "+v"(g[6]), "+v"(g[7])::"memory")
#define BAD_Q() (tagbad(g[0], ta, tb) | tagbad(g[1], ta, tb) | tagbad(g[2], ta, tb) | tagbad(g[3], ta, tb) | tagbad(g[4], ta, tb) | tagbad(g[5], ta, tb) | tagbad(g[6], ta, tb) | tagbad(g[7], ta, tb))
    WAIT_Q();
    if (!wave_ok(BAD_Q())) {
      for (int it = 0; it < SPIN_CAP; ++it) {
        __builtin_amdgcn_s_sleep(2);
        GATHER_Q()
        WAIT_Q();
        STAMPV(30, it + 1);
        if (wave_ok(BAD_Q())) break;
      }
    }
    STAMP(7);
#pragma unroll
    for (int i = 0; i < 8; ++i) {
      const u32 gi = 64u * (i & 1) + lane, f = ((u32)(i >> 1) << 2) | (u32)(w & 3);
      *(LAS v2u*)(L + L_S4 + 1024u * (4u * w + (i >> 1)) + 16u * ((gi >> 1) ^ f) + 8u * (gi & 1u)) = v2u{g[i].x, g[i].z};
    }
    BAR();
    STAMP(8);
    dma_wblock((const char*)wuv + (size_t)qq_ * 128 * 8192 + hh_ * 256, 8192u, L_S2, lane, w);
    {
      f32x16 acc;
#pragma unroll
      for (int i = 0; i < 16; ++i) acc[i] = 0.f;
#pragma unroll
      for (int s = 0; s < 16; ++s) acc = mfma32(ka[s], lds128(L, L_S4 + ((rowo ^ (32u * (s & 7))) + 256u * (s >> 3))), acc);
      const u32 so = L_S0 + 16896u * ch + 528u * r31 + 128u * kt + 16u * hf;
#pragma unroll
      for (int a = 0; a < 4; ++a) *(LAS f32x4*)(L + so + 32u * a) = f32x4{acc[4 * a], acc[4 * a + 1], acc[4 * a + 2], acc[4 * a + 3]};
    }
    BAR();
    STAMP(9);
    constexpr u32 L_P = L_S1 + 16384;
    float m2, lsum;
    {
      const u32 hd = 4u * w + ((u32)lane >> 4), j = (u32)lane & 15u;
      const u32 so = L_S0 + 528u * hd + 32u * j;
      const f32x4 s0 = *(LAS f32x4*)(L + so) + *(LAS f32x4*)(L + so + 16896u);
      const f32x4 s1 = *(LAS f32x4*)(L + so + 16u) + *(LAS f32x4*)(L + so + 16896u + 16u);
      float m = fmaxf(fmaxf(fmaxf(s0[0], s0[1]), fmaxf(s0[2], s0[3])), fmaxf(fmaxf(s1[0], s1[1]), fmaxf(s1[2], s1[3])));
      m = fmaxf(m, dppf<XOR1>(m)); m = fmaxf(m, dppf<XOR2>(m)); m = fmaxf(m, dppf<ROR4>(m)); m = fmaxf(m, dppf<ROR8>(m));
      m2 = m * SC2;
      float p[8];
#pragma unroll
      for (int i = 0; i < 4; ++i) { p[i] = __builtin_amdgcn_exp2f(fmaf(s0[i], SC2, -m2)); p[4 + i] = __builtin_amdgcn_exp2f(fmaf(s1[i], SC2, -m2)); }
      *(LAS v4u*)(L + L_P + 256u * hd + 16u * (j ^ (hd & 15u))) = v4u{pk2(p[0], p[1]), pk2(p[2], p[3]), pk2(p[4], p[5]), pk2(p[6], p[7])};
      float l = ((p[0] + p[1]) + (p[2] + p[3])) + ((p[4] + p[5]) + (p[6] + p[7]));
      l += dppf<XOR1>(l); l += dppf<XOR2>(l); l += dppf<ROR4>(l); l += dppf<ROR8>(l);
      lsum = l;
    }
    BAR();
    STAMP(10);
    {
      f32x16 o0, o1;
#pragma unroll
      for (int i = 0; i < 16; ++i) { o0[i] = 0.f; o1[i] = 0.f; }
      const u32 po = L_P + 256u * r31 + 16u * (hf ^ (r31 & 15u));
#pragma unroll
      for (int s = 0; s < 8; ++s) {
        const v4u pf = lds128(L, po ^ (32u * s));
        o0 = mfma32(vt[s][0], pf, o0);
        o1 = mfma32(vt[s][1], pf, o1);
      }
      const u32 sb = L_S3 + lw + 128u * r31 + 8u * hf, sx = 16u * (r31 & 7u);
#pragma unroll
      for (int a = 0; a < 4; ++a) {
        *(LAS v2u*)(L + sb + ((16u * a) ^ sx)) = v2u{pk2(o0[4 * a], o0[4 * a + 1]), pk2(o0[4 * a + 2], o0[4 * a + 3])};
        *(LAS v2u*)(L + sb + ((64u + 16u * a) ^ sx)) = v2u{pk2(o1[4 * a], o1[4 * a + 1]), pk2(o1[4 * a + 2], o1[4 * a + 3])};
      }
      const u32 rr = (u32)lane >> 3, kk = (u32)lane & 7u;
      char* od = ws + WS_OA + (((u32)b * 32u * 4u + (u32)(w >> 1)) * 64u + sp) * 256u + (u32)(w & 1) * 128u;
      v4u t[4];
#pragma unroll
      for (int i = 0; i < 4; ++i) t[i] = lds128(L, L_S3 + lw + 1024u * i + 128u * rr + 16u * (kk ^ rr));
#pragma unroll
      for (int i = 0; i < 4; ++i) st_sc1(od, (8u * i + rr) * 65536u + 16u * kk, t[i]);
    }
    STAMP(11);
    WAITVM(0); BAR();
    STAMP(12);
    if ((lane & 15) == 0)
      st_sc1(ws + WS_MLA + (u32)b * 32768u + (u32)sp * 16u, (4u * w + ((u32)lane >> 4)) * 1024u, v4u{__float_as_uint(m2), ta, __float_as_uint(lsum), tb});
    v4u wa[4];
    read_wfrags(L, L_S2, lane, w, wa);
    constexpr u32 L_IMG = L_S4;
    {
      const u32 bb = (u32)w >> 2, cs = (u32)w & 3u;
      const u32 rec = ((u32)lane & 3u) | (((u32)lane >> 4) << 2), chk = ((u32)lane >> 2) & 3u;
      const char* fsrc = ws + WS_MLA + (bb * 32u + hh_) * 1024u;
      v4u f[4];
      for (int it = 0;; ++it) {
        ld_sc1<0>(f[0], fsrc, rec * 16u); ld_sc1<256>(f[1], fsrc, rec * 16u); ld_sc1<512>(f[2], fsrc, rec * 16u); ld_sc1<768>(f[3], fsrc, rec * 16u);
        asm volatile("s_waitcnt vmcnt(0)" : "+v"(f[0]), "+v"(f[1]), "+v"(f[2]), "+v"(f[3])::"memory");
        if (wave_ok(tagbad(f[0], ta, tb) | tagbad(f[1], ta, tb) | tagbad(f[2], ta, tb) | tagbad(f[3], ta, tb)) || it > SPIN_CAP) break;
        __builtin_amdgcn_s_sleep(1);
      }
      STAMP(13);
      const char* psrc = ws + WS_OA + ((bb * 32u + hh_) * 4u + qq_) * 16384u + cs * 64u;
      const u32 po = rec * 256u + chk * 16u;
      v4u d[4];
      ld_sc1<0>(d[0], psrc, po); ld_sc1<0>(d[1], psrc + 4096, po); ld_sc1<0>(d[2], psrc + 8192, po); ld_sc1<0>(d[3], psrc + 12288, po);
      dma_wblock((const char*)wo + (size_t)hh_ * 128 * 1024 + qq_ * 256, 1024u, L_S0, lane, w);
      float M = fmaxf(fmaxf(__uint_as_float(f[0].x), __uint_as_float(f[1].x)), fmaxf(__uint_as_float(f[2].x), __uint_as_float(f[3].x)));
      M = fmaxf(M, dppf<XOR1>(M)); M = fmaxf(M, dppf<XOR2>(M)); M = sw16_max(M); M = sw32_max(M);
      float e[4], Ls = 0.f;
#pragma unroll
      for (int i = 0; i < 4; ++i) { e[i] = __builtin_amdgcn_exp2f(__uint_as_float(f[i].x) - M); Ls = fmaf(e[i], __uint_as_float(f[i].z), Ls); }
      Ls += dppf<XOR1>(Ls); Ls += dppf<XOR2>(Ls); Ls = sw16_add(Ls, Ls); Ls = sw32_add(Ls, Ls);
      const float inv = __builtin_amdgcn_rcpf(Ls);
      asm volatile("s_waitcnt vmcnt(4)" : "+v"(d[0]), "+v"(d[1]), "+v"(d[2]), "+v"(d[3])::"memory");
      STAMP(14);
      float a[8];
#pragma unroll
      for (int k = 0; k < 8; ++k) a[k] = 0.f;
#pragma unroll
      for (int i = 0; i < 4; ++i) {
        const float wi = e[i] * inv;
#pragma unroll
        for (int k = 0; k < 4; ++k) { a[2 * k] = fmaf(wi, bflo(d[i][k]), a[2 * k]); a[2 * k + 1] = fmaf(wi, bfhi(d[i][k]), a[2 * k + 1]); }
      }
      const float r0 = sw32_add(a[0], a[4]), r1 = sw32_add(a[1], a[5]), r2 = sw32_add(a[2], a[6]), r3 = sw32_add(a[3], a[7]);
      float u0 = sw16_add(r0, r2), u1 = sw16_add(r1, r3);
      u0 += dppf<XOR1>(u0); u1 += dppf<XOR1>(u1); u0 += dppf<XOR2>(u0); u1 += dppf<XOR2>(u1);
      const u32 hi = pk2(u0, u1), lo = pk2(u0 - bflo(hi), u1 - bfhi(hi));
      const u32 cl = 32u * cs + 8u * chk + 4u * ((u32)lane >> 5) + 2u * (((u32)lane >> 4) & 1u);
      *(LAS u32*)(L + L_IMG + 272u * (bb + 2u * ((u32)lane & 1u)) + 2u * cl) = (lane & 1) ? lo : hi;
    }
    BAR();
    {
      v4u bf[4]; read_bfrags<3>(L, L_IMG, lane, bf);
      f32x4 d = mv4(wa, bf);
#pragma unroll
      for (int i = 0; i < 4; ++i) d[i] += dppf<XOR2>(d[i]);
      publish_d(ws + WS_CTX + ((u32)hh_ * 4u + qq_) * 2048u, d, lane, w, ta, tb);
    }
    STAMP(15);
    gather4(ws + WS_CTX + (u32)hh_ * 8192u, L, L_IMG + 2048u, lane, w, ta, tb);
    STAMP(16);
    BAR();
    {
      v4u a[4], bf[4];
      read_wfrags(L, L_S0, lane, w, a);
      read_bfrags<1>(L, L_IMG + 2048u, lane, bf);
      publish_d(ws + WS_OUTP + ((u32)qq_ * 32u + hh_) * 2048u, mv4(a, bf), lane, w, ta, tb);
    }
    STAMP(17);
    if (w < 2) {
      final_reduce(ws, out, 0, idx, lane, w, ta, tb);
      if (idx == 0 && w == 0 && lane == 0) __hip_atomic_store((GAS u32*)(ws + WS_E), e0 + 1u, __ATOMIC_RELAXED, __HIP_MEMORY_SCOPE_AGENT);
    }
    STAMP(18);
  } else {
    const u32 r4 = (u32)lane >> 4, c16 = (u32)lane & 15u;
    LOAD_EX(xB)
    {
      const u32 bs = (16u * w + r4) * 8192u, x = c16 ^ r4;
      dma4((const char*)wuk + (size_t)qq_ * 128 * 8192 + hh_ * 256, bs + 16u * x, bs + 32768u - 1024u + 16u * (x ^ 4u), bs + 65536u - 2048u + 16u * (x ^ 8u),
           bs + 98304u - 3072u + 16u * (x ^ 12u), L_S0 + 4096u * w);
    }
    asm volatile("s_waitcnt vmcnt(0)" : "+v"(ev), "+v"(xf[0]), "+v"(xf[1]), "+v"(xf[2]), "+v"(xf[3])::"memory");
    STAMP(1);
    e0 = __builtin_amdgcn_readfirstlane(ev);
    ta = e0 * 0x9E3779B1u + 0x7F4A7C15u; tb = (e0 ^ (e0 >> 15)) * 0x85EBCA77u + 0x3C6EF372u;
    constexpr u32 L_IMG = L_S1;
    v4u uk[4];
    {
      const u32 o = L_S0 + 4096u * w + 256u * c16 + 16u * (r4 ^ c16);
#pragma unroll
      for (int s = 0; s < 4; ++s) uk[s] = lds128(L, o ^ (64u * s));
    }
    gather4(ws + WS_QA + (u32)hh_ * 8192u, L, L_IMG, lane, w, ta, tb);
    STAMP(2);
    dma_wblock((const char*)wqB + (size_t)qq_ * 128 * 8192 + hh_ * 256, 8192u, L_S4, lane, w);
    BAR();
    {
      v4u bf[4]; read_bfrags<1>(L, L_IMG, lane, bf);
      const f32x4 d = mv4(uk, bf);
      if (c16 < 2u)
        st_sc1(ws + WS_QABS + (u32)hh_ * 2048u + (32u * qq_ + 4u * w) * 16u, c16 * 65536u + r4 * 16u, v4u{pk2(d[0], d[1]), ta, pk2(d[2], d[3]), tb});
    }
    STAMP(3);
    WAITVM(0); BAR();
    STAMP(4);
    {
      v4u a[4]; read_wfrags(L, L_S4, lane, w, a);
      publish_d(ws + WS_QB + ((u32)hh_ * 4u + qq_) * 2048u, mv4(a, xf), lane, w, ta, tb);
    }
    STAMP(5);
    const int b = idx >> 6, kvh = (idx >> 3) & 7, j = idx & 7;
    const size_t kvo = (((size_t)b * 8 + kvh) * 8192 + 6144 + 256u * j + 32u * w) * 256u;
    const u32 kb = L_S0 + 8192u * w, vb = L_S2 + 8192u * w;
    {
      const u32 x = c16 ^ r4, bs = 256u * r4;
      dma4((const char*)Kc + kvo, bs + 16u * x, bs + 16u * (x ^ 4u), bs + 16u * (x ^ 8u), bs + 16u * (x ^ 12u), kb);
      dma4((const char*)Kc + kvo + 4096, bs + 16u * x, bs + 16u * (x ^ 4u), bs + 16u * (x ^ 8u), bs + 16u * (x ^ 12u), kb + 4096u);
    }
    v4u g[2];
    const char* qsrc = ws + WS_QB + (4u * kvh + (u32)(w >> 1)) * 8192u + (u32)b * 1024u + (u32)(w & 1) * 512u;
    const u32 go = ((u32)lane & 3u) * 2048u + ((u32)lane >> 2) * 16u;
    ld_sc1<0>(g[0], qsrc, go); ld_sc1<256>(g[1], qsrc, go);
    {
      const u32 bs = 256u * r4 + 16u * (c16 & 1u), x = (c16 >> 1) ^ r4;
      dma4((const char*)Vc + kvo, bs + 32u * x, bs + 32u * (x ^ 4u), bs + 32u * x, bs + 32u * (x ^ 4u), vb);
      dma4((const char*)Vc + kvo + 4096, bs + 32u * x, bs + 32u * (x ^ 4u), bs + 32u * x, bs + 32u * (x ^ 4u), vb + 4096u);
    }
    asm volatile("s_waitcnt vmcnt(8)" : "+v"(g[0]), "+v"(g[1])::"memory");
    STAMP(6);
    v4u kf[2][4];
    {
      const u32 o = kb + 256u * c16 + 16u * (r4 ^ c16);
#pragma unroll
      for (int t = 0; t < 2; ++t)
#pragma unroll
        for (int s = 0; s < 4; ++s) kf[t][s] = lds128(L, (o ^ (64u * s)) + 4096u * t);
    }
    for (int it = 0;; ++it) {
      if (wave_ok(tagbad(g[0], ta, tb) | tagbad(g[1], ta, tb)) || it > SPIN_CAP) { STAMPV(30, it); break; }
      __builtin_amdgcn_s_sleep(1);
      ld_sc1<0>(g[0], qsrc, go); ld_sc1<256>(g[1], qsrc, go);
      asm volatile("s_waitcnt vmcnt(0)" : "+v"(g[0]), "+v"(g[1])::"memory");
    }
    constexpr u32 L_IMG2 = L_S4;
    BAR();
#pragma unroll
    for (int i = 0; i < 2; ++i) {
      float v0 = __uint_as_float(g[i].x), v1 = __uint_as_float(g[i].z);
      v0 += dppf<XOR1>(v0); v1 += dppf<XOR1>(v1); v0 += dppf<XOR2>(v0); v1 += dppf<XOR2>(v1);
      *(LAS u32*)(L + L_IMG2 + 272u * (u32)(w >> 1) + 4u * ((u32)(w & 1) * 32u + 16u * i + ((u32)lane >> 2))) = pk2(v0, v1);
    }
    BAR();
    STAMP(7);
    v4u pb; float m2, lsum;
    {
      v4u bf[4]; read_bfrags<3>(L, L_IMG2, lane, bf);
      const f32x4 s0 = mv4(kf[0], bf), s1 = mv4(kf[1], bf);
      float m = fmaxf(fmaxf(fmaxf(s0[0], s0[1]), fmaxf(s0[2], s0[3])), fmaxf(fmaxf(s1[0], s1[1]), fmaxf(s1[2], s1[3])));
      m = sw16_max(m); m = sw32_max(m);
      m2 = m * SC2;
      float p[8];
#pragma unroll
      for (int i = 0; i < 4; ++i) { p[i] = __builtin_amdgcn_exp2f(fmaf(s0[i], SC2, -m2)); p[4 + i] = __builtin_amdgcn_exp2f(fmaf(s1[i], SC2, -m2)); }
      pb = v4u{pk2(p[0], p[1]), pk2(p[2], p[3]), pk2(p[4], p[5]), pk2(p[6], p[7])};
      float l = ((p[0] + p[1]) + (p[2] + p[3])) + ((p[4] + p[5]) + (p[6] + p[7]));
      l = sw16_add(l, l); lsum = sw32_add(l, l);
    }
    BAR();
    dma_wblock((const char*)woB + (size_t)hh_ * 128 * 1024 + qq_ * 256, 1024u, L_S4, lane, w);
    WAITVM(4);
    STAMP(8);
    {
      const u32 q4 = c16 >> 2, p4 = (u32)lane & 3u, rw = 4u * r4 + q4;
      const u32 o = vb + 256u * rw + 32u * (rw & 7u) + 8u * p4;
#pragma unroll
      for (int c = 0; c < 8; ++c) {
        const v4u a = tr128(L, o ^ (32u * c), (o ^ (32u * c)) + 4096u);
        const f32x4 d = mfma16(a, pb, f32x4{0.f, 0.f, 0.f, 0.f});
        if (c16 < 4u) *(LAS f32x4*)(L + kb + 528u * c16 + 64u * c + 16u * r4) = d;
      }
      if (lane < 4) *(LAS f32x2*)(L + kb + 4096u + 8u * lane) = f32x2{m2, lsum};
    }
    BAR();
    STAMP(9);
    if (w < 4) {
      const f32x2 ml = *(LAS f32x2*)(L + L_S0 + 8192u * ((u32)lane & 7u) + 4096u + 8u * w);
      float M = ml[0];
      M = fmaxf(M, dppf<XOR1>(M)); M = fmaxf(M, dppf<XOR2>(M)); M = fmaxf(M, dppf<HMIR>(M));
      const float e = __builtin_amdgcn_exp2f(ml[0] - M);
      float Ls = e * ml[1];
      Ls += dppf<XOR1>(Ls); Ls += dppf<XOR2>(Ls); Ls += dppf<HMIR>(Ls);
      float o0 = 0.f, o1 = 0.f;
#pragma unroll
      for (int v = 0; v < 8; ++v) {
        const float ev_ = __builtin_bit_cast(float, __builtin_amdgcn_readlane(__builtin_bit_cast(int, e), v));
        const f32x2 ov = *(LAS f32x2*)(L + L_S0 + 8192u * v + 528u * w + 8u * lane);
        o0 = fmaf(ev_, ov[0], o0); o1 = fmaf(ev_, ov[1], o1);
      }
      const u32 rec = ((u32)b * 32u + 4u * kvh + w) * 8u + j;
      st_sc1(ws + WS_OB + rec * 1024u, 16u * lane, v4u{__float_as_uint(o0), ta, __float_as_uint(o1), tb});
      if (lane == 0) st_sc1(ws + WS_MLB + rec * 16u, 0u, v4u{__float_as_uint(M), ta, __float_as_uint(Ls), tb});
    }
    STAMP(10);
    constexpr u32 L_IMG3 = L_S2;
    {
      const u32 bb = (u32)w >> 2, jj = ((u32)lane & 3u) | (((u32)lane >> 5) << 2), gl = ((u32)lane >> 2) & 7u;
      const char* osrc = ws + WS_OB + (bb * 32u + hh_) * 8192u + (u32)(w & 3) * 256u;
      const char* msrc = ws + WS_MLB + (bb * 32u + hh_) * 128u;
      v4u d0, d1, f;
      for (int it = 0;; ++it) {
        ld_sc1<0>(d0, osrc, jj * 1024u + gl * 16u); ld_sc1<128>(d1, osrc, jj * 1024u + gl * 16u); ld_sc1<0>(f, msrc, jj * 16u);
        asm volatile("s_waitcnt vmcnt(0)" : "+v"(d0), "+v"(d1), "+v"(f)::"memory");
        if (wave_ok(tagbad(d0, ta, tb) | tagbad(d1, ta, tb) | tagbad(f, ta, tb)) || it > SPIN_CAP) break;
        __builtin_amdgcn_s_sleep(1);
      }
      STAMP(11);
      float M = __uint_as_float(f.x);
      M = fmaxf(M, dppf<XOR1>(M)); M = fmaxf(M, dppf<XOR2>(M)); M = sw32_max(M);
      const float e = __builtin_amdgcn_exp2f(__uint_as_float(f.x) - M);
      float Ls = e * __uint_as_float(f.z);
      Ls += dppf<XOR1>(Ls); Ls += dppf<XOR2>(Ls); Ls = sw32_add(Ls, Ls);
      const float wi = e * __builtin_amdgcn_rcpf(Ls);
      float v0 = sw32_add(wi * __uint_as_float(d0.x), wi * __uint_as_float(d1.x));
      float v1 = sw32_add(wi * __uint_as_float(d0.z), wi * __uint_as_float(d1.z));
      v0 += dppf<XOR1>(v0); v1 += dppf<XOR1>(v1); v0 += dppf<XOR2>(v0); v1 += dppf<XOR2>(v1);
      *(LAS u32*)(L + L_IMG3 + 272u * bb + 4u * ((u32)(w & 3) * 16u + ((u32)lane >> 5) * 8u + gl)) = pk2(v0, v1);
    }
    BAR();
    {
      v4u a[4], bf[4];
      read_wfrags(L, L_S4, lane, w, a);
      read_bfrags<1>(L, L_IMG3, lane, bf);
      publish_d(ws + WS_OUTP + ((4u + qq_) * 32u + hh_) * 2048u, mv4(a, bf), lane, w, ta, tb);
    }
    STAMP(17);
    if (w < 2) final_reduce(ws, out, 1, idx, lane, w, ta, tb);
    STAMP(18);
  }
  if (CAL_STAMP == 99) { for (;;) { u64 t_; asm volatile("s_memtime %0\n\ts_waitcnt lgkmcnt(0)" : "=s"(t_)::"memory"); if (t_ - c0_ >= 96000ull) break; __builtin_amdgcn_s_sleep(1); } }
  else { for (;;) { u64 t_; asm volatile("s_memrealtime %0\n\ts_waitcnt lgkmcnt(0)" : "=s"(t_)::"memory"); if ((u32)t_ - cal0 >= 4000u + calx) break; __builtin_amdgcn_s_sleep(1); } }
}

extern "C" void kernel_launch(void* const* d_in, const int* in_sizes, int n_in, void* d_out, int out_size, void* d_ws, size_t ws_size,
                              hipStream_t stream) {
  fused_decode<<<256, 512, 0, stream>>>((const u16*)d_in[0], (const u16*)d_in[1], (const u16*)d_in[2], (const u16*)d_in[3], (const u16*)d_in[4],
                                        (const u16*)d_in[5], (const u16*)d_in[6], (const u16*)d_in[7], (const u16*)d_in[8], (const u16*)d_in[9],
                                        (const u16*)d_in[10], (u16*)d_out, (char*)d_ws);
}
```

```cpp
#include <hip/hip_runtime.h>
#include <stdint.h>

typedef unsigned u32;
typedef unsigned long long u64;
typedef unsigned short u16;
typedef u32 v4u __attribute__((ext_vector_type(4)));
typedef u32 v2u __attribute__((ext_vector_type(2)));
typedef float f32x2 __attribute__((ext_vector_type(2)));
typedef float f32x4 __attribute__((ext_vector_type(4)));
typedef float f32x16 __attribute__((ext_vector_type(16)));
typedef __bf16 bf16x8 __attribute__((ext_vector_type(8)));
typedef __bf16 bf16x2 __attribute__((ext_vector_type(2)));
typedef short s16x4 __attribute__((ext_vector_type(4)));
#define GAS __attribute__((address_space(1)))
#define LAS __attribute__((address_space(3)))
#define DI __device__ __forceinline__

constexpr u32 WS_E = 0;
constexpr u32 WS_QA = 4096;
constexpr u32 WS_QB = WS_QA + 262144;
constexpr u32 WS_QABS = WS_QB + 262144;
constexpr u32 WS_MLA = WS_QABS + 131072;
constexpr u32 WS_CTX = WS_MLA + 65536;
constexpr u32 WS_OB = WS_CTX + 262144;
constexpr u32 WS_MLB = WS_OB + 524288;
constexpr u32 WS_OUTP = WS_MLB + 8192;
constexpr u32 WS_OA = 2u << 20;
constexpr u32 WS_DBG = 8u << 20;
static_assert(WS_OUTP + 524288 <= WS_OA, "ws map");

constexpr float SC2 = 0.08838834764831845f * 1.4426950408889634f;
constexpr int SPIN_CAP = 1 << 17;

constexpr u32 L_S0 = 0, L_S1 = 32768, L_S2 = 65536, L_S3 = 98304, L_S4 = 131072;

#define STAMP(k)
#define STAMPV(k, v)

#define WAITVM(n) asm volatile("s_waitcnt vmcnt(%0)" ::"i"(n) : "memory")
#define BAR() asm volatile("s_waitcnt lgkmcnt(0)\n\ts_barrier" ::: "memory")

template <int CTRL> DI float dppf(float x) {
  return __builtin_bit_cast(float, __builtin_amdgcn_mov_dpp(__builtin_bit_cast(int, x), CTRL, 0xf, 0xf, true));
}
constexpr int XOR1 = 0xB1, XOR2 = 0x4E, HMIR = 0x141, ROR4 = 0x124, ROR8 = 0x128;
DI float sw32_add(float a, float b) { auto s = __builtin_amdgcn_permlane32_swap(__float_as_uint(a), __float_as_uint(b), false, false); return __uint_as_float(s[0]) + __uint_as_float(s[1]); }
DI float sw16_add(float a, float b) { auto s = __builtin_amdgcn_permlane16_swap(__float_as_uint(a), __float_as_uint(b), false, false); return __uint_as_float(s[0]) + __uint_as_float(s[1]); }
DI float sw32_max(float a) { auto s = __builtin_amdgcn_permlane32_swap(__float_as_uint(a), __float_as_uint(a), false, false); return fmaxf(__uint_as_float(s[0]), __uint_as_float(s[1])); }
DI float sw16_max(float a) { auto s = __builtin_amdgcn_permlane16_swap(__float_as_uint(a), __float_as_uint(a), false, false); return fmaxf(__uint_as_float(s[0]), __uint_as_float(s[1])); }
DI u32 pk2(float lo, float hi) { f32x2 p = {lo, hi}; return __builtin_bit_cast(u32, __builtin_convertvector(p, bf16x2)); }
DI float bflo(u32 x) { return __uint_as_float(x << 16); }
DI float bfhi(u32 x) { return __uint_as_float(x & 0xffff0000u); }
DI f32x4 mfma16(v4u a, v4u b, f32x4 c) { return __builtin_amdgcn_mfma_f32_16x16x32_bf16(__builtin_bit_cast(bf16x8, a), __builtin_bit_cast(bf16x8, b), c, 0, 0, 0); }
DI f32x16 mfma32(v4u a, v4u b, f32x16 c) { return __builtin_amdgcn_mfma_f32_32x32x16_bf16(__builtin_bit_cast(bf16x8, a), __builtin_bit_cast(bf16x8, b), c, 0, 0, 0); }

DI v4u lds128(LAS char* L, u32 off) { return *(LAS v4u*)(L + off); }
DI v2u tr64(LAS char* L, u32 off) { return __builtin_bit_cast(v2u, __builtin_amdgcn_ds_read_tr16_b64_v4i16((LAS s16x4*)(L + off))); }
DI v4u tr128(LAS char* L, u32 o0, u32 o1) { v2u a = tr64(L, o0), b = tr64(L, o1); return v4u{a.x, a.y, b.x, b.y}; }

DI void dma4(const void* sbase, u32 v0, u32 v1, u32 v2, u32 v3, u32 lds) {
  asm volatile("s_mov_b32 m0, %5\n\ts_nop 0\n\t"
               "global_load_lds_dwordx4 %0, %4 nt\n\t"
               "global_load_lds_dwordx4 %1, %4 offset:1024 nt\n\t"
               "global_load_lds_dwordx4 %2, %4 offset:2048 nt\n\t"
               "global_load_lds_dwordx4 %3, %4 offset:3072 nt"
               ::"v"(v0), "v"(v1), "v"(v2), "v"(v3), "s"(sbase), "s"(lds) : "memory");
}
DI void dma2a(const void* sbase, u32 v0, u32 v1, u32 lds) {
  asm volatile("s_mov_b32 m0, %3\n\ts_nop 0\n\t"
               "global_load_lds_dwordx4 %0, %2 nt\n\t"
               "global_load_lds_dwordx4 %1, %2 offset:1024 nt"
               ::"v"(v0), "v"(v1), "s"(sbase), "s"(lds) : "memory");
}
DI void dma2b(const void* sbase, u32 v2, u32 v3, u32 lds) {
  asm volatile("s_mov_b32 m0, %3\n\ts_nop 0\n\t"
               "global_load_lds_dwordx4 %0, %2 offset:2048 nt\n\t"
               "global_load_lds_dwordx4 %1, %2 offset:3072 nt"
               ::"v"(v2), "v"(v3), "s"(sbase), "s"(lds) : "memory");
}
template <int OFF> DI void ld_sc1(v4u& d, const void* sbase, u32 voff) {
  asm volatile("global_load_dwordx4 %0, %1, %2 offset:%3 sc1" : "=v"(d) : "v"(voff), "s"(sbase), "i"(OFF) : "memory");
}
template <int OFF> DI void ld_plain(v4u& d, const void* sbase, u32 voff) {
  asm volatile("global_load_dwordx4 %0, %1, %2 offset:%3" : "=v"(d) : "v"(voff), "s"(sbase), "i"(OFF) : "memory");
}
DI void st_sc1(void* sbase, u32 voff, v4u d) {
  asm volatile("global_store_dwordx4 %0, %1, %2 sc1\n\ts_nop 1" ::"v"(voff), "v"(d), "s"(sbase) : "memory");
}
#define PIN1(a) asm volatile("" : "+v"(a))
DI u32 tagbad(const v4u& g, u32 ta, u32 tb) { return (g.y ^ ta) | (g.w ^ tb); }
DI bool wave_ok(u32 bad) { return __builtin_amdgcn_ballot_w64(bad != 0u) == 0ull; }

struct Ctx { LAS char* L; char* ws; int lane, w; u32 ta, tb; };

DI void dma_wblock(const char* blk, u32 rstride, u32 lds, int lane, int w) {
  const u32 r = (u32)lane >> 4, c = (u32)lane & 15u;
  const u32 b = (16u * w + r) * rstride;
  const u32 x = c ^ (r << 2);
  dma4(blk, b + 16u * x, b + 4u * rstride - 1024u + 16u * (x ^ 1u), b + 8u * rstride - 2048u + 16u * (x ^ 2u), b + 12u * rstride - 3072u + 16u * (x ^ 3u),
       lds + 4096u * w);
}
DI void read_wfrags(LAS char* L, u32 lds, int lane, int w, v4u (&a)[4]) {
  const u32 g = (u32)lane >> 4, q = ((u32)lane & 15u) >> 2, p = (u32)lane & 3u;
  const u32 ch = 2u * w + (p >> 1);
  const u32 o0 = lds + 256u * (8u * g + q) + 16u * (ch ^ ((q << 2) | ((2u * g) & 3u))) + 8u * (p & 1u);
  const u32 o1 = lds + 256u * (8u * g + 4u + q) + 16u * (ch ^ ((q << 2) | ((2u * g + 1u) & 3u))) + 8u * (p & 1u);
#pragma unroll
  for (int ks = 0; ks < 4; ++ks) a[ks] = tr128(L, o0 + 8192u * ks, o1 + 8192u * ks);
}
template <int MASK> DI void read_bfrags(LAS char* L, u32 img, int lane, v4u (&b)[4]) {
  const u32 o = img + 272u * ((u32)lane & MASK) + 16u * ((u32)lane >> 4);
#pragma unroll
  for (int s = 0; s < 4; ++s) b[s] = lds128(L, o + 64u * s);
}
DI void publish_d(void* dst, f32x4 d, int lane, int w, u32 ta, u32 tb) {
  const u32 col = (u32)lane & 15u, g = (u32)lane >> 4, pr = (col >> 1) & 1u;
  const float v0 = pr ? d[2] : d[0], v1 = pr ? d[3] : d[1];
  if (col < 4u) st_sc1(dst, (col & 1u) * 1024u + (8u * w + 2u * g + pr) * 16u, v4u{__float_as_uint(v0), ta, __float_as_uint(v1), tb});
}
DI f32x4 mv4(const v4u (&a)[4], const v4u (&b)[4]) {
  f32x4 d = {0.f, 0.f, 0.f, 0.f};
#pragma unroll
  for (int s = 0; s < 4; ++s) d = mfma16(a[s], b[s], d);
  return d;
}
DI void gather4(const char* src, LAS char* L, u32 img, int lane, int w, u32 ta, u32 tb) {
  const u32 gi = (16u * w + ((u32)lane >> 2)) & 63u, b = (u32)w >> 2;
  const u32 off = ((u32)lane & 3u) * 2048u + b * 1024u + gi * 16u;
  v4u g;
  for (int it = 0;; ++it) {
    ld_sc1<0>(g, src, off);
    asm volatile("s_waitcnt vmcnt(0)" : "+v"(g)::"memory");
    if (wave_ok(tagbad(g, ta, tb)) || it > SPIN_CAP) break;
    __builtin_amdgcn_s_sleep(1);
  }
  float v0 = __uint_as_float(g.x), v1 = __uint_as_float(g.z);
  v0 += dppf<XOR1>(v0); v1 += dppf<XOR1>(v1);
  v0 += dppf<XOR2>(v0); v1 += dppf<XOR2>(v1);
  *(LAS u32*)(L + img + 272u * b + 4u * gi) = pk2(v0, v1);
}
DI void final_reduce(const char* ws, u16* out, int which, int idx, int lane, int w, u32 ta, u32 tb) {
  const u32 mq = (u32)idx >> 5, sub = (u32)idx & 31u;
  const char* src = ws + WS_OUTP + (which * 4u + mq) * 65536u;
  const u32 off = ((u32)lane >> 1) * 2048u + (u32)w * 1024u + (2u * sub + ((u32)lane & 1u)) * 16u;
  v4u g;
  for (int it = 0;; ++it) {
    ld_sc1<0>(g, src, off);
    asm volatile("s_waitcnt vmcnt(0)" : "+v"(g)::"memory");
    if (wave_ok(tagbad(g, ta, tb)) || it > SPIN_CAP) break;
    __builtin_amdgcn_s_sleep(1);
  }
  float v0 = __uint_as_float(g.x), v1 = __uint_as_float(g.z);
  v0 += dppf<XOR2>(v0); v1 += dppf<XOR2>(v1);
  v0 += dppf<ROR4>(v0); v1 += dppf<ROR4>(v1);
  v0 += dppf<ROR8>(v0); v1 += dppf<ROR8>(v1);
  v0 = sw16_add(v0, v0); v1 = sw16_add(v1, v1);
  v0 = sw32_add(v0, v0); v1 = sw32_add(v1, v1);
  if (lane < 2) *(GAS u32*)((GAS char*)out + ((u32)w * 1024u + which * 512u + 128u * mq + 4u * sub + 2u * lane) * 2u) = pk2(v0, v1);
}

__global__ void __launch_bounds__(512, 2)
fused_decode(const u16* __restrict__ xA, const u16* __restrict__ ckv, const u16* __restrict__ wq, const u16* __restrict__ wuk,
             const u16* __restrict__ wuv, const u16* __restrict__ wo, const u16* __restrict__ xB, const u16* __restrict__ Kc,
             const u16* __restrict__ Vc, const u16* __restrict__ wqB, const u16* __restrict__ woB, u16* __restrict__ out, char* __restrict__ ws) {
  __shared__ __attribute__((aligned(16))) char smem[163840];
  LAS char* L = (LAS char*)smem;
  const int lane = threadIdx.x & 63, w = __builtin_amdgcn_readfirstlane(threadIdx.x >> 6);
  const int bid = blockIdx.x, roleB = (bid >> 3) & 1, idx = ((bid >> 4) << 3) | (bid & 7);
  const int hh_ = (idx & 7) + 8 * (idx >> 5), qq_ = (idx >> 3) & 3;
  STAMP(0);
  {
    u64 pc; asm volatile("s_getpc_b64 %0" : "=s"(pc));
    pc &= ~(u64)0x7f;
    if (w >= 5) {
      asm volatile("s_mov_b32 m0, %2\n\ts_nop 0\n\tglobal_load_lds_dword %0, %1"
                   ::"v"((u32)lane * 128u + (u32)(w - 5) * 8192u), "s"(pc), "s"(L_S3 + 4096u * (u32)w) : "memory");
    } else {
      asm volatile("s_mov_b32 m0, %2\n\ts_nop 0\n\tglobal_load_lds_dword %0, %1" ::"v"(0u), "s"(pc), "s"(L_S3 + 4096u * (u32)w) : "memory");
    }
  }
  const u32 xo = ((u32)lane & 1u) * 1024u + (u32)qq_ * 256u + ((u32)lane >> 4) * 16u;
#define LOAD_EX(x) \
  u32 ev; v4u xf[4]; \
  asm volatile("global_load_dword %0, %1, %2 sc1" : "=v"(ev) : "v"(0u), "s"(ws) : "memory"); \
  ld_plain<0>(xf[0], x, xo); ld_plain<64>(xf[1], x, xo); ld_plain<128>(xf[2], x, xo); ld_plain<192>(xf[3], x, xo);
  asm volatile("" ::"s"(xA), "s"(ckv), "s"(wq), "s"(wuk), "s"(wuv), "s"(wo), "s"(xB), "s"(Kc), "s"(Vc), "s"(wqB), "s"(woB), "s"(out), "s"(ws));
  u32 ta, tb, e0;

  if (!roleB) {
    const int b = (idx & 7) >> 2, sp = (idx >> 3) * 4 + (idx & 3);
    const char* kv = (const char*)ckv + ((size_t)b * 8192 + (size_t)sp * 128 + 4u * w) * 1024u;
    const u32 kx = (16u * lane) ^ (16u * (w & 3));
    const u32 k0 = kx, k1 = kx ^ 64u, k2 = kx ^ 128u, k3 = kx ^ 192u;
    const u32 lw = 4096u * w;
    LOAD_EX(xA)
    dma_wblock((const char*)wq + (size_t)qq_ * 128 * 8192 + hh_ * 256, 8192u, L_S4, lane, w);
    dma2a(kv, k0, k1, L_S0 + lw);
    asm volatile("s_waitcnt vmcnt(2)" : "+v"(ev), "+v"(xf[0]), "+v"(xf[1]), "+v"(xf[2]), "+v"(xf[3])::"memory");
    STAMP(1);
    dma2b(kv, k2, k3, L_S0 + lw);
    e0 = __builtin_amdgcn_readfirstlane(ev);
    ta = e0 * 0x9E3779B1u + 0x7F4A7C15u; tb = (e0 ^ (e0 >> 15)) * 0x85EBCA77u + 0x3C6EF372u;
    BAR();
    {
      v4u a[4]; read_wfrags(L, L_S4, lane, w, a);
      f32x4 d = mv4(a, xf);
      publish_d(ws + WS_QA + ((u32)hh_ * 4u + qq_) * 2048u, d, lane, w, ta, tb);
    }
    STAMP(2);
    dma4(kv + 32768, k0, k1, k2, k3, L_S1 + lw);
    dma4(kv + 65536, k0, k1, k2, k3, L_S2 + lw);
    const int kt = w & 3, ch = w >> 2;
    v4u ka[16], vt[8][2];
    const u32 r31 = (u32)lane & 31u, hf = (u32)lane >> 5;
    const u32 fr = ((r31 & 3u) << 2) | ((r31 >> 2) & 3u);
    const u32 rowo = 1024u * r31 + 16u * (hf ^ fr) + 512u * ch;
    const u32 blk = ((u32)lane >> 4) & 1u, q4 = ((u32)lane & 15u) >> 2, p4 = (u32)lane & 3u;
    const u32 chx = 8u * w + 2u * blk + (p4 >> 1);
    const u32 tro0 = 1024u * (8u * hf + q4) + 16u * (chx ^ ((q4 << 2) | (2u * hf))) + 8u * (p4 & 1u);
    const u32 tro1 = 1024u * (8u * hf + 4u + q4) + 16u * (chx ^ ((q4 << 2) | (2u * hf + 1u))) + 8u * (p4 & 1u);
#define READ_TILE(t)                                                                                          \
    {                                                                                                         \
      if (kt == (t)) {                                                                                        \
        _Pragma("unroll") for (int s = 0; s < 16; ++s) ka[s] = lds128(L, 32768u * (t) + ((rowo ^ (32u * (s & 7))) + 256u * (s >> 3))); \
      }                                                                                                       \
      _Pragma("unroll") for (int ks = 0; ks < 2; ++ks)                                                        \
        _Pragma("unroll") for (int u = 0; u < 2; ++u)                                                         \
          vt[2 * (t) + ks][u] = tr128(L, 32768u * (t) + 16384u * ks + (tro0 ^ (64u * u)), 32768u * (t) + 16384u * ks + (tro1 ^ (64u * u))); \
    }
#pragma unroll
    for (int s = 0; s < 16; ++s) ka[s] = v4u{0u, 0u, 0u, 0u};
    WAITVM(8); BAR(); STAMP(3);
    READ_TILE(0)
    dma4(kv + 98304, k0, k1, k2, k3, L_S3 + lw);
    WAITVM(8); BAR(); STAMP(4);
    READ_TILE(1)
    v4u g[8];
    const char* qsrc = ws + WS_QABS + (u32)b * 65536u + (u32)w * 8192u;
    const u32 go = 16u * lane;
#define GATHER_Q() { ld_sc1<0>(g[0], qsrc, go); ld_sc1<1024>(g[1], qsrc, go); ld_sc1<2048>(g[2], qsrc, go); ld_sc1<3072>(g[3], qsrc, go); \
                     ld_sc1<0>(g[4], qsrc + 4096, go); ld_sc1<1024>(g[5], qsrc + 4096, go); ld_sc1<2048>(g[6], qsrc + 4096, go); ld_sc1<3072>(g[7], qsrc + 4096, go); }
    GATHER_Q()
    WAITVM(12); BAR(); STAMP(5);
    READ_TILE(2)
    WAITVM(8); BAR(); STAMP(6);
    READ_TILE(3)
#define WAIT_Q() asm volatile("s_waitcnt vmcnt(0)" : "+v"(g[0]), "+v"(g[1]), "+v"(g[2]), "+v"(g[3]), "+v"(g[4]), "+v"(g[5]), "+v"(g[6]), "+v"(g[7])::"memory")
#define BAD_Q() (tagbad(g[0], ta, tb) | tagbad(g[1], ta, tb) | tagbad(g[2], ta, tb) | tagbad(g[3], ta, tb) | tagbad(g[4], ta, tb) | tagbad(g[5], ta, tb) | tagbad(g[6], ta, tb) | tagbad(g[7], ta, tb))
    WAIT_Q();
    if (!wave_ok(BAD_Q())) {
      for (int it = 0; it < SPIN_CAP; ++it) {
        __builtin_amdgcn_s_sleep(2);
        GATHER_Q()
        WAIT_Q();
        STAMPV(30, it + 1);
        if (wave_ok(BAD_Q())) break;
      }
    }
    STAMP(7);
#pragma unroll
    for (int i = 0; i < 8; ++i) {
      const u32 gi = 64u * (i & 1) + lane, f = ((u32)(i >> 1) << 2) | (u32)(w & 3);
      *(LAS v2u*)(L + L_S4 + 1024u * (4u * w + (i >> 1)) + 16u * ((gi >> 1) ^ f) + 8u * (gi & 1u)) = v2u{g[i].x, g[i].z};
    }
    BAR();
    STAMP(8);
    dma_wblock((const char*)wuv + (size_t)qq_ * 128 * 8192 + hh_ * 256, 8192u, L_S2, lane, w);
    {
      f32x16 acc;
#pragma unroll
      for (int i = 0; i < 16; ++i) acc[i] = 0.f;
#pragma unroll
      for (int s = 0; s < 16; ++s) acc = mfma32(ka[s], lds128(L, L_S4 + ((rowo ^ (32u * (s & 7))) + 256u * (s >> 3))), acc);
      const u32 so = L_S0 + 16896u * ch + 528u * r31 + 128u * kt + 16u * hf;
#pragma unroll
      for (int a = 0; a < 4; ++a) *(LAS f32x4*)(L + so + 32u * a) = f32x4{acc[4 * a], acc[4 * a + 1], acc[4 * a + 2], acc[4 * a + 3]};
    }
    BAR();
    STAMP(9);
    constexpr u32 L_P = L_S1 + 16384;
    float m2, lsum;
    {
      const u32 hd = 4u * w + ((u32)lane >> 4), j = (u32)lane & 15u;
      const u32 so = L_S0 + 528u * hd + 32u * j;
      const f32x4 s0 = *(LAS f32x4*)(L + so) + *(LAS f32x4*)(L + so + 16896u);
      const f32x4 s1 = *(LAS f32x4*)(L + so + 16u) + *(LAS f32x4*)(L + so + 16896u + 16u);
      float m = fmaxf(fmaxf(fmaxf(s0[0], s0[1]), fmaxf(s0[2], s0[3])), fmaxf(fmaxf(s1[0], s1[1]), fmaxf(s1[2], s1[3])));
      m = fmaxf(m, dppf<XOR1>(m)); m = fmaxf(m, dppf<XOR2>(m)); m = fmaxf(m, dppf<ROR4>(m)); m = fmaxf(m, dppf<ROR8>(m));
      m2 = m * SC2;
      float p[8];
#pragma unroll
      for (int i = 0; i < 4; ++i) { p[i] = __builtin_amdgcn_exp2f(fmaf(s0[i], SC2, -m2)); p[4 + i] = __builtin_amdgcn_exp2f(fmaf(s1[i], SC2, -m2)); }
      *(LAS v4u*)(L + L_P + 256u * hd + 16u * (j ^ (hd & 15u))) = v4u{pk2(p[0], p[1]), pk2(p[2], p[3]), pk2(p[4], p[5]), pk2(p[6], p[7])};
      float l = ((p[0] + p[1]) + (p[2] + p[3])) + ((p[4] + p[5]) + (p[6] + p[7]));
      l += dppf<XOR1>(l); l += dppf<XOR2>(l); l += dppf<ROR4>(l); l += dppf<ROR8>(l);
      lsum = l;
    }
    BAR();
    STAMP(10);
    {
      f32x16 o0, o1;
#pragma unroll
      for (int i = 0; i < 16; ++i) { o0[i] = 0.f; o1[i] = 0.f; }
      const u32 po = L_P + 256u * r31 + 16u * (hf ^ (r31 & 15u));
#pragma unroll
      for (int s = 0; s < 8; ++s) {
        const v4u pf = lds128(L, po ^ (32u * s));
        o0 = mfma32(vt[s][0], pf, o0);
        o1 = mfma32(vt[s][1], pf, o1);
      }
      const u32 sb = L_S3 + lw + 128u * r31 + 8u * hf, sx = 16u * (r31 & 7u);
#pragma unroll
      for (int a = 0; a < 4; ++a) {
        *(LAS v2u*)(L + sb + ((16u * a) ^ sx)) = v2u{pk2(o0[4 * a], o0[4 * a + 1]), pk2(o0[4 * a + 2], o0[4 * a + 3])};
        *(LAS v2u*)(L + sb + ((64u + 16u * a) ^ sx)) = v2u{pk2(o1[4 * a], o1[4 * a + 1]), pk2(o1[4 * a + 2], o1[4 * a + 3])};
      }
      const u32 rr = (u32)lane >> 3, kk = (u32)lane & 7u;
      char* od = ws + WS_OA + (((u32)b * 32u * 4u + (u32)(w >> 1)) * 64u + sp) * 256u + (u32)(w & 1) * 128u;
      v4u t[4];
#pragma unroll
      for (int i = 0; i < 4; ++i) t[i] = lds128(L, L_S3 + lw + 1024u * i + 128u * rr + 16u * (kk ^ rr));
#pragma unroll
      for (int i = 0; i < 4; ++i) st_sc1(od, (8u * i + rr) * 65536u + 16u * kk, t[i]);
    }
    STAMP(11);
    WAITVM(0); BAR();
    STAMP(12);
    if ((lane & 15) == 0)
      st_sc1(ws + WS_MLA + (u32)b * 32768u + (u32)sp * 16u, (4u * w + ((u32)lane >> 4)) * 1024u, v4u{__float_as_uint(m2), ta, __float_as_uint(lsum), tb});
    v4u wa[4];
    read_wfrags(L, L_S2, lane, w, wa);
    constexpr u32 L_IMG = L_S4;
    {
      const u32 bb = (u32)w >> 2, cs = (u32)w & 3u;
      const u32 rec = ((u32)lane & 3u) | (((u32)lane >> 4) << 2), chk = ((u32)lane >> 2) & 3u;
      const char* fsrc = ws + WS_MLA + (bb * 32u + hh_) * 1024u;
      v4u f[4];
      for (int it = 0;; ++it) {
        ld_sc1<0>(f[0], fsrc, rec * 16u); ld_sc1<256>(f[1], fsrc, rec * 16u); ld_sc1<512>(f[2], fsrc, rec * 16u); ld_sc1<768>(f[3], fsrc, rec * 16u);
        asm volatile("s_waitcnt vmcnt(0)" : "+v"(f[0]), "+v"(f[1]), "+v"(f[2]), "+v"(f[3])::"memory");
        if (wave_ok(tagbad(f[0], ta, tb) | tagbad(f[1], ta, tb) | tagbad(f[2], ta, tb) | tagbad(f[3], ta, tb)) || it > SPIN_CAP) break;
        __builtin_amdgcn_s_sleep(1);
      }
      STAMP(13);
      const char* psrc = ws + WS_OA + ((bb * 32u + hh_) * 4u + qq_) * 16384u + cs * 64u;
      const u32 po = rec * 256u + chk * 16u;
      v4u d[4];
      ld_sc1<0>(d[0], psrc, po); ld_sc1<0>(d[1], psrc + 4096, po); ld_sc1<0>(d[2], psrc + 8192, po); ld_sc1<0>(d[3], psrc + 12288, po);
      dma_wblock((const char*)wo + (size_t)hh_ * 128 * 1024 + qq_ * 256, 1024u, L_S0, lane, w);
      float M = fmaxf(fmaxf(__uint_as_float(f[0].x), __uint_as_float(f[1].x)), fmaxf(__uint_as_float(f[2].x), __uint_as_float(f[3].x)));
      M = fmaxf(M, dppf<XOR1>(M)); M = fmaxf(M, dppf<XOR2>(M)); M = sw16_max(M); M = sw32_max(M);
      float e[4], Ls = 0.f;
#pragma unroll
      for (int i = 0; i < 4; ++i) { e[i] = __builtin_amdgcn_exp2f(__uint_as_float(f[i].x) - M); Ls = fmaf(e[i], __uint_as_float(f[i].z), Ls); }
      Ls += dppf<XOR1>(Ls); Ls += dppf<XOR2>(Ls); Ls = sw16_add(Ls, Ls); Ls = sw32_add(Ls, Ls);
      const float inv = __builtin_amdgcn_rcpf(Ls);
      asm volatile("s_waitcnt vmcnt(4)" : "+v"(d[0]), "+v"(d[1]), "+v"(d[2]), "+v"(d[3])::"memory");
      STAMP(14);
      float a[8];
#pragma unroll
      for (int k = 0; k < 8; ++k) a[k] = 0.f;
#pragma unroll
      for (int i = 0; i < 4; ++i) {
        const float wi = e[i] * inv;
#pragma unroll
        for (int k = 0; k < 4; ++k) { a[2 * k] = fmaf(wi, bflo(d[i][k]), a[2 * k]); a[2 * k + 1] = fmaf(wi, bfhi(d[i][k]), a[2 * k + 1]); }
      }
      const float r0 = sw32_add(a[0], a[4]), r1 = sw32_add(a[1], a[5]), r2 = sw32_add(a[2], a[6]), r3 = sw32_add(a[3], a[7]);
      float u0 = sw16_add(r0, r2), u1 = sw16_add(r1, r3);
      u0 += dppf<XOR1>(u0); u1 += dppf<XOR1>(u1); u0 += dppf<XOR2>(u0); u1 += dppf<XOR2>(u1);
      const u32 hi = pk2(u0, u1), lo = pk2(u0 - bflo(hi), u1 - bfhi(hi));
      const u32 cl = 32u * cs + 8u * chk + 4u * ((u32)lane >> 5) + 2u * (((u32)lane >> 4) & 1u);
      *(LAS u32*)(L + L_IMG + 272u * (bb + 2u * ((u32)lane & 1u)) + 2u * cl) = (lane & 1) ? lo : hi;
    }
    BAR();
    {
      v4u bf[4]; read_bfrags<3>(L, L_IMG, lane, bf);
      f32x4 d = mv4(wa, bf);
#pragma unroll
      for (int i = 0; i < 4; ++i) d[i] += dppf<XOR2>(d[i]);
      publish_d(ws + WS_CTX + ((u32)hh_ * 4u + qq_) * 2048u, d, lane, w, ta, tb);
    }
    STAMP(15);
    gather4(ws + WS_CTX + (u32)hh_ * 8192u, L, L_IMG + 2048u, lane, w, ta, tb);
    STAMP(16);
    BAR();
    {
      v4u a[4], bf[4];
      read_wfrags(L, L_S0, lane, w, a);
      read_bfrags<1>(L, L_IMG + 2048u, lane, bf);
      publish_d(ws + WS_OUTP + ((u32)qq_ * 32u + hh_) * 2048u, mv4(a, bf), lane, w, ta, tb);
    }
    STAMP(17);
    if (w < 2) {
      final_reduce(ws, out, 0, idx, lane, w, ta, tb);
      if (idx == 0 && w == 0 && lane == 0) __hip_atomic_store((GAS u32*)(ws + WS_E), e0 + 1u, __ATOMIC_RELAXED, __HIP_MEMORY_SCOPE_AGENT);
    }
    STAMP(18);
  } else {
    const u32 r4 = (u32)lane >> 4, c16 = (u32)lane & 15u;
    LOAD_EX(xB)
    {
      const u32 bs = (16u * w + r4) * 8192u, x = c16 ^ r4;
      dma4((const char*)wuk + (size_t)qq_ * 128 * 8192 + hh_ * 256, bs + 16u * x, bs + 32768u - 1024u + 16u * (x ^ 4u), bs + 65536u - 2048u + 16u * (x ^ 8u),
           bs + 98304u - 3072u + 16u * (x ^ 12u), L_S0 + 4096u * w);
    }
    asm volatile("s_waitcnt vmcnt(0)" : "+v"(ev), "+v"(xf[0]), "+v"(xf[1]), "+v"(xf[2]), "+v"(xf[3])::"memory");
    STAMP(1);
    e0 = __builtin_amdgcn_readfirstlane(ev);
    ta = e0 * 0x9E3779B1u + 0x7F4A7C15u; tb = (e0 ^ (e0 >> 15)) * 0x85EBCA77u + 0x3C6EF372u;
    constexpr u32 L_IMG = L_S1;
    v4u uk[4];
    {
      const u32 o = L_S0 + 4096u * w + 256u * c16 + 16u * (r4 ^ c16);
#pragma unroll
      for (int s = 0; s < 4; ++s) uk[s] = lds128(L, o ^ (64u * s));
    }
    gather4(ws + WS_QA + (u32)hh_ * 8192u, L, L_IMG, lane, w, ta, tb);
    STAMP(2);
    dma_wblock((const char*)wqB + (size_t)qq_ * 128 * 8192 + hh_ * 256, 8192u, L_S4, lane, w);
    BAR();
    {
      v4u bf[4]; read_bfrags<1>(L, L_IMG, lane, bf);
      const f32x4 d = mv4(uk, bf);
      if (c16 < 2u)
        st_sc1(ws + WS_QABS + (u32)hh_ * 2048u + (32u * qq_ + 4u * w) * 16u, c16 * 65536u + r4 * 16u, v4u{pk2(d[0], d[1]), ta, pk2(d[2], d[3]), tb});
    }
    STAMP(3);
    WAITVM(0); BAR();
    STAMP(4);
    {
      v4u a[4]; read_wfrags(L, L_S4, lane, w, a);
      publish_d(ws + WS_QB + ((u32)hh_ * 4u + qq_) * 2048u, mv4(a, xf), lane, w, ta, tb);
    }
    STAMP(5);
    const int b = idx >> 6, kvh = (idx >> 3) & 7, j = idx & 7;
    const size_t kvo = (((size_t)b * 8 + kvh) * 8192 + 6144 + 256u * j + 32u * w) * 256u;
    const u32 kb = L_S0 + 8192u * w, vb = L_S2 + 8192u * w;
    {
      const u32 x = c16 ^ r4, bs = 256u * r4;
      dma4((const char*)Kc + kvo, bs + 16u * x, bs + 16u * (x ^ 4u), bs + 16u * (x ^ 8u), bs + 16u * (x ^ 12u), kb);
      dma4((const char*)Kc + kvo + 4096, bs + 16u * x, bs + 16u * (x ^ 4u), bs + 16u * (x ^ 8u), bs + 16u * (x ^ 12u), kb + 4096u);
    }
    v4u g[2];
    const char* qsrc = ws + WS_QB + (4u * kvh + (u32)(w >> 1)) * 8192u + (u32)b * 1024u + (u32)(w & 1) * 512u;
    const u32 go = ((u32)lane & 3u) * 2048u + ((u32)lane >> 2) * 16u;
    ld_sc1<0>(g[0], qsrc, go); ld_sc1<256>(g[1], qsrc, go);
    {
      const u32 bs = 256u * r4 + 16u * (c16 & 1u), x = (c16 >> 1) ^ r4;
      dma4((const char*)Vc + kvo, bs + 32u * x, bs + 32u * (x ^ 4u), bs + 32u * x, bs + 32u * (x ^ 4u), vb);
      dma4((const char*)Vc + kvo + 4096, bs + 32u * x, bs + 32u * (x ^ 4u), bs + 32u * x, bs + 32u * (x ^ 4u), vb + 4096u);
    }
    asm volatile("s_waitcnt vmcnt(8)" : "+v"(g[0]), "+v"(g[1])::"memory");
    STAMP(6);
    v4u kf[2][4];
    {
      const u32 o = kb + 256u * c16 + 16u * (r4 ^ c16);
#pragma unroll
      for (int t = 0; t < 2; ++t)
#pragma unroll
        for (int s = 0; s < 4; ++s) kf[t][s] = lds128(L, (o ^ (64u * s)) + 4096u * t);
    }
    for (int it = 0;; ++it) {
      if (wave_ok(tagbad(g[0], ta, tb) | tagbad(g[1], ta, tb)) || it > SPIN_CAP) { STAMPV(30, it); break; }
      __builtin_amdgcn_s_sleep(1);
      ld_sc1<0>(g[0], qsrc, go); ld_sc1<256>(g[1], qsrc, go);
      asm volatile("s_waitcnt vmcnt(0)" : "+v"(g[0]), "+v"(g[1])::"memory");
    }
    constexpr u32 L_IMG2 = L_S4;
    BAR();
#pragma unroll
    for (int i = 0; i < 2; ++i) {
      float v0 = __uint_as_float(g[i].x), v1 = __uint_as_float(g[i].z);
      v0 += dppf<XOR1>(v0); v1 += dppf<XOR1>(v1); v0 += dppf<XOR2>(v0); v1 += dppf<XOR2>(v1);
      *(LAS u32*)(L + L_IMG2 + 272u * (u32)(w >> 1) + 4u * ((u32)(w & 1) * 32u + 16u * i + ((u32)lane >> 2))) = pk2(v0, v1);
    }
    BAR();
    STAMP(7);
    v4u pb; float m2, lsum;
    {
      v4u bf[4]; read_bfrags<3>(L, L_IMG2, lane, bf);
      const f32x4 s0 = mv4(kf[0], bf), s1 = mv4(kf[1], bf);
      float m = fmaxf(fmaxf(fmaxf(s0[0], s0[1]), fmaxf(s0[2], s0[3])), fmaxf(fmaxf(s1[0], s1[1]), fmaxf(s1[2], s1[3])));
      m = sw16_max(m); m = sw32_max(m);
      m2 = m * SC2;
      float p[8];
#pragma unroll
      for (int i = 0; i < 4; ++i) { p[i] = __builtin_amdgcn_exp2f(fmaf(s0[i], SC2, -m2)); p[4 + i] = __builtin_amdgcn_exp2f(fmaf(s1[i], SC2, -m2)); }
      pb = v4u{pk2(p[0], p[1]), pk2(p[2], p[3]), pk2(p[4], p[5]), pk2(p[6], p[7])};
      float l = ((p[0] + p[1]) + (p[2] + p[3])) + ((p[4] + p[5]) + (p[6] + p[7]));
      l = sw16_add(l, l); lsum = sw32_add(l, l);
    }
    BAR();
    dma_wblock((const char*)woB + (size_t)hh_ * 128 * 1024 + qq_ * 256, 1024u, L_S4, lane, w);
    WAITVM(4);
    STAMP(8);
    {
      const u32 q4 = c16 >> 2, p4 = (u32)lane & 3u, rw = 4u * r4 + q4;
      const u32 o = vb + 256u * rw + 32u * (rw & 7u) + 8u * p4;
#pragma unroll
      for (int c = 0; c < 8; ++c) {
        const v4u a = tr128(L, o ^ (32u * c), (o ^ (32u * c)) + 4096u);
        const f32x4 d = mfma16(a, pb, f32x4{0.f, 0.f, 0.f, 0.f});
        if (c16 < 4u) *(LAS f32x4*)(L + kb + 528u * c16 + 64u * c + 16u * r4) = d;
      }
      if (lane < 4) *(LAS f32x2*)(L + kb + 4096u + 8u * lane) = f32x2{m2, lsum};
    }
    BAR();
    STAMP(9);
    if (w < 4) {
      const f32x2 ml = *(LAS f32x2*)(L + L_S0 + 8192u * ((u32)lane & 7u) + 4096u + 8u * w);
      float M = ml[0];
      M = fmaxf(M, dppf<XOR1>(M)); M = fmaxf(M, dppf<XOR2>(M)); M = fmaxf(M, dppf<HMIR>(M));
      const float e = __builtin_amdgcn_exp2f(ml[0] - M);
      float Ls = e * ml[1];
      Ls += dppf<XOR1>(Ls); Ls += dppf<XOR2>(Ls); Ls += dppf<HMIR>(Ls);
      float o0 = 0.f, o1 = 0.f;
#pragma unroll
      for (int v = 0; v < 8; ++v) {
        const float ev_ = __builtin_bit_cast(float, __builtin_amdgcn_readlane(__builtin_bit_cast(int, e), v));
        const f32x2 ov = *(LAS f32x2*)(L + L_S0 + 8192u * v + 528u * w + 8u * lane);
        o0 = fmaf(ev_, ov[0], o0); o1 = fmaf(ev_, ov[1], o1);
      }
      const u32 rec = ((u32)b * 32u + 4u * kvh + w) * 8u + j;
      st_sc1(ws + WS_OB + rec * 1024u, 16u * lane, v4u{__float_as_uint(o0), ta, __float_as_uint(o1), tb});
      if (lane == 0) st_sc1(ws + WS_MLB + rec * 16u, 0u, v4u{__float_as_uint(M), ta, __float_as_uint(Ls), tb});
    }
    STAMP(10);
    constexpr u32 L_IMG3 = L_S2;
    {
      const u32 bb = (u32)w >> 2, jj = ((u32)lane & 3u) | (((u32)lane >> 5) << 2), gl = ((u32)lane >> 2) & 7u;
      const char* osrc = ws + WS_OB + (bb * 32u + hh_) * 8192u + (u32)(w & 3) * 256u;
      const char* msrc = ws + WS_MLB + (bb * 32u + hh_) * 128u;
      v4u d0, d1, f;
      for (int it = 0;; ++it) {
        ld_sc1<0>(d0, osrc, jj * 1024u + gl * 16u); ld_sc1<128>(d1, osrc, jj * 1024u + gl * 16u); ld_sc1<0>(f, msrc, jj * 16u);
        asm volatile("s_waitcnt vmcnt(0)" : "+v"(d0), "+v"(d1), "+v"(f)::"memory");
        if (wave_ok(tagbad(d0, ta, tb) | tagbad(d1, ta, tb) | tagbad(f, ta, tb)) || it > SPIN_CAP) break;
        __builtin_amdgcn_s_sleep(1);
      }
      STAMP(11);
      float M = __uint_as_float(f.x);
      M = fmaxf(M, dppf<XOR1>(M)); M = fmaxf(M, dppf<XOR2>(M)); M = sw32_max(M);
      const float e = __builtin_amdgcn_exp2f(__uint_as_float(f.x) - M);
      float Ls = e * __uint_as_float(f.z);
      Ls += dppf<XOR1>(Ls); Ls += dppf<XOR2>(Ls); Ls = sw32_add(Ls, Ls);
      const float wi = e * __builtin_amdgcn_rcpf(Ls);
      float v0 = sw32_add(wi * __uint_as_float(d0.x), wi * __uint_as_float(d1.x));
      float v1 = sw32_add(wi * __uint_as_float(d0.z), wi * __uint_as_float(d1.z));
      v0 += dppf<XOR1>(v0); v1 += dppf<XOR1>(v1); v0 += dppf<XOR2>(v0); v1 += dppf<XOR2>(v1);
      *(LAS u32*)(L + L_IMG3 + 272u * bb + 4u * ((u32)(w & 3) * 16u + ((u32)lane >> 5) * 8u + gl)) = pk2(v0, v1);
    }
    BAR();
    {
      v4u a[4], bf[4];
      read_wfrags(L, L_S4, lane, w, a);
      read_bfrags<1>(L, L_IMG3, lane, bf);
      publish_d(ws + WS_OUTP + ((4u + qq_) * 32u + hh_) * 2048u, mv4(a, bf), lane, w, ta, tb);
    }
    STAMP(17);
    if (w < 2) final_reduce(ws, out, 1, idx, lane, w, ta, tb);
    STAMP(18);
  }
}

template<int NT,int LDS,int BIGV>
__global__ void __launch_bounds__(NT) spin_k(char* ws, int dur) {
  __shared__ char sm2[LDS?LDS:16];
  u64 t0; asm volatile("s_memrealtime %0\n\ts_waitcnt lgkmcnt(0)" : "=s"(t0)::"memory");
  if (dur < 0) { sm2[threadIdx.x] = 1; __syncthreads(); ws[threadIdx.x] = sm2[threadIdx.x ^ 1]; }
  if (BIGV) asm volatile("v_mov_b32 v200, 0" ::: "v200");
  for (;;) { u64 t_; asm volatile("s_memrealtime %0\n\ts_waitcnt lgkmcnt(0)" : "=s"(t_)::"memory"); if ((u32)t_ - (u32)t0 >= (u32)dur) break; __builtin_amdgcn_s_sleep(1); }
}
extern "C" void kernel_launch(void* const* d_in, const int* in_sizes, int n_in, void* d_out, int out_size, void* d_ws, size_t ws_size,
                              hipStream_t stream) {
  fused_decode<<<256, 512, 0, stream>>>((const u16*)d_in[0], (const u16*)d_in[1], (const u16*)d_in[2], (const u16*)d_in[3], (const u16*)d_in[4],
                                        (const u16*)d_in[5], (const u16*)d_in[6], (const u16*)d_in[7], (const u16*)d_in[8], (const u16*)d_in[9],
                                        (const u16*)d_in[10], (u16*)d_out, (char*)d_ws);
}
```

```cpp
#define CAL_STAMP 98
#define CAL_ROLE 0
#include <hip/hip_runtime.h>
#include <stdint.h>

typedef unsigned u32;
typedef unsigned long long u64;
typedef unsigned short u16;
typedef u32 v4u __attribute__((ext_vector_type(4)));
typedef u32 v2u __attribute__((ext_vector_type(2)));
typedef float f32x2 __attribute__((ext_vector_type(2)));
typedef float f32x4 __attribute__((ext_vector_type(4)));
typedef float f32x16 __attribute__((ext_vector_type(16)));
typedef __bf16 bf16x8 __attribute__((ext_vector_type(8)));
typedef __bf16 bf16x2 __attribute__((ext_vector_type(2)));
typedef short s16x4 __attribute__((ext_vector_type(4)));
#define GAS __attribute__((address_space(1)))
#define LAS __attribute__((address_space(3)))
#define DI __device__ __forceinline__

constexpr u32 WS_E = 0;
constexpr u32 WS_QA = 4096;
constexpr u32 WS_QB = WS_QA + 262144;
constexpr u32 WS_QABS = WS_QB + 262144;
constexpr u32 WS_MLA = WS_QABS + 131072;
constexpr u32 WS_CTX = WS_MLA + 65536;
constexpr u32 WS_OB = WS_CTX + 262144;
constexpr u32 WS_MLB = WS_OB + 524288;
constexpr u32 WS_OUTP = WS_MLB + 8192;
constexpr u32 WS_OA = 2u << 20;
constexpr u32 WS_DBG = 8u << 20;
static_assert(WS_OUTP + 524288 <= WS_OA, "ws map");

constexpr float SC2 = 0.08838834764831845f * 1.4426950408889634f;
constexpr int SPIN_CAP = 1 << 17;

constexpr u32 L_S0 = 0, L_S1 = 32768, L_S2 = 65536, L_S3 = 98304, L_S4 = 131072;

#define STAMP(k) do { if ((k) == 0 || (k) == CAL_STAMP) { u64 t_; asm volatile("s_memrealtime %0\n\ts_waitcnt lgkmcnt(0)" : "=s"(t_)::"memory"); if ((k) == CAL_STAMP && CAL_ROLE == roleB) calx = (u32)t_ - cal0; } } while (0)
#define STAMPV(k, v)

#define WAITVM(n) asm volatile("s_waitcnt vmcnt(%0)" ::"i"(n) : "memory")
#define BAR() asm volatile("s_waitcnt lgkmcnt(0)\n\ts_barrier" ::: "memory")

template <int CTRL> DI float dppf(float x) {
  return __builtin_bit_cast(float, __builtin_amdgcn_mov_dpp(__builtin_bit_cast(int, x), CTRL, 0xf, 0xf, true));
}
constexpr int XOR1 = 0xB1, XOR2 = 0x4E, HMIR = 0x141, ROR4 = 0x124, ROR8 = 0x128;
DI float sw32_add(float a, float b) { auto s = __builtin_amdgcn_permlane32_swap(__float_as_uint(a), __float_as_uint(b), false, false); return __uint_as_float(s[0]) + __uint_as_float(s[1]); }
DI float sw16_add(float a, float b) { auto s = __builtin_amdgcn_permlane16_swap(__float_as_uint(a), __float_as_uint(b), false, false); return __uint_as_float(s[0]) + __uint_as_float(s[1]); }
DI float sw32_max(float a) { auto s = __builtin_amdgcn_permlane32_swap(__float_as_uint(a), __float_as_uint(a), false, false); return fmaxf(__uint_as_float(s[0]), __uint_as_float(s[1])); }
DI float sw16_max(float a) { auto s = __builtin_amdgcn_permlane16_swap(__float_as_uint(a), __float_as_uint(a), false, false); return fmaxf(__uint_as_float(s[0]), __uint_as_float(s[1])); }
DI u32 pk2(float lo, float hi) { f32x2 p = {lo, hi}; return __builtin_bit_cast(u32, __builtin_convertvector(p, bf16x2)); }
DI float bflo(u32 x) { return __uint_as_float(x << 16); }
DI float bfhi(u32 x) { return __uint_as_float(x & 0xffff0000u); }
DI f32x4 mfma16(v4u a, v4u b, f32x4 c) { return __builtin_amdgcn_mfma_f32_16x16x32_bf16(__builtin_bit_cast(bf16x8, a), __builtin_bit_cast(bf16x8, b), c, 0, 0, 0); }
DI f32x16 mfma32(v4u a, v4u b, f32x16 c) { return __builtin_amdgcn_mfma_f32_32x32x16_bf16(__builtin_bit_cast(bf16x8, a), __builtin_bit_cast(bf16x8, b), c, 0, 0, 0); }

DI v4u lds128(LAS char* L, u32 off) { return *(LAS v4u*)(L + off); }
DI v2u tr64(LAS char* L, u32 off) { return __builtin_bit_cast(v2u, __builtin_amdgcn_ds_read_tr16_b64_v4i16((LAS s16x4*)(L + off))); }
DI v4u tr128(LAS char* L, u32 o0, u32 o1) { v2u a = tr64(L, o0), b = tr64(L, o1); return v4u{a.x, a.y, b.x, b.y}; }

DI void dma4(const void* sbase, u32 v0, u32 v1, u32 v2, u32 v3, u32 lds) {
  asm volatile("s_mov_b32 m0, %5\n\ts_nop 0\n\t"
               "global_load_lds_dwordx4 %0, %4 nt\n\t"
               "global_load_lds_dwordx4 %1, %4 offset:1024 nt\n\t"
               "global_load_lds_dwordx4 %2, %4 offset:2048 nt\n\t"
               "global_load_lds_dwordx4 %3, %4 offset:3072 nt"
               ::"v"(v0), "v"(v1), "v"(v2), "v"(v3), "s"(sbase), "s"(lds) : "memory");
}
DI void dma2a(const void* sbase, u32 v0, u32 v1, u32 lds) {
  asm volatile("s_mov_b32 m0, %3\n\ts_nop 0\n\t"
               "global_load_lds_dwordx4 %0, %2 nt\n\t"
               "global_load_lds_dwordx4 %1, %2 offset:1024 nt"
               ::"v"(v0), "v"(v1), "s"(sbase), "s"(lds) : "memory");
}
DI void dma2b(const void* sbase, u32 v2, u32 v3, u32 lds) {
  asm volatile("s_mov_b32 m0, %3\n\ts_nop 0\n\t"
               "global_load_lds_dwordx4 %0, %2 offset:2048 nt\n\t"
               "global_load_lds_dwordx4 %1, %2 offset:3072 nt"
               ::"v"(v2), "v"(v3), "s"(sbase), "s"(lds) : "memory");
}
template <int OFF> DI void ld_sc1(v4u& d, const void* sbase, u32 voff) {
  asm volatile("global_load_dwordx4 %0, %1, %2 offset:%3 sc1" : "=v"(d) : "v"(voff), "s"(sbase), "i"(OFF) : "memory");
}
template <int OFF> DI void ld_plain(v4u& d, const void* sbase, u32 voff) {
  asm volatile("global_load_dwordx4 %0, %1, %2 offset:%3" : "=v"(d) : "v"(voff), "s"(sbase), "i"(OFF) : "memory");
}
DI void st_sc1(void* sbase, u32 voff, v4u d) {
  asm volatile("global_store_dwordx4 %0, %1, %2 sc1\n\ts_nop 1" ::"v"(voff), "v"(d), "s"(sbase) : "memory");
}
#define PIN1(a) asm volatile("" : "+v"(a))
DI u32 tagbad(const v4u& g, u32 ta, u32 tb) { return (g.y ^ ta) | (g.w ^ tb); }
DI bool wave_ok(u32 bad) { return __builtin_amdgcn_ballot_w64(bad != 0u) == 0ull; }

struct Ctx { LAS char* L; char* ws; int lane, w; u32 ta, tb; };

DI void dma_wblock(const char* blk, u32 rstride, u32 lds, int lane, int w) {
  const u32 r = (u32)lane >> 4, c = (u32)lane & 15u;
  const u32 b = (16u * w + r) * rstride;
  const u32 x = c ^ (r << 2);
  dma4(blk, b + 16u * x, b + 4u * rstride - 1024u + 16u * (x ^ 1u), b + 8u * rstride - 2048u + 16u * (x ^ 2u), b + 12u * rstride - 3072u + 16u * (x ^ 3u),
       lds + 4096u * w);
}
DI void read_wfrags(LAS char* L, u32 lds, int lane, int w, v4u (&a)[4]) {
  const u32 g = (u32)lane >> 4, q = ((u32)lane & 15u) >> 2, p = (u32)lane & 3u;
  const u32 ch = 2u * w + (p >> 1);
  const u32 o0 = lds + 256u * (8u * g + q) + 16u * (ch ^ ((q << 2) | ((2u * g) & 3u))) + 8u * (p & 1u);
  const u32 o1 = lds + 256u * (8u * g + 4u + q) + 16u * (ch ^ ((q << 2) | ((2u * g + 1u) & 3u))) + 8u * (p & 1u);
#pragma unroll
  for (int ks = 0; ks < 4; ++ks) a[ks] = tr128(L, o0 + 8192u * ks, o1 + 8192u * ks);
}
template <int MASK> DI void read_bfrags(LAS char* L, u32 img, int lane, v4u (&b)[4]) {
  const u32 o = img + 272u * ((u32)lane & MASK) + 16u * ((u32)lane >> 4);
#pragma unroll
  for (int s = 0; s < 4; ++s) b[s] = lds128(L, o + 64u * s);
}
DI void publish_d(void* dst, f32x4 d, int lane, int w, u32 ta, u32 tb) {
  const u32 col = (u32)lane & 15u, g = (u32)lane >> 4, pr = (col >> 1) & 1u;
  const float v0 = pr ? d[2] : d[0], v1 = pr ? d[3] : d[1];
  if (col < 4u) st_sc1(dst, (col & 1u) * 1024u + (8u * w + 2u * g + pr) * 16u, v4u{__float_as_uint(v0), ta, __float_as_uint(v1), tb});
}
DI f32x4 mv4(const v4u (&a)[4], const v4u (&b)[4]) {
  f32x4 d = {0.f, 0.f, 0.f, 0.f};
#pragma unroll
  for (int s = 0; s < 4; ++s) d = mfma16(a[s], b[s], d);
  return d;
}
DI void gather4(const char* src, LAS char* L, u32 img, int lane, int w, u32 ta, u32 tb) {
  const u32 gi = (16u * w + ((u32)lane >> 2)) & 63u, b = (u32)w >> 2;
  const u32 off = ((u32)lane & 3u) * 2048u + b * 1024u + gi * 16u;
  v4u g;
  for (int it = 0;; ++it) {
    ld_sc1<0>(g, src, off);
    asm volatile("s_waitcnt vmcnt(0)" : "+v"(g)::"memory");
    if (wave_ok(tagbad(g, ta, tb)) || it > SPIN_CAP) break;
    __builtin_amdgcn_s_sleep(1);
  }
  float v0 = __uint_as_float(g.x), v1 = __uint_as_float(g.z);
  v0 += dppf<XOR1>(v0); v1 += dppf<XOR1>(v1);
  v0 += dppf<XOR2>(v0); v1 += dppf<XOR2>(v1);
  *(LAS u32*)(L + img + 272u * b + 4u * gi) = pk2(v0, v1);
}
DI void final_reduce(const char* ws, u16* out, int which, int idx, int lane, int w, u32 ta, u32 tb) {
  const u32 mq = (u32)idx >> 5, sub = (u32)idx & 31u;
  const char* src = ws + WS_OUTP + (which * 4u + mq) * 65536u;
  const u32 off = ((u32)lane >> 1) * 2048u + (u32)w * 1024u + (2u * sub + ((u32)lane & 1u)) * 16u;
  v4u g;
  for (int it = 0;; ++it) {
    ld_sc1<0>(g, src, off);
    asm volatile("s_waitcnt vmcnt(0)" : "+v"(g)::"memory");
    if (wave_ok(tagbad(g, ta, tb)) || it > SPIN_CAP) break;
    __builtin_amdgcn_s_sleep(1);
  }
  float v0 = __uint_as_float(g.x), v1 = __uint_as_float(g.z);
  v0 += dppf<XOR2>(v0); v1 += dppf<XOR2>(v1);
  v0 += dppf<ROR4>(v0); v1 += dppf<ROR4>(v1);
  v0 += dppf<ROR8>(v0); v1 += dppf<ROR8>(v1);
  v0 = sw16_add(v0, v0); v1 = sw16_add(v1, v1);
  v0 = sw32_add(v0, v0); v1 = sw32_add(v1, v1);
  if (lane < 2) *(GAS u32*)((GAS char*)out + ((u32)w * 1024u + which * 512u + 128u * mq + 4u * sub + 2u * lane) * 2u) = pk2(v0, v1);
}

typedef u32 v16u __attribute__((ext_vector_type(16)));
typedef u32 v8u __attribute__((ext_vector_type(8)));
DI const u16* mkp(u32 lo, u32 hi) { return (const u16*)(((u64)hi << 32) | lo); }
__global__ void __launch_bounds__(512, 2)
fused_decode(const u16* p0, const u16* p1, const u16* p2, const u16* p3, const u16* p4, const u16* p5, const u16* p6, const u16* p7,
             const u16* p8, const u16* p9, const u16* p10, u16* p11, char* p12) {
  u64 tt0; asm volatile("s_memrealtime %0\n\ts_waitcnt lgkmcnt(0)" : "=s"(tt0)::"memory");
  v16u ka0; v8u ka1; v2u ka2;
  asm volatile("s_load_dwordx16 %0, %3, 0x0\n\ts_load_dwordx8 %1, %3, 0x40\n\ts_load_dwordx2 %2, %3, 0x60\n\ts_waitcnt lgkmcnt(0)"
               : "=&s"(ka0), "=&s"(ka1), "=&s"(ka2) : "s"(__builtin_amdgcn_kernarg_segment_ptr()) : "memory");
  const u16 *xA = mkp(ka0[0], ka0[1]), *ckv = mkp(ka0[2], ka0[3]), *wq = mkp(ka0[4], ka0[5]), *wuk = mkp(ka0[6], ka0[7]);
  const u16 *wuv = mkp(ka0[8], ka0[9]), *wo = mkp(ka0[10], ka0[11]), *xB = mkp(ka0[12], ka0[13]), *Kc = mkp(ka0[14], ka0[15]);
  const u16 *Vc = mkp(ka1[0], ka1[1]), *wqB = mkp(ka1[2], ka1[3]), *woB = mkp(ka1[4], ka1[5]);
  u16* out = (u16*)mkp(ka1[6], ka1[7]); char* ws = (char*)mkp(ka2[0], ka2[1]);
  __shared__ __attribute__((aligned(16))) char smem[163840];
  LAS char* L = (LAS char*)smem;
  const int lane = threadIdx.x & 63, w = __builtin_amdgcn_readfirstlane(threadIdx.x >> 6);
  const int bid = blockIdx.x, roleB = (bid >> 3) & 1, idx = ((bid >> 4) << 3) | (bid & 7);
  const int hh_ = (idx & 7) + 8 * (idx >> 5), qq_ = (idx >> 3) & 3;
  u32 cal0 = (u32)tt0, calx = 0; u64 c0_; asm volatile("s_memtime %0\n\ts_waitcnt lgkmcnt(0)" : "=s"(c0_)::"memory");
  STAMP(0);
  {
    u64 pc; asm volatile("s_getpc_b64 %0" : "=s"(pc));
    pc &= ~(u64)0x7f;
    if (w >= 5) {
      asm volatile("s_mov_b32 m0, %2\n\ts_nop 0\n\tglobal_load_lds_dword %0, %1"
                   ::"v"((u32)lane * 128u + (u32)(w - 5) * 8192u), "s"(pc), "s"(L_S3 + 4096u * (u32)w) : "memory");
    } else {
      asm volatile("s_mov_b32 m0, %2\n\ts_nop 0\n\tglobal_load_lds_dword %0, %1" ::"v"(0u), "s"(pc), "s"(L_S3 + 4096u * (u32)w) : "memory");
    }
  }
  const u32 xo = ((u32)lane & 1u) * 1024u + (u32)qq_ * 256u + ((u32)lane >> 4) * 16u;
#define LOAD_EX(x) \
  u32 ev; v4u xf[4]; \
  asm volatile("global_load_dword %0, %1, %2 sc1" : "=v"(ev) : "v"(0u), "s"(ws) : "memory"); \
  ld_plain<0>(xf[0], x, xo); ld_plain<64>(xf[1], x, xo); ld_plain<128>(xf[2], x, xo); ld_plain<192>(xf[3], x, xo);
  asm volatile("" ::"s"(xA), "s"(ckv), "s"(wq), "s"(wuk), "s"(wuv), "s"(wo), "s"(xB), "s"(Kc), "s"(Vc), "s"(wqB), "s"(woB), "s"(out), "s"(ws));
  u32 ta, tb, e0;

  if (!roleB) {
    const int b = (idx & 7) >> 2, sp = (idx >> 3) * 4 + (idx & 3);
    const char* kv = (const char*)ckv + ((size_t)b * 8192 + (size_t)sp * 128 + 4u * w) * 1024u;
    const u32 kx = (16u * lane) ^ (16u * (w & 3));
    const u32 k0 = kx, k1 = kx ^ 64u, k2 = kx ^ 128u, k3 = kx ^ 192u;
    const u32 lw = 4096u * w;
    LOAD_EX(xA)
    dma_wblock((const char*)wq + (size_t)qq_ * 128 * 8192 + hh_ * 256, 8192u, L_S4, lane, w);
    dma2a(kv, k0, k1, L_S0 + lw);
    asm volatile("s_waitcnt vmcnt(2)" : "+v"(ev), "+v"(xf[0]), "+v"(xf[1]), "+v"(xf[2]), "+v"(xf[3])::"memory");
    STAMP(1);
    dma2b(kv, k2, k3, L_S0 + lw);
    e0 = __builtin_amdgcn_readfirstlane(ev);
    ta = e0 * 0x9E3779B1u + 0x7F4A7C15u; tb = (e0 ^ (e0 >> 15)) * 0x85EBCA77u + 0x3C6EF372u;
    BAR();
    {
      v4u a[4]; read_wfrags(L, L_S4, lane, w, a);
      f32x4 d = mv4(a, xf);
      publish_d(ws + WS_QA + ((u32)hh_ * 4u + qq_) * 2048u, d, lane, w, ta, tb);
    }
    STAMP(2);
    dma4(kv + 32768, k0, k1, k2, k3, L_S1 + lw);
    dma4(kv + 65536, k0, k1, k2, k3, L_S2 + lw);
    const int kt = w & 3, ch = w >> 2;
    v4u ka[16], vt[8][2];
    const u32 r31 = (u32)lane & 31u, hf = (u32)lane >> 5;
    const u32 fr = ((r31 & 3u) << 2) | ((r31 >> 2) & 3u);
    const u32 rowo = 1024u * r31 + 16u * (hf ^ fr) + 512u * ch;
    const u32 blk = ((u32)lane >> 4) & 1u, q4 = ((u32)lane & 15u) >> 2, p4 = (u32)lane & 3u;
    const u32 chx = 8u * w + 2u * blk + (p4 >> 1);
    const u32 tro0 = 1024u * (8u * hf + q4) + 16u * (chx ^ ((q4 << 2) | (2u * hf))) + 8u * (p4 & 1u);
    const u32 tro1 = 1024u * (8u * hf + 4u + q4) + 16u * (chx ^ ((q4 << 2) | (2u * hf + 1u))) + 8u * (p4 & 1u);
#define READ_TILE(t)                                                                                          \
    {                                                                                                         \
      if (kt == (t)) {                                                                                        \
        _Pragma("unroll") for (int s = 0; s < 16; ++s) ka[s] = lds128(L, 32768u * (t) + ((rowo ^ (32u * (s & 7))) + 256u * (s >> 3))); \
      }                                                                                                       \
      _Pragma("unroll") for (int ks = 0; ks < 2; ++ks)                                                        \
        _Pragma("unroll") for (int u = 0; u < 2; ++u)                                                         \
          vt[2 * (t) + ks][u] = tr128(L, 32768u * (t) + 16384u * ks + (tro0 ^ (64u * u)), 32768u * (t) + 16384u * ks + (tro1 ^ (64u * u))); \
    }
#pragma unroll
    for (int s = 0; s < 16; ++s) ka[s] = v4u{0u, 0u, 0u, 0u};
    WAITVM(8); BAR(); STAMP(3);
    READ_TILE(0)
    dma4(kv + 98304, k0, k1, k2, k3, L_S3 + lw);
    WAITVM(8); BAR(); STAMP(4);
    READ_TILE(1)
    v4u g[8];
    const char* qsrc = ws + WS_QABS + (u32)b * 65536u + (u32)w * 8192u;
    const u32 go = 16u * lane;
#define GATHER_Q() { ld_sc1<0>(g[0], qsrc, go); ld_sc1<1024>(g[1], qsrc, go); ld_sc1<2048>(g[2], qsrc, go); ld_sc1<3072>(g[3], qsrc, go); \
                     ld_sc1<0>(g[4], qsrc + 4096, go); ld_sc1<1024>(g[5], qsrc + 4096, go); ld_sc1<2048>(g[6], qsrc + 4096, go); ld_sc1<3072>(g[7], qsrc + 4096, go); }
    GATHER_Q()
    WAITVM(12); BAR(); STAMP(5);
    READ_TILE(2)
    WAITVM(8); BAR(); STAMP(6);
    READ_TILE(3)
#define WAIT_Q() asm volatile("s_waitcnt vmcnt(0)" : "+v"(g[0]), "+v"(g[1]), "+v"(g[2]), "+v"(g[3]), "+v"(g[4]), "+v"(g[5]), "+v"(g[6]), "+v"(g[7])::"memory")
#define BAD_Q() (tagbad(g[0], ta, tb) | tagbad(g[1], ta, tb) | tagbad(g[2], ta, tb) | tagbad(g[3], ta, tb) | tagbad(g[4], ta, tb) | tagbad(g[5], ta, tb) | tagbad(g[6], ta, tb) | tagbad(g[7], ta, tb))
    WAIT_Q();
    if (!wave_ok(BAD_Q())) {
      for (int it = 0; it < SPIN_CAP; ++it) {
        __builtin_amdgcn_s_sleep(2);
        GATHER_Q()
        WAIT_Q();
        STAMPV(30, it + 1);
        if (wave_ok(BAD_Q())) break;
      }
    }
    STAMP(7);
#pragma unroll
    for (int i = 0; i < 8; ++i) {
      const u32 gi = 64u * (i & 1) + lane, f = ((u32)(i >> 1) << 2) | (u32)(w & 3);
      *(LAS v2u*)(L + L_S4 + 1024u * (4u * w + (i >> 1)) + 16u * ((gi >> 1) ^ f) + 8u * (gi & 1u)) = v2u{g[i].x, g[i].z};
    }
    BAR();
    STAMP(8);
    dma_wblock((const char*)wuv + (size_t)qq_ * 128 * 8192 + hh_ * 256, 8192u, L_S2, lane, w);
    {
      f32x16 acc;
#pragma unroll
      for (int i = 0; i < 16; ++i) acc[i] = 0.f;
#pragma unroll
      for (int s = 0; s < 16; ++s) acc = mfma32(ka[s], lds128(L, L_S4 + ((rowo ^ (32u * (s & 7))) + 256u * (s >> 3))), acc);
      const u32 so = L_S0 + 16896u * ch + 528u * r31 + 128u * kt + 16u * hf;
#pragma unroll
      for (int a = 0; a < 4; ++a) *(LAS f32x4*)(L + so + 32u * a) = f32x4{acc[4 * a], acc[4 * a + 1], acc[4 * a + 2], acc[4 * a + 3]};
    }
    BAR();
    STAMP(9);
    constexpr u32 L_P = L_S1 + 16384;
    float m2, lsum;
    {
      const u32 hd = 4u * w + ((u32)lane >> 4), j = (u32)lane & 15u;
      const u32 so = L_S0 + 528u * hd + 32u * j;
      const f32x4 s0 = *(LAS f32x4*)(L + so) + *(LAS f32x4*)(L + so + 16896u);
      const f32x4 s1 = *(LAS f32x4*)(L + so + 16u) + *(LAS f32x4*)(L + so + 16896u + 16u);
      float m = fmaxf(fmaxf(fmaxf(s0[0], s0[1]), fmaxf(s0[2], s0[3])), fmaxf(fmaxf(s1[0], s1[1]), fmaxf(s1[2], s1[3])));
      m = fmaxf(m, dppf<XOR1>(m)); m = fmaxf(m, dppf<XOR2>(m)); m = fmaxf(m, dppf<ROR4>(m)); m = fmaxf(m, dppf<ROR8>(m));
      m2 = m * SC2;
      float p[8];
#pragma unroll
      for (int i = 0; i < 4; ++i) { p[i] = __builtin_amdgcn_exp2f(fmaf(s0[i], SC2, -m2)); p[4 + i] = __builtin_amdgcn_exp2f(fmaf(s1[i], SC2, -m2)); }
      *(LAS v4u*)(L + L_P + 256u * hd + 16u * (j ^ (hd & 15u))) = v4u{pk2(p[0], p[1]), pk2(p[2], p[3]), pk2(p[4], p[5]), pk2(p[6], p[7])};
      float l = ((p[0] + p[1]) + (p[2] + p[3])) + ((p[4] + p[5]) + (p[6] + p[7]));
      l += dppf<XOR1>(l); l += dppf<XOR2>(l); l += dppf<ROR4>(l); l += dppf<ROR8>(l);
      lsum = l;
    }
    BAR();
    STAMP(10);
    {
      f32x16 o0, o1;
#pragma unroll
      for (int i = 0; i < 16; ++i) { o0[i] = 0.f; o1[i] = 0.f; }
      const u32 po = L_P + 256u * r31 + 16u * (hf ^ (r31 & 15u));
#pragma unroll
      for (int s = 0; s < 8; ++s) {
        const v4u pf = lds128(L, po ^ (32u * s));
        o0 = mfma32(vt[s][0], pf, o0);
        o1 = mfma32(vt[s][1], pf, o1);
      }
      const u32 sb = L_S3 + lw + 128u * r31 + 8u * hf, sx = 16u * (r31 & 7u);
#pragma unroll
      for (int a = 0; a < 4; ++a) {
        *(LAS v2u*)(L + sb + ((16u * a) ^ sx)) = v2u{pk2(o0[4 * a], o0[4 * a + 1]), pk2(o0[4 * a + 2], o0[4 * a + 3])};
        *(LAS v2u*)(L + sb + ((64u + 16u * a) ^ sx)) = v2u{pk2(o1[4 * a], o1[4 * a + 1]), pk2(o1[4 * a + 2], o1[4 * a + 3])};
      }
      const u32 rr = (u32)lane >> 3, kk = (u32)lane & 7u;
      char* od = ws + WS_OA + (((u32)b * 32u * 4u + (u32)(w >> 1)) * 64u + sp) * 256u + (u32)(w & 1) * 128u;
      v4u t[4];
#pragma unroll
      for (int i = 0; i < 4; ++i) t[i] = lds128(L, L_S3 + lw + 1024u * i + 128u * rr + 16u * (kk ^ rr));
#pragma unroll
      for (int i = 0; i < 4; ++i) st_sc1(od, (8u * i + rr) * 65536u + 16u * kk, t[i]);
    }
    STAMP(11);
    WAITVM(0); BAR();
    STAMP(12);
    if ((lane & 15) == 0)
      st_sc1(ws + WS_MLA + (u32)b * 32768u + (u32)sp * 16u, (4u * w + ((u32)lane >> 4)) * 1024u, v4u{__float_as_uint(m2), ta, __float_as_uint(lsum), tb});
    v4u wa[4];
    read_wfrags(L, L_S2, lane, w, wa);
    constexpr u32 L_IMG = L_S4;
    {
      const u32 bb = (u32)w >> 2, cs = (u32)w & 3u;
      const u32 rec = ((u32)lane & 3u) | (((u32)lane >> 4) << 2), chk = ((u32)lane >> 2) & 3u;
      const char* fsrc = ws + WS_MLA + (bb * 32u + hh_) * 1024u;
      v4u f[4];
      for (int it = 0;; ++it) {
        ld_sc1<0>(f[0], fsrc, rec * 16u); ld_sc1<256>(f[1], fsrc, rec * 16u); ld_sc1<512>(f[2], fsrc, rec * 16u); ld_sc1<768>(f[3], fsrc, rec * 16u);
        asm volatile("s_waitcnt vmcnt(0)" : "+v"(f[0]), "+v"(f[1]), "+v"(f[2]), "+v"(f[3])::"memory");
        if (wave_ok(tagbad(f[0], ta, tb) | tagbad(f[1], ta, tb) | tagbad(f[2], ta, tb) | tagbad(f[3], ta, tb)) || it > SPIN_CAP) break;
        __builtin_amdgcn_s_sleep(1);
      }
      STAMP(13);
      const char* psrc = ws + WS_OA + ((bb * 32u + hh_) * 4u + qq_) * 16384u + cs * 64u;
      const u32 po = rec * 256u + chk * 16u;
      v4u d[4];
      ld_sc1<0>(d[0], psrc, po); ld_sc1<0>(d[1], psrc + 4096, po); ld_sc1<0>(d[2], psrc + 8192, po); ld_sc1<0>(d[3], psrc + 12288, po);
      dma_wblock((const char*)wo + (size_t)hh_ * 128 * 1024 + qq_ * 256, 1024u, L_S0, lane, w);
      float M = fmaxf(fmaxf(__uint_as_float(f[0].x), __uint_as_float(f[1].x)), fmaxf(__uint_as_float(f[2].x), __uint_as_float(f[3].x)));
      M = fmaxf(M, dppf<XOR1>(M)); M = fmaxf(M, dppf<XOR2>(M)); M = sw16_max(M); M = sw32_max(M);
      float e[4], Ls = 0.f;
#pragma unroll
      for (int i = 0; i < 4; ++i) { e[i] = __builtin_amdgcn_exp2f(__uint_as_float(f[i].x) - M); Ls = fmaf(e[i], __uint_as_float(f[i].z), Ls); }
      Ls += dppf<XOR1>(Ls); Ls += dppf<XOR2>(Ls); Ls = sw16_add(Ls, Ls); Ls = sw32_add(Ls, Ls);
      const float inv = __builtin_amdgcn_rcpf(Ls);
      asm volatile("s_waitcnt vmcnt(4)" : "+v"(d[0]), "+v"(d[1]), "+v"(d[2]), "+v"(d[3])::"memory");
      STAMP(14);
      float a[8];
#pragma unroll
      for (int k = 0; k < 8; ++k) a[k] = 0.f;
#pragma unroll
      for (int i = 0; i < 4; ++i) {
        const float wi = e[i] * inv;
#pragma unroll
        for (int k = 0; k < 4; ++k) { a[2 * k] = fmaf(wi, bflo(d[i][k]), a[2 * k]); a[2 * k + 1] = fmaf(wi, bfhi(d[i][k]), a[2 * k + 1]); }
      }
      const float r0 = sw32_add(a[0], a[4]), r1 = sw32_add(a[1], a[5]), r2 = sw32_add(a[2], a[6]), r3 = sw32_add(a[3], a[7]);
      float u0 = sw16_add(r0, r2), u1 = sw16_add(r1, r3);
      u0 += dppf<XOR1>(u0); u1 += dppf<XOR1>(u1); u0 += dppf<XOR2>(u0); u1 += dppf<XOR2>(u1);
      const u32 hi = pk2(u0, u1), lo = pk2(u0 - bflo(hi), u1 - bfhi(hi));
      const u32 cl = 32u * cs + 8u * chk + 4u * ((u32)lane >> 5) + 2u * (((u32)lane >> 4) & 1u);
      *(LAS u32*)(L + L_IMG + 272u * (bb + 2u * ((u32)lane & 1u)) + 2u * cl) = (lane & 1) ? lo : hi;
    }
    BAR();
    {
      v4u bf[4]; read_bfrags<3>(L, L_IMG, lane, bf);
      f32x4 d = mv4(wa, bf);
#pragma unroll
      for (int i = 0; i < 4; ++i) d[i] += dppf<XOR2>(d[i]);
      publish_d(ws + WS_CTX + ((u32)hh_ * 4u + qq_) * 2048u, d, lane, w, ta, tb);
    }
    STAMP(15);
    gather4(ws + WS_CTX + (u32)hh_ * 8192u, L, L_IMG + 2048u, lane, w, ta, tb);
    STAMP(16);
    BAR();
    {
      v4u a[4], bf[4];
      read_wfrags(L, L_S0, lane, w, a);
      read_bfrags<1>(L, L_IMG + 2048u, lane, bf);
      publish_d(ws + WS_OUTP + ((u32)qq_ * 32u + hh_) * 2048u, mv4(a, bf), lane, w, ta, tb);
    }
    STAMP(17);
    if (w < 2) {
      final_reduce(ws, out, 0, idx, lane, w, ta, tb);
      if (idx == 0 && w == 0 && lane == 0) __hip_atomic_store((GAS u32*)(ws + WS_E), e0 + 1u, __ATOMIC_RELAXED, __HIP_MEMORY_SCOPE_AGENT);
    }
    STAMP(18);
  } else {
    const u32 r4 = (u32)lane >> 4, c16 = (u32)lane & 15u;
    LOAD_EX(xB)
    {
      const u32 bs = (16u * w + r4) * 8192u, x = c16 ^ r4;
      dma4((const char*)wuk + (size_t)qq_ * 128 * 8192 + hh_ * 256, bs + 16u * x, bs + 32768u - 1024u + 16u * (x ^ 4u), bs + 65536u - 2048u + 16u * (x ^ 8u),
           bs + 98304u - 3072u + 16u * (x ^ 12u), L_S0 + 4096u * w);
    }
    asm volatile("s_waitcnt vmcnt(0)" : "+v"(ev), "+v"(xf[0]), "+v"(xf[1]), "+v"(xf[2]), "+v"(xf[3])::"memory");
    STAMP(1);
    e0 = __builtin_amdgcn_readfirstlane(ev);
    ta = e0 * 0x9E3779B1u + 0x7F4A7C15u; tb = (e0 ^ (e0 >> 15)) * 0x85EBCA77u + 0x3C6EF372u;
    constexpr u32 L_IMG = L_S1;
    v4u uk[4];
    {
      const u32 o = L_S0 + 4096u * w + 256u * c16 + 16u * (r4 ^ c16);
#pragma unroll
      for (int s = 0; s < 4; ++s) uk[s] = lds128(L, o ^ (64u * s));
    }
    gather4(ws + WS_QA + (u32)hh_ * 8192u, L, L_IMG, lane, w, ta, tb);
    STAMP(2);
    dma_wblock((const char*)wqB + (size_t)qq_ * 128 * 8192 + hh_ * 256, 8192u, L_S4, lane, w);
    BAR();
    {
      v4u bf[4]; read_bfrags<1>(L, L_IMG, lane, bf);
      const f32x4 d = mv4(uk, bf);
      if (c16 < 2u)
        st_sc1(ws + WS_QABS + (u32)hh_ * 2048u + (32u * qq_ + 4u * w) * 16u, c16 * 65536u + r4 * 16u, v4u{pk2(d[0], d[1]), ta, pk2(d[2], d[3]), tb});
    }
    STAMP(3);
    WAITVM(0); BAR();
    STAMP(4);
    {
      v4u a[4]; read_wfrags(L, L_S4, lane, w, a);
      publish_d(ws + WS_QB + ((u32)hh_ * 4u + qq_) * 2048u, mv4(a, xf), lane, w, ta, tb);
    }
    STAMP(5);
    const int b = idx >> 6, kvh = (idx >> 3) & 7, j = idx & 7;
    const size_t kvo = (((size_t)b * 8 + kvh) * 8192 + 6144 + 256u * j + 32u * w) * 256u;
    const u32 kb = L_S0 + 8192u * w, vb = L_S2 + 8192u * w;
    {
      const u32 x = c16 ^ r4, bs = 256u * r4;
      dma4((const char*)Kc + kvo, bs + 16u * x, bs + 16u * (x ^ 4u), bs + 16u * (x ^ 8u), bs + 16u * (x ^ 12u), kb);
      dma4((const char*)Kc + kvo + 4096, bs + 16u * x, bs + 16u * (x ^ 4u), bs + 16u * (x ^ 8u), bs + 16u * (x ^ 12u), kb + 4096u);
    }
    v4u g[2];
    const char* qsrc = ws + WS_QB + (4u * kvh + (u32)(w >> 1)) * 8192u + (u32)b * 1024u + (u32)(w & 1) * 512u;
    const u32 go = ((u32)lane & 3u) * 2048u + ((u32)lane >> 2) * 16u;
    ld_sc1<0>(g[0], qsrc, go); ld_sc1<256>(g[1], qsrc, go);
    {
      const u32 bs = 256u * r4 + 16u * (c16 & 1u), x = (c16 >> 1) ^ r4;
      dma4((const char*)Vc + kvo, bs + 32u * x, bs + 32u * (x ^ 4u), bs + 32u * x, bs + 32u * (x ^ 4u), vb);
      dma4((const char*)Vc + kvo + 4096, bs + 32u * x, bs + 32u * (x ^ 4u), bs + 32u * x, bs + 32u * (x ^ 4u), vb + 4096u);
    }
    asm volatile("s_waitcnt vmcnt(8)" : "+v"(g[0]), "+v"(g[1])::"memory");
    STAMP(6);
    v4u kf[2][4];
    {
      const u32 o = kb + 256u * c16 + 16u * (r4 ^ c16);
#pragma unroll
      for (int t = 0; t < 2; ++t)
#pragma unroll
        for (int s = 0; s < 4; ++s) kf[t][s] = lds128(L, (o ^ (64u * s)) + 4096u * t);
    }
    for (int it = 0;; ++it) {
      if (wave_ok(tagbad(g[0], ta, tb) | tagbad(g[1], ta, tb)) || it > SPIN_CAP) { STAMPV(30, it); break; }
      __builtin_amdgcn_s_sleep(1);
      ld_sc1<0>(g[0], qsrc, go); ld_sc1<256>(g[1], qsrc, go);
      asm volatile("s_waitcnt vmcnt(0)" : "+v"(g[0]), "+v"(g[1])::"memory");
    }
    constexpr u32 L_IMG2 = L_S4;
    BAR();
#pragma unroll
    for (int i = 0; i < 2; ++i) {
      float v0 = __uint_as_float(g[i].x), v1 = __uint_as_float(g[i].z);
      v0 += dppf<XOR1>(v0); v1 += dppf<XOR1>(v1); v0 += dppf<XOR2>(v0); v1 += dppf<XOR2>(v1);
      *(LAS u32*)(L + L_IMG2 + 272u * (u32)(w >> 1) + 4u * ((u32)(w & 1) * 32u + 16u * i + ((u32)lane >> 2))) = pk2(v0, v1);
    }
    BAR();
    STAMP(7);
    v4u pb; float m2, lsum;
    {
      v4u bf[4]; read_bfrags<3>(L, L_IMG2, lane, bf);
      const f32x4 s0 = mv4(kf[0], bf), s1 = mv4(kf[1], bf);
      float m = fmaxf(fmaxf(fmaxf(s0[0], s0[1]), fmaxf(s0[2], s0[3])), fmaxf(fmaxf(s1[0], s1[1]), fmaxf(s1[2], s1[3])));
      m = sw16_max(m); m = sw32_max(m);
      m2 = m * SC2;
      float p[8];
#pragma unroll
      for (int i = 0; i < 4; ++i) { p[i] = __builtin_amdgcn_exp2f(fmaf(s0[i], SC2, -m2)); p[4 + i] = __builtin_amdgcn_exp2f(fmaf(s1[i], SC2, -m2)); }
      pb = v4u{pk2(p[0], p[1]), pk2(p[2], p[3]), pk2(p[4], p[5]), pk2(p[6], p[7])};
      float l = ((p[0] + p[1]) + (p[2] + p[3])) + ((p[4] + p[5]) + (p[6] + p[7]));
      l = sw16_add(l, l); lsum = sw32_add(l, l);
    }
    BAR();
    dma_wblock((const char*)woB + (size_t)hh_ * 128 * 1024 + qq_ * 256, 1024u, L_S4, lane, w);
    WAITVM(4);
    STAMP(8);
    {
      const u32 q4 = c16 >> 2, p4 = (u32)lane & 3u, rw = 4u * r4 + q4;
      const u32 o = vb + 256u * rw + 32u * (rw & 7u) + 8u * p4;
#pragma unroll
      for (int c = 0; c < 8; ++c) {
        const v4u a = tr128(L, o ^ (32u * c), (o ^ (32u * c)) + 4096u);
        const f32x4 d = mfma16(a, pb, f32x4{0.f, 0.f, 0.f, 0.f});
        if (c16 < 4u) *(LAS f32x4*)(L + kb + 528u * c16 + 64u * c + 16u * r4) = d;
      }
      if (lane < 4) *(LAS f32x2*)(L + kb + 4096u + 8u * lane) = f32x2{m2, lsum};
    }
    BAR();
    STAMP(9);
    if (w < 4) {
      const f32x2 ml = *(LAS f32x2*)(L + L_S0 + 8192u * ((u32)lane & 7u) + 4096u + 8u * w);
      float M = ml[0];
      M = fmaxf(M, dppf<XOR1>(M)); M = fmaxf(M, dppf<XOR2>(M)); M = fmaxf(M, dppf<HMIR>(M));
      const float e = __builtin_amdgcn_exp2f(ml[0] - M);
      float Ls = e * ml[1];
      Ls += dppf<XOR1>(Ls); Ls += dppf<XOR2>(Ls); Ls += dppf<HMIR>(Ls);
      float o0 = 0.f, o1 = 0.f;
#pragma unroll
      for (int v = 0; v < 8; ++v) {
        const float ev_ = __builtin_bit_cast(float, __builtin_amdgcn_readlane(__builtin_bit_cast(int, e), v));
        const f32x2 ov = *(LAS f32x2*)(L + L_S0 + 8192u * v + 528u * w + 8u * lane);
        o0 = fmaf(ev_, ov[0], o0); o1 = fmaf(ev_, ov[1], o1);
      }
      const u32 rec = ((u32)b * 32u + 4u * kvh + w) * 8u + j;
      st_sc1(ws + WS_OB + rec * 1024u, 16u * lane, v4u{__float_as_uint(o0), ta, __float_as_uint(o1), tb});
      if (lane == 0) st_sc1(ws + WS_MLB + rec * 16u, 0u, v4u{__float_as_uint(M), ta, __float_as_uint(Ls), tb});
    }
    STAMP(10);
    constexpr u32 L_IMG3 = L_S2;
    {
      const u32 bb = (u32)w >> 2, jj = ((u32)lane & 3u) | (((u32)lane >> 5) << 2), gl = ((u32)lane >> 2) & 7u;
      const char* osrc = ws + WS_OB + (bb * 32u + hh_) * 8192u + (u32)(w & 3) * 256u;
      const char* msrc = ws + WS_MLB + (bb * 32u + hh_) * 128u;
      v4u d0, d1, f;
      for (int it = 0;; ++it) {
        ld_sc1<0>(d0, osrc, jj * 1024u + gl * 16u); ld_sc1<128>(d1, osrc, jj * 1024u + gl * 16u); ld_sc1<0>(f, msrc, jj * 16u);
        asm volatile("s_waitcnt vmcnt(0)" : "+v"(d0), "+v"(d1), "+v"(f)::"memory");
        if (wave_ok(tagbad(d0, ta, tb) | tagbad(d1, ta, tb) | tagbad(f, ta, tb)) || it > SPIN_CAP) break;
        __builtin_amdgcn_s_sleep(1);
      }
      STAMP(11);
      float M = __uint_as_float(f.x);
      M = fmaxf(M, dppf<XOR1>(M)); M = fmaxf(M, dppf<XOR2>(M)); M = sw32_max(M);
      const float e = __builtin_amdgcn_exp2f(__uint_as_float(f.x) - M);
      float Ls = e * __uint_as_float(f.z);
      Ls += dppf<XOR1>(Ls); Ls += dppf<XOR2>(Ls); Ls = sw32_add(Ls, Ls);
      const float wi = e * __builtin_amdgcn_rcpf(Ls);
      float v0 = sw32_add(wi * __uint_as_float(d0.x), wi * __uint_as_float(d1.x));
      float v1 = sw32_add(wi * __uint_as_float(d0.z), wi * __uint_as_float(d1.z));
      v0 += dppf<XOR1>(v0); v1 += dppf<XOR1>(v1); v0 += dppf<XOR2>(v0); v1 += dppf<XOR2>(v1);
      *(LAS u32*)(L + L_IMG3 + 272u * bb + 4u * ((u32)(w & 3) * 16u + ((u32)lane >> 5) * 8u + gl)) = pk2(v0, v1);
    }
    BAR();
    {
      v4u a[4], bf[4];
      read_wfrags(L, L_S4, lane, w, a);
      read_bfrags<1>(L, L_IMG3, lane, bf);
      publish_d(ws + WS_OUTP + ((4u + qq_) * 32u + hh_) * 2048u, mv4(a, bf), lane, w, ta, tb);
    }
    STAMP(17);
    if (w < 2) final_reduce(ws, out, 1, idx, lane, w, ta, tb);
    STAMP(18);
  }
  if (CAL_STAMP == 99) { for (;;) { u64 t_; asm volatile("s_memtime %0\n\ts_waitcnt lgkmcnt(0)" : "=s"(t_)::"memory"); if (t_ - c0_ >= 96000ull) break; __builtin_amdgcn_s_sleep(1); } }
  else { for (;;) { u64 t_; asm volatile("s_memrealtime %0\n\ts_waitcnt lgkmcnt(0)" : "=s"(t_)::"memory"); if ((u32)t_ - cal0 >= 4000u + calx) break; __builtin_amdgcn_s_sleep(1); } }
}

extern "C" void kernel_launch(void* const* d_in, const int* in_sizes, int n_in, void* d_out, int out_size, void* d_ws, size_t ws_size,
                              hipStream_t stream) {
  fused_decode<<<256, 512, 0, stream>>>((const u16*)d_in[0], (const u16*)d_in[1], (const u16*)d_in[2], (const u16*)d_in[3], (const u16*)d_in[4],
                                        (const u16*)d_in[5], (const u16*)d_in[6], (const u16*)d_in[7], (const u16*)d_in[8], (const u16*)d_in[9],
                                        (const u16*)d_in[10], (u16*)d_out, (char*)d_ws);
}
```

```cpp
#define CAL_STAMP 98
#define CAL_ROLE 0
#define CAL_RANK 0
#include <hip/hip_runtime.h>
#include <stdint.h>

typedef unsigned u32;
typedef unsigned long long u64;
typedef unsigned short u16;
typedef u32 v4u __attribute__((ext_vector_type(4)));
typedef u32 v2u __attribute__((ext_vector_type(2)));
typedef float f32x2 __attribute__((ext_vector_type(2)));
typedef float f32x4 __attribute__((ext_vector_type(4)));
typedef float f32x16 __attribute__((ext_vector_type(16)));
typedef __bf16 bf16x8 __attribute__((ext_vector_type(8)));
typedef __bf16 bf16x2 __attribute__((ext_vector_type(2)));
typedef short s16x4 __attribute__((ext_vector_type(4)));
#define GAS __attribute__((address_space(1)))
#define LAS __attribute__((address_space(3)))
#define DI __device__ __forceinline__

constexpr u32 WS_E = 0;
constexpr u32 WS_QA = 4096;
constexpr u32 WS_QB = WS_QA + 262144;
constexpr u32 WS_QABS = WS_QB + 262144;
constexpr u32 WS_MLA = WS_QABS + 131072;
constexpr u32 WS_CTX = WS_MLA + 65536;
constexpr u32 WS_OB = WS_CTX + 262144;
constexpr u32 WS_MLB = WS_OB + 524288;
constexpr u32 WS_OUTP = WS_MLB + 8192;
constexpr u32 WS_OA = 2u << 20;
constexpr u32 WS_DBG = 8u << 20;
static_assert(WS_OUTP + 524288 <= WS_OA, "ws map");

constexpr float SC2 = 0.08838834764831845f * 1.4426950408889634f;
constexpr int SPIN_CAP = 1 << 17;

constexpr u32 L_S0 = 0, L_S1 = 32768, L_S2 = 65536, L_S3 = 98304, L_S4 = 131072;

#define STAMP(k) do { if ((k) == 0 || (k) == CAL_STAMP) { u64 t_; asm volatile("s_memrealtime %0\n\ts_waitcnt lgkmcnt(0)" : "=s"(t_)::"memory"); if ((k) == CAL_STAMP && CAL_ROLE == roleB) calx = (u32)t_ - cal0; } } while (0)
#define STAMPV(k, v)

#define WAITVM(n) asm volatile("s_waitcnt vmcnt(%0)" ::"i"(n) : "memory")
#define BAR() asm volatile("s_waitcnt lgkmcnt(0)\n\ts_barrier" ::: "memory")

template <int CTRL> DI float dppf(float x) {
  return __builtin_bit_cast(float, __builtin_amdgcn_mov_dpp(__builtin_bit_cast(int, x), CTRL, 0xf, 0xf, true));
}
constexpr int XOR1 = 0xB1, XOR2 = 0x4E, HMIR = 0x141, ROR4 = 0x124, ROR8 = 0x128;
DI float sw32_add(float a, float b) { auto s = __builtin_amdgcn_permlane32_swap(__float_as_uint(a), __float_as_uint(b), false, false); return __uint_as_float(s[0]) + __uint_as_float(s[1]); }
DI float sw16_add(float a, float b) { auto s = __builtin_amdgcn_permlane16_swap(__float_as_uint(a), __float_as_uint(b), false, false); return __uint_as_float(s[0]) + __uint_as_float(s[1]); }
DI float sw32_max(float a) { auto s = __builtin_amdgcn_permlane32_swap(__float_as_uint(a), __float_as_uint(a), false, false); return fmaxf(__uint_as_float(s[0]), __uint_as_float(s[1])); }
DI float sw16_max(float a) { auto s = __builtin_amdgcn_permlane16_swap(__float_as_uint(a), __float_as_uint(a), false, false); return fmaxf(__uint_as_float(s[0]), __uint_as_float(s[1])); }
DI u32 pk2(float lo, float hi) { f32x2 p = {lo, hi}; return __builtin_bit_cast(u32, __builtin_convertvector(p, bf16x2)); }
DI float bflo(u32 x) { return __uint_as_float(x << 16); }
DI float bfhi(u32 x) { return __uint_as_float(x & 0xffff0000u); }
DI f32x4 mfma16(v4u a, v4u b, f32x4 c) { return __builtin_amdgcn_mfma_f32_16x16x32_bf16(__builtin_bit_cast(bf16x8, a), __builtin_bit_cast(bf16x8, b), c, 0, 0, 0); }
DI f32x16 mfma32(v4u a, v4u b, f32x16 c) { return __builtin_amdgcn_mfma_f32_32x32x16_bf16(__builtin_bit_cast(bf16x8, a), __builtin_bit_cast(bf16x8, b), c, 0, 0, 0); }

DI v4u lds128(LAS char* L, u32 off) { return *(LAS v4u*)(L + off); }
DI v2u tr64(LAS char* L, u32 off) { return __builtin_bit_cast(v2u, __builtin_amdgcn_ds_read_tr16_b64_v4i16((LAS s16x4*)(L + off))); }
DI v4u tr128(LAS char* L, u32 o0, u32 o1) { v2u a = tr64(L, o0), b = tr64(L, o1); return v4u{a.x, a.y, b.x, b.y}; }

DI void dma4(const void* sbase, u32 v0, u32 v1, u32 v2, u32 v3, u32 lds) {
  asm volatile("s_mov_b32 m0, %5\n\ts_nop 0\n\t"
               "global_load_lds_dwordx4 %0, %4 nt\n\t"
               "global_load_lds_dwordx4 %1, %4 offset:1024 nt\n\t"
               "global_load_lds_dwordx4 %2, %4 offset:2048 nt\n\t"
               "global_load_lds_dwordx4 %3, %4 offset:3072 nt"
               ::"v"(v0), "v"(v1), "v"(v2), "v"(v3), "s"(sbase), "s"(lds) : "memory");
}
DI void dma2a(const void* sbase, u32 v0, u32 v1, u32 lds) {
  asm volatile("s_mov_b32 m0, %3\n\ts_nop 0\n\t"
               "global_load_lds_dwordx4 %0, %2 nt\n\t"
               "global_load_lds_dwordx4 %1, %2 offset:1024 nt"
               ::"v"(v0), "v"(v1), "s"(sbase), "s"(lds) : "memory");
}
DI void dma2b(const void* sbase, u32 v2, u32 v3, u32 lds) {
  asm volatile("s_mov_b32 m0, %3\n\ts_nop 0\n\t"
               "global_load_lds_dwordx4 %0, %2 offset:2048 nt\n\t"
               "global_load_lds_dwordx4 %1, %2 offset:3072 nt"
               ::"v"(v2), "v"(v3), "s"(sbase), "s"(lds) : "memory");
}
template <int OFF> DI void ld_sc1(v4u& d, const void* sbase, u32 voff) {
  asm volatile("global_load_dwordx4 %0, %1, %2 offset:%3 sc1" : "=v"(d) : "v"(voff), "s"(sbase), "i"(OFF) : "memory");
}
template <int OFF> DI void ld_plain(v4u& d, const void* sbase, u32 voff) {
  asm volatile("global_load_dwordx4 %0, %1, %2 offset:%3" : "=v"(d) : "v"(voff), "s"(sbase), "i"(OFF) : "memory");
}
DI void st_sc1(void* sbase, u32 voff, v4u d) {
  asm volatile("global_store_dwordx4 %0, %1, %2 sc1\n\ts_nop 1" ::"v"(voff), "v"(d), "s"(sbase) : "memory");
}
#define PIN1(a) asm volatile("" : "+v"(a))
DI u32 tagbad(const v4u& g, u32 ta, u32 tb) { return (g.y ^ ta) | (g.w ^ tb); }
DI bool wave_ok(u32 bad) { return __builtin_amdgcn_ballot_w64(bad != 0u) == 0ull; }

struct Ctx { LAS char* L; char* ws; int lane, w; u32 ta, tb; };

DI void dma_wblock(const char* blk, u32 rstride, u32 lds, int lane, int w) {
  const u32 r = (u32)lane >> 4, c = (u32)lane & 15u;
  const u32 b = (16u * w + r) * rstride;
  const u32 x = c ^ (r << 2);
  dma4(blk, b + 16u * x, b + 4u * rstride - 1024u + 16u * (x ^ 1u), b + 8u * rstride - 2048u + 16u * (x ^ 2u), b + 12u * rstride - 3072u + 16u * (x ^ 3u),
       lds + 4096u * w);
}
DI void read_wfrags(LAS char* L, u32 lds, int lane, int w, v4u (&a)[4]) {
  const u32 g = (u32)lane >> 4, q = ((u32)lane & 15u) >> 2, p = (u32)lane & 3u;
  const u32 ch = 2u * w + (p >> 1);
  const u32 o0 = lds + 256u * (8u * g + q) + 16u * (ch ^ ((q << 2) | ((2u * g) & 3u))) + 8u * (p & 1u);
  const u32 o1 = lds + 256u * (8u * g + 4u + q) + 16u * (ch ^ ((q << 2) | ((2u * g + 1u) & 3u))) + 8u * (p & 1u);
#pragma unroll
  for (int ks = 0; ks < 4; ++ks) a[ks] = tr128(L, o0 + 8192u * ks, o1 + 8192u * ks);
}
template <int MASK> DI void read_bfrags(LAS char* L, u32 img, int lane, v4u (&b)[4]) {
  const u32 o = img + 272u * ((u32)lane & MASK) + 16u * ((u32)lane >> 4);
#pragma unroll
  for (int s = 0; s < 4; ++s) b[s] = lds128(L, o + 64u * s);
}
DI void publish_d(void* dst, f32x4 d, int lane, int w, u32 ta, u32 tb) {
  const u32 col = (u32)lane & 15u, g = (u32)lane >> 4, pr = (col >> 1) & 1u;
  const float v0 = pr ? d[2] : d[0], v1 = pr ? d[3] : d[1];
  if (col < 4u) st_sc1(dst, (col & 1u) * 1024u + (8u * w + 2u * g + pr) * 16u, v4u{__float_as_uint(v0), ta, __float_as_uint(v1), tb});
}
DI f32x4 mv4(const v4u (&a)[4], const v4u (&b)[4]) {
  f32x4 d = {0.f, 0.f, 0.f, 0.f};
#pragma unroll
  for (int s = 0; s < 4; ++s) d = mfma16(a[s], b[s], d);
  return d;
}
DI void gather4(const char* src, LAS char* L, u32 img, int lane, int w, u32 ta, u32 tb) {
  const u32 gi = (16u * w + ((u32)lane >> 2)) & 63u, b = (u32)w >> 2;
  const u32 off = ((u32)lane & 3u) * 2048u + b * 1024u + gi * 16u;
  v4u g;
  for (int it = 0;; ++it) {
    ld_sc1<0>(g, src, off);
    asm volatile("s_waitcnt vmcnt(0)" : "+v"(g)::"memory");
    if (wave_ok(tagbad(g, ta, tb)) || it > SPIN_CAP) break;
    __builtin_amdgcn_s_sleep(1);
  }
  float v0 = __uint_as_float(g.x), v1 = __uint_as_float(g.z);
  v0 += dppf<XOR1>(v0); v1 += dppf<XOR1>(v1);
  v0 += dppf<XOR2>(v0); v1 += dppf<XOR2>(v1);
  *(LAS u32*)(L + img + 272u * b + 4u * gi) = pk2(v0, v1);
}
DI void final_reduce(const char* ws, u16* out, int which, int idx, int lane, int w, u32 ta, u32 tb) {
  const u32 mq = (u32)idx >> 5, sub = (u32)idx & 31u;
  const char* src = ws + WS_OUTP + (which * 4u + mq) * 65536u;
  const u32 off = ((u32)lane >> 1) * 2048u + (u32)w * 1024u + (2u * sub + ((u32)lane & 1u)) * 16u;
  v4u g;
  for (int it = 0;; ++it) {
    ld_sc1<0>(g, src, off);
    asm volatile("s_waitcnt vmcnt(0)" : "+v"(g)::"memory");
    if (wave_ok(tagbad(g, ta, tb)) || it > SPIN_CAP) break;
    __builtin_amdgcn_s_sleep(1);
  }
  float v0 = __uint_as_float(g.x), v1 = __uint_as_float(g.z);
  v0 += dppf<XOR2>(v0); v1 += dppf<XOR2>(v1);
  v0 += dppf<ROR4>(v0); v1 += dppf<ROR4>(v1);
  v0 += dppf<ROR8>(v0); v1 += dppf<ROR8>(v1);
  v0 = sw16_add(v0, v0); v1 = sw16_add(v1, v1);
  v0 = sw32_add(v0, v0); v1 = sw32_add(v1, v1);
  if (lane < 2) *(GAS u32*)((GAS char*)out + ((u32)w * 1024u + which * 512u + 128u * mq + 4u * sub + 2u * lane) * 2u) = pk2(v0, v1);
}

typedef u32 v16u __attribute__((ext_vector_type(16)));
typedef u32 v8u __attribute__((ext_vector_type(8)));
DI const u16* mkp(u32 lo, u32 hi) { return (const u16*)(((u64)hi << 32) | lo); }
__global__ void __launch_bounds__(512, 2)
fused_decode(const u16* p0, const u16* p1, const u16* p2, const u16* p3, const u16* p4, const u16* p5, const u16* p6, const u16* p7,
             const u16* p8, const u16* p9, const u16* p10, u16* p11, char* p12) {
  u64 tt0; asm volatile("s_memrealtime %0\n\ts_waitcnt lgkmcnt(0)" : "=s"(tt0)::"memory");
  v16u ka0; v8u ka1; v2u ka2;
  asm volatile("s_load_dwordx16 %0, %3, 0x0\n\ts_load_dwordx8 %1, %3, 0x40\n\ts_load_dwordx2 %2, %3, 0x60\n\ts_waitcnt lgkmcnt(0)"
               : "=&s"(ka0), "=&s"(ka1), "=&s"(ka2) : "s"(__builtin_amdgcn_kernarg_segment_ptr()) : "memory");
  const u16 *xA = mkp(ka0[0], ka0[1]), *ckv = mkp(ka0[2], ka0[3]), *wq = mkp(ka0[4], ka0[5]), *wuk = mkp(ka0[6], ka0[7]);
  const u16 *wuv = mkp(ka0[8], ka0[9]), *wo = mkp(ka0[10], ka0[11]), *xB = mkp(ka0[12], ka0[13]), *Kc = mkp(ka0[14], ka0[15]);
  const u16 *Vc = mkp(ka1[0], ka1[1]), *wqB = mkp(ka1[2], ka1[3]), *woB = mkp(ka1[4], ka1[5]);
  u16* out = (u16*)mkp(ka1[6], ka1[7]); char* ws = (char*)mkp(ka2[0], ka2[1]);
  __shared__ __attribute__((aligned(16))) char smem[163840];
  LAS char* L = (LAS char*)smem;
  const int lane = threadIdx.x & 63, w = __builtin_amdgcn_readfirstlane(threadIdx.x >> 6);
  const int bid = blockIdx.x, roleB = (bid >> 3) & 1, idx = ((bid >> 4) << 3) | (bid & 7);
  const int hh_ = (idx & 7) + 8 * (idx >> 5), qq_ = (idx >> 3) & 3;
  u32 cal0 = (u32)tt0, calx = 0; u64 c0_; asm volatile("s_memtime %0\n\ts_waitcnt lgkmcnt(0)" : "=s"(c0_)::"memory");
  STAMP(0);
  {
    u64 pc; asm volatile("s_getpc_b64 %0" : "=s"(pc));
    pc &= ~(u64)0x7f;
    if (w >= 5) {
      asm volatile("s_mov_b32 m0, %2\n\ts_nop 0\n\tglobal_load_lds_dword %0, %1"
                   ::"v"((u32)lane * 128u + (u32)(w - 5) * 8192u), "s"(pc), "s"(L_S3 + 4096u * (u32)w) : "memory");
    } else {
      asm volatile("s_mov_b32 m0, %2\n\ts_nop 0\n\tglobal_load_lds_dword %0, %1" ::"v"(0u), "s"(pc), "s"(L_S3 + 4096u * (u32)w) : "memory");
    }
  }
  const u32 xo = ((u32)lane & 1u) * 1024u + (u32)qq_ * 256u + ((u32)lane >> 4) * 16u;
#define LOAD_EX(x) \
  u32 ev; v4u xf[4]; \
  asm volatile("global_load_dword %0, %1, %2 sc1" : "=v"(ev) : "v"(0u), "s"(ws) : "memory"); \
  ld_plain<0>(xf[0], x, xo); ld_plain<64>(xf[1], x, xo); ld_plain<128>(xf[2], x, xo); ld_plain<192>(xf[3], x, xo);
  asm volatile("" ::"s"(xA), "s"(ckv), "s"(wq), "s"(wuk), "s"(wuv), "s"(wo), "s"(xB), "s"(Kc), "s"(Vc), "s"(wqB), "s"(woB), "s"(out), "s"(ws));
  u32 ta, tb, e0;

  if (!roleB) {
    const int b = (idx & 7) >> 2, sp = (idx >> 3) * 4 + (idx & 3);
    const char* kv = (const char*)ckv + ((size_t)b * 8192 + (size_t)sp * 128 + 4u * w) * 1024u;
    const u32 kx = (16u * lane) ^ (16u * (w & 3));
    const u32 k0 = kx, k1 = kx ^ 64u, k2 = kx ^ 128u, k3 = kx ^ 192u;
    const u32 lw = 4096u * w;
    LOAD_EX(xA)
    dma_wblock((const char*)wq + (size_t)qq_ * 128 * 8192 + hh_ * 256, 8192u, L_S4, lane, w);
    dma2a(kv, k0, k1, L_S0 + lw);
    asm volatile("s_waitcnt vmcnt(2)" : "+v"(ev), "+v"(xf[0]), "+v"(xf[1]), "+v"(xf[2]), "+v"(xf[3])::"memory");
    STAMP(1);
    dma2b(kv, k2, k3, L_S0 + lw);
    e0 = __builtin_amdgcn_readfirstlane(ev);
    ta = e0 * 0x9E3779B1u + 0x7F4A7C15u; tb = (e0 ^ (e0 >> 15)) * 0x85EBCA77u + 0x3C6EF372u;
    BAR();
    {
      v4u a[4]; read_wfrags(L, L_S4, lane, w, a);
      f32x4 d = mv4(a, xf);
      publish_d(ws + WS_QA + ((u32)hh_ * 4u + qq_) * 2048u, d, lane, w, ta, tb);
    }
    STAMP(2);
    dma4(kv + 32768, k0, k1, k2, k3, L_S1 + lw);
    dma4(kv + 65536, k0, k1, k2, k3, L_S2 + lw);
    const int kt = w & 3, ch = w >> 2;
    v4u ka[16], vt[8][2];
    const u32 r31 = (u32)lane & 31u, hf = (u32)lane >> 5;
    const u32 fr = ((r31 & 3u) << 2) | ((r31 >> 2) & 3u);
    const u32 rowo = 1024u * r31 + 16u * (hf ^ fr) + 512u * ch;
    const u32 blk = ((u32)lane >> 4) & 1u, q4 = ((u32)lane & 15u) >> 2, p4 = (u32)lane & 3u;
    const u32 chx = 8u * w + 2u * blk + (p4 >> 1);
    const u32 tro0 = 1024u * (8u * hf + q4) + 16u * (chx ^ ((q4 << 2) | (2u * hf))) + 8u * (p4 & 1u);
    const u32 tro1 = 1024u * (8u * hf + 4u + q4) + 16u * (chx ^ ((q4 << 2) | (2u * hf + 1u))) + 8u * (p4 & 1u);
#define READ_TILE(t)                                                                                          \
    {                                                                                                         \
      if (kt == (t)) {                                                                                        \
        _Pragma("unroll") for (int s = 0; s < 16; ++s) ka[s] = lds128(L, 32768u * (t) + ((rowo ^ (32u * (s & 7))) + 256u * (s >> 3))); \
      }                                                                                                       \
      _Pragma("unroll") for (int ks = 0; ks < 2; ++ks)                                                        \
        _Pragma("unroll") for (int u = 0; u < 2; ++u)                                                         \
          vt[2 * (t) + ks][u] = tr128(L, 32768u * (t) + 16384u * ks + (tro0 ^ (64u * u)), 32768u * (t) + 16384u * ks + (tro1 ^ (64u * u))); \
    }
#pragma unroll
    for (int s = 0; s < 16; ++s) ka[s] = v4u{0u, 0u, 0u, 0u};
    WAITVM(8); BAR(); STAMP(3);
    READ_TILE(0)
    dma4(kv + 98304, k0, k1, k2, k3, L_S3 + lw);
    WAITVM(8); BAR(); STAMP(4);
    READ_TILE(1)
    v4u g[8];
    const char* qsrc = ws + WS_QABS + (u32)b * 65536u + (u32)w * 8192u;
    const u32 go = 16u * lane;
#define GATHER_Q() { ld_sc1<0>(g[0], qsrc, go); ld_sc1<1024>(g[1], qsrc, go); ld_sc1<2048>(g[2], qsrc, go); ld_sc1<3072>(g[3], qsrc, go); \
                     ld_sc1<0>(g[4], qsrc + 4096, go); ld_sc1<1024>(g[5], qsrc + 4096, go); ld_sc1<2048>(g[6], qsrc + 4096, go); ld_sc1<3072>(g[7], qsrc + 4096, go); }
    GATHER_Q()
    WAITVM(12); BAR(); STAMP(5);
    READ_TILE(2)
    WAITVM(8); BAR(); STAMP(6);
    READ_TILE(3)
#define WAIT_Q() asm volatile("s_waitcnt vmcnt(0)" : "+v"(g[0]), "+v"(g[1]), "+v"(g[2]), "+v"(g[3]), "+v"(g[4]), "+v"(g[5]), "+v"(g[6]), "+v"(g[7])::"memory")
#define BAD_Q() (tagbad(g[0], ta, tb) | tagbad(g[1], ta, tb) | tagbad(g[2], ta, tb) | tagbad(g[3], ta, tb) | tagbad(g[4], ta, tb) | tagbad(g[5], ta, tb) | tagbad(g[6], ta, tb) | tagbad(g[7], ta, tb))
    WAIT_Q();
    if (!wave_ok(BAD_Q())) {
      for (int it = 0; it < SPIN_CAP; ++it) {
        __builtin_amdgcn_s_sleep(2);
        GATHER_Q()
        WAIT_Q();
        STAMPV(30, it + 1);
        if (wave_ok(BAD_Q())) break;
      }
    }
    STAMP(7);
#pragma unroll
    for (int i = 0; i < 8; ++i) {
      const u32 gi = 64u * (i & 1) + lane, f = ((u32)(i >> 1) << 2) | (u32)(w & 3);
      *(LAS v2u*)(L + L_S4 + 1024u * (4u * w + (i >> 1)) + 16u * ((gi >> 1) ^ f) + 8u * (gi & 1u)) = v2u{g[i].x, g[i].z};
    }
    BAR();
    STAMP(8);
    dma_wblock((const char*)wuv + (size_t)qq_ * 128 * 8192 + hh_ * 256, 8192u, L_S2, lane, w);
    {
      f32x16 acc;
#pragma unroll
      for (int i = 0; i < 16; ++i) acc[i] = 0.f;
#pragma unroll
      for (int s = 0; s < 16; ++s) acc = mfma32(ka[s], lds128(L, L_S4 + ((rowo ^ (32u * (s & 7))) + 256u * (s >> 3))), acc);
      const u32 so = L_S0 + 16896u * ch + 528u * r31 + 128u * kt + 16u * hf;
#pragma unroll
      for (int a = 0; a < 4; ++a) *(LAS f32x4*)(L + so + 32u * a) = f32x4{acc[4 * a], acc[4 * a + 1], acc[4 * a + 2], acc[4 * a + 3]};
    }
    BAR();
    STAMP(9);
    constexpr u32 L_P = L_S1 + 16384;
    float m2, lsum;
    {
      const u32 hd = 4u * w + ((u32)lane >> 4), j = (u32)lane & 15u;
      const u32 so = L_S0 + 528u * hd + 32u * j;
      const f32x4 s0 = *(LAS f32x4*)(L + so) + *(LAS f32x4*)(L + so + 16896u);
      const f32x4 s1 = *(LAS f32x4*)(L + so + 16u) + *(LAS f32x4*)(L + so + 16896u + 16u);
      float m = fmaxf(fmaxf(fmaxf(s0[0], s0[1]), fmaxf(s0[2], s0[3])), fmaxf(fmaxf(s1[0], s1[1]), fmaxf(s1[2], s1[3])));
      m = fmaxf(m, dppf<XOR1>(m)); m = fmaxf(m, dppf<XOR2>(m)); m = fmaxf(m, dppf<ROR4>(m)); m = fmaxf(m, dppf<ROR8>(m));
      m2 = m * SC2;
      float p[8];
#pragma unroll
      for (int i = 0; i < 4; ++i) { p[i] = __builtin_amdgcn_exp2f(fmaf(s0[i], SC2, -m2)); p[4 + i] = __builtin_amdgcn_exp2f(fmaf(s1[i], SC2, -m2)); }
      *(LAS v4u*)(L + L_P + 256u * hd + 16u * (j ^ (hd & 15u))) = v4u{pk2(p[0], p[1]), pk2(p[2], p[3]), pk2(p[4], p[5]), pk2(p[6], p[7])};
      float l = ((p[0] + p[1]) + (p[2] + p[3])) + ((p[4] + p[5]) + (p[6] + p[7]));
      l += dppf<XOR1>(l); l += dppf<XOR2>(l); l += dppf<ROR4>(l); l += dppf<ROR8>(l);
      lsum = l;
    }
    BAR();
    STAMP(10);
    {
      f32x16 o0, o1;
#pragma unroll
      for (int i = 0; i < 16; ++i) { o0[i] = 0.f; o1[i] = 0.f; }
      const u32 po = L_P + 256u * r31 + 16u * (hf ^ (r31 & 15u));
#pragma unroll
      for (int s = 0; s < 8; ++s) {
        const v4u pf = lds128(L, po ^ (32u * s));
        o0 = mfma32(vt[s][0], pf, o0);
        o1 = mfma32(vt[s][1], pf, o1);
      }
      const u32 sb = L_S3 + lw + 128u * r31 + 8u * hf, sx = 16u * (r31 & 7u);
#pragma unroll
      for (int a = 0; a < 4; ++a) {
        *(LAS v2u*)(L + sb + ((16u * a) ^ sx)) = v2u{pk2(o0[4 * a], o0[4 * a + 1]), pk2(o0[4 * a + 2], o0[4 * a + 3])};
        *(LAS v2u*)(L + sb + ((64u + 16u * a) ^ sx)) = v2u{pk2(o1[4 * a], o1[4 * a + 1]), pk2(o1[4 * a + 2], o1[4 * a + 3])};
      }
      const u32 rr = (u32)lane >> 3, kk = (u32)lane & 7u;
      char* od = ws + WS_OA + (((u32)b * 32u * 4u + (u32)(w >> 1)) * 64u + sp) * 256u + (u32)(w & 1) * 128u;
      v4u t[4];
#pragma unroll
      for (int i = 0; i < 4; ++i) t[i] = lds128(L, L_S3 + lw + 1024u * i + 128u * rr + 16u * (kk ^ rr));
#pragma unroll
      for (int i = 0; i < 4; ++i) st_sc1(od, (8u * i + rr) * 65536u + 16u * kk, t[i]);
    }
    STAMP(11);
    WAITVM(0); BAR();
    STAMP(12);
    if ((lane & 15) == 0)
      st_sc1(ws + WS_MLA + (u32)b * 32768u + (u32)sp * 16u, (4u * w + ((u32)lane >> 4)) * 1024u, v4u{__float_as_uint(m2), ta, __float_as_uint(lsum), tb});
    v4u wa[4];
    read_wfrags(L, L_S2, lane, w, wa);
    constexpr u32 L_IMG = L_S4;
    {
      const u32 bb = (u32)w >> 2, cs = (u32)w & 3u;
      const u32 rec = ((u32)lane & 3u) | (((u32)lane >> 4) << 2), chk = ((u32)lane >> 2) & 3u;
      const char* fsrc = ws + WS_MLA + (bb * 32u + hh_) * 1024u;
      v4u f[4];
      for (int it = 0;; ++it) {
        ld_sc1<0>(f[0], fsrc, rec * 16u); ld_sc1<256>(f[1], fsrc, rec * 16u); ld_sc1<512>(f[2], fsrc, rec * 16u); ld_sc1<768>(f[3], fsrc, rec * 16u);
        asm volatile("s_waitcnt vmcnt(0)" : "+v"(f[0]), "+v"(f[1]), "+v"(f[2]), "+v"(f[3])::"memory");
        if (wave_ok(tagbad(f[0], ta, tb) | tagbad(f[1], ta, tb) | tagbad(f[2], ta, tb) | tagbad(f[3], ta, tb)) || it > SPIN_CAP) break;
        __builtin_amdgcn_s_sleep(1);
      }
      STAMP(13);
      const char* psrc = ws + WS_OA + ((bb * 32u + hh_) * 4u + qq_) * 16384u + cs * 64u;
      const u32 po = rec * 256u + chk * 16u;
      v4u d[4];
      ld_sc1<0>(d[0], psrc, po); ld_sc1<0>(d[1], psrc + 4096, po); ld_sc1<0>(d[2], psrc + 8192, po); ld_sc1<0>(d[3], psrc + 12288, po);
      dma_wblock((const char*)wo + (size_t)hh_ * 128 * 1024 + qq_ * 256, 1024u, L_S0, lane, w);
      float M = fmaxf(fmaxf(__uint_as_float(f[0].x), __uint_as_float(f[1].x)), fmaxf(__uint_as_float(f[2].x), __uint_as_float(f[3].x)));
      M = fmaxf(M, dppf<XOR1>(M)); M = fmaxf(M, dppf<XOR2>(M)); M = sw16_max(M); M = sw32_max(M);
      float e[4], Ls = 0.f;
#pragma unroll
      for (int i = 0; i < 4; ++i) { e[i] = __builtin_amdgcn_exp2f(__uint_as_float(f[i].x) - M); Ls = fmaf(e[i], __uint_as_float(f[i].z), Ls); }
      Ls += dppf<XOR1>(Ls); Ls += dppf<XOR2>(Ls); Ls = sw16_add(Ls, Ls); Ls = sw32_add(Ls, Ls);
      const float inv = __builtin_amdgcn_rcpf(Ls);
      asm volatile("s_waitcnt vmcnt(4)" : "+v"(d[0]), "+v"(d[1]), "+v"(d[2]), "+v"(d[3])::"memory");
      STAMP(14);
      float a[8];
#pragma unroll
      for (int k = 0; k < 8; ++k) a[k] = 0.f;
#pragma unroll
      for (int i = 0; i < 4; ++i) {
        const float wi = e[i] * inv;
#pragma unroll
        for (int k = 0; k < 4; ++k) { a[2 * k] = fmaf(wi, bflo(d[i][k]), a[2 * k]); a[2 * k + 1] = fmaf(wi, bfhi(d[i][k]), a[2 * k + 1]); }
      }
      const float r0 = sw32_add(a[0], a[4]), r1 = sw32_add(a[1], a[5]), r2 = sw32_add(a[2], a[6]), r3 = sw32_add(a[3], a[7]);
      float u0 = sw16_add(r0, r2), u1 = sw16_add(r1, r3);
      u0 += dppf<XOR1>(u0); u1 += dppf<XOR1>(u1); u0 += dppf<XOR2>(u0); u1 += dppf<XOR2>(u1);
      const u32 hi = pk2(u0, u1), lo = pk2(u0 - bflo(hi), u1 - bfhi(hi));
      const u32 cl = 32u * cs + 8u * chk + 4u * ((u32)lane >> 5) + 2u * (((u32)lane >> 4) & 1u);
      *(LAS u32*)(L + L_IMG + 272u * (bb + 2u * ((u32)lane & 1u)) + 2u * cl) = (lane & 1) ? lo : hi;
    }
    BAR();
    {
      v4u bf[4]; read_bfrags<3>(L, L_IMG, lane, bf);
      f32x4 d = mv4(wa, bf);
#pragma unroll
      for (int i = 0; i < 4; ++i) d[i] += dppf<XOR2>(d[i]);
      publish_d(ws + WS_CTX + ((u32)hh_ * 4u + qq_) * 2048u, d, lane, w, ta, tb);
    }
    STAMP(15);
    gather4(ws + WS_CTX + (u32)hh_ * 8192u, L, L_IMG + 2048u, lane, w, ta, tb);
    STAMP(16);
    BAR();
    {
      v4u a[4], bf[4];
      read_wfrags(L, L_S0, lane, w, a);
      read_bfrags<1>(L, L_IMG + 2048u, lane, bf);
      publish_d(ws + WS_OUTP + ((u32)qq_ * 32u + hh_) * 2048u, mv4(a, bf), lane, w, ta, tb);
    }
    STAMP(17);
    if (w < 2) {
      final_reduce(ws, out, 0, idx, lane, w, ta, tb);
      if (idx == 0 && w == 0 && lane == 0) __hip_atomic_store((GAS u32*)(ws + WS_E), e0 + 1u, __ATOMIC_RELAXED, __HIP_MEMORY_SCOPE_AGENT);
    }
    STAMP(18);
  } else {
    const u32 r4 = (u32)lane >> 4, c16 = (u32)lane & 15u;
    LOAD_EX(xB)
    {
      const u32 bs = (16u * w + r4) * 8192u, x = c16 ^ r4;
      dma4((const char*)wuk + (size_t)qq_ * 128 * 8192 + hh_ * 256, bs + 16u * x, bs + 32768u - 1024u + 16u * (x ^ 4u), bs + 65536u - 2048u + 16u * (x ^ 8u),
           bs + 98304u - 3072u + 16u * (x ^ 12u), L_S0 + 4096u * w);
    }
    asm volatile("s_waitcnt vmcnt(0)" : "+v"(ev), "+v"(xf[0]), "+v"(xf[1]), "+v"(xf[2]), "+v"(xf[3])::"memory");
    STAMP(1);
    e0 = __builtin_amdgcn_readfirstlane(ev);
    ta = e0 * 0x9E3779B1u + 0x7F4A7C15u; tb = (e0 ^ (e0 >> 15)) * 0x85EBCA77u + 0x3C6EF372u;
    constexpr u32 L_IMG = L_S1;
    v4u uk[4];
    {
      const u32 o = L_S0 + 4096u * w + 256u * c16 + 16u * (r4 ^ c16);
#pragma unroll
      for (int s = 0; s < 4; ++s) uk[s] = lds128(L, o ^ (64u * s));
    }
    gather4(ws + WS_QA + (u32)hh_ * 8192u, L, L_IMG, lane, w, ta, tb);
    STAMP(2);
    dma_wblock((const char*)wqB + (size_t)qq_ * 128 * 8192 + hh_ * 256, 8192u, L_S4, lane, w);
    BAR();
    {
      v4u bf[4]; read_bfrags<1>(L, L_IMG, lane, bf);
      const f32x4 d = mv4(uk, bf);
      if (c16 < 2u)
        st_sc1(ws + WS_QABS + (u32)hh_ * 2048u + (32u * qq_ + 4u * w) * 16u, c16 * 65536u + r4 * 16u, v4u{pk2(d[0], d[1]), ta, pk2(d[2], d[3]), tb});
    }
    STAMP(3);
    WAITVM(0); BAR();
    STAMP(4);
    {
      v4u a[4]; read_wfrags(L, L_S4, lane, w, a);
      publish_d(ws + WS_QB + ((u32)hh_ * 4u + qq_) * 2048u, mv4(a, xf), lane, w, ta, tb);
    }
    STAMP(5);
    const int b = idx >> 6, kvh = (idx >> 3) & 7, j = idx & 7;
    const size_t kvo = (((size_t)b * 8 + kvh) * 8192 + 6144 + 256u * j + 32u * w) * 256u;
    const u32 kb = L_S0 + 8192u * w, vb = L_S2 + 8192u * w;
    {
      const u32 x = c16 ^ r4, bs = 256u * r4;
      dma4((const char*)Kc + kvo, bs + 16u * x, bs + 16u * (x ^ 4u), bs + 16u * (x ^ 8u), bs + 16u * (x ^ 12u), kb);
      dma4((const char*)Kc + kvo + 4096, bs + 16u * x, bs + 16u * (x ^ 4u), bs + 16u * (x ^ 8u), bs + 16u * (x ^ 12u), kb + 4096u);
    }
    v4u g[2];
    const char* qsrc = ws + WS_QB + (4u * kvh + (u32)(w >> 1)) * 8192u + (u32)b * 1024u + (u32)(w & 1) * 512u;
    const u32 go = ((u32)lane & 3u) * 2048u + ((u32)lane >> 2) * 16u;
    ld_sc1<0>(g[0], qsrc, go); ld_sc1<256>(g[1], qsrc, go);
    {
      const u32 bs = 256u * r4 + 16u * (c16 & 1u), x = (c16 >> 1) ^ r4;
      dma4((const char*)Vc + kvo, bs + 32u * x, bs + 32u * (x ^ 4u), bs + 32u * x, bs + 32u * (x ^ 4u), vb);
      dma4((const char*)Vc + kvo + 4096, bs + 32u * x, bs + 32u * (x ^ 4u), bs + 32u * x, bs + 32u * (x ^ 4u), vb + 4096u);
    }
    asm volatile("s_waitcnt vmcnt(8)" : "+v"(g[0]), "+v"(g[1])::"memory");
    STAMP(6);
    v4u kf[2][4];
    {
      const u32 o = kb + 256u * c16 + 16u * (r4 ^ c16);
#pragma unroll
      for (int t = 0; t < 2; ++t)
#pragma unroll
        for (int s = 0; s < 4; ++s) kf[t][s] = lds128(L, (o ^ (64u * s)) + 4096u * t);
    }
    for (int it = 0;; ++it) {
      if (wave_ok(tagbad(g[0], ta, tb) | tagbad(g[1], ta, tb)) || it > SPIN_CAP) { STAMPV(30, it); break; }
      __builtin_amdgcn_s_sleep(1);
      ld_sc1<0>(g[0], qsrc, go); ld_sc1<256>(g[1], qsrc, go);
      asm volatile("s_waitcnt vmcnt(0)" : "+v"(g[0]), "+v"(g[1])::"memory");
    }
    constexpr u32 L_IMG2 = L_S4;
    BAR();
#pragma unroll
    for (int i = 0; i < 2; ++i) {
      float v0 = __uint_as_float(g[i].x), v1 = __uint_as_float(g[i].z);
      v0 += dppf<XOR1>(v0); v1 += dppf<XOR1>(v1); v0 += dppf<XOR2>(v0); v1 += dppf<XOR2>(v1);
      *(LAS u32*)(L + L_IMG2 + 272u * (u32)(w >> 1) + 4u * ((u32)(w & 1) * 32u + 16u * i + ((u32)lane >> 2))) = pk2(v0, v1);
    }
    BAR();
    STAMP(7);
    v4u pb; float m2, lsum;
    {
      v4u bf[4]; read_bfrags<3>(L, L_IMG2, lane, bf);
      const f32x4 s0 = mv4(kf[0], bf), s1 = mv4(kf[1], bf);
      float m = fmaxf(fmaxf(fmaxf(s0[0], s0[1]), fmaxf(s0[2], s0[3])), fmaxf(fmaxf(s1[0], s1[1]), fmaxf(s1[2], s1[3])));
      m = sw16_max(m); m = sw32_max(m);
      m2 = m * SC2;
      float p[8];
#pragma unroll
      for (int i = 0; i < 4; ++i) { p[i] = __builtin_amdgcn_exp2f(fmaf(s0[i], SC2, -m2)); p[4 + i] = __builtin_amdgcn_exp2f(fmaf(s1[i], SC2, -m2)); }
      pb = v4u{pk2(p[0], p[1]), pk2(p[2], p[3]), pk2(p[4], p[5]), pk2(p[6], p[7])};
      float l = ((p[0] + p[1]) + (p[2] + p[3])) + ((p[4] + p[5]) + (p[6] + p[7]));
      l = sw16_add(l, l); lsum = sw32_add(l, l);
    }
    BAR();
    dma_wblock((const char*)woB + (size_t)hh_ * 128 * 1024 + qq_ * 256, 1024u, L_S4, lane, w);
    WAITVM(4);
    STAMP(8);
    {
      const u32 q4 = c16 >> 2, p4 = (u32)lane & 3u, rw = 4u * r4 + q4;
      const u32 o = vb + 256u * rw + 32u * (rw & 7u) + 8u * p4;
#pragma unroll
      for (int c = 0; c < 8; ++c) {
        const v4u a = tr128(L, o ^ (32u * c), (o ^ (32u * c)) + 4096u);
        const f32x4 d = mfma16(a, pb, f32x4{0.f, 0.f, 0.f, 0.f});
        if (c16 < 4u) *(LAS f32x4*)(L + kb + 528u * c16 + 64u * c + 16u * r4) = d;
      }
      if (lane < 4) *(LAS f32x2*)(L + kb + 4096u + 8u * lane) = f32x2{m2, lsum};
    }
    BAR();
    STAMP(9);
    if (w < 4) {
      const f32x2 ml = *(LAS f32x2*)(L + L_S0 + 8192u * ((u32)lane & 7u) + 4096u + 8u * w);
      float M = ml[0];
      M = fmaxf(M, dppf<XOR1>(M)); M = fmaxf(M, dppf<XOR2>(M)); M = fmaxf(M, dppf<HMIR>(M));
      const float e = __builtin_amdgcn_exp2f(ml[0] - M);
      float Ls = e * ml[1];
      Ls += dppf<XOR1>(Ls); Ls += dppf<XOR2>(Ls); Ls += dppf<HMIR>(Ls);
      float o0 = 0.f, o1 = 0.f;
#pragma unroll
      for (int v = 0; v < 8; ++v) {
        const float ev_ = __builtin_bit_cast(float, __builtin_amdgcn_readlane(__builtin_bit_cast(int, e), v));
        const f32x2 ov = *(LAS f32x2*)(L + L_S0 + 8192u * v + 528u * w + 8u * lane);
        o0 = fmaf(ev_, ov[0], o0); o1 = fmaf(ev_, ov[1], o1);
      }
      const u32 rec = ((u32)b * 32u + 4u * kvh + w) * 8u + j;
      st_sc1(ws + WS_OB + rec * 1024u, 16u * lane, v4u{__float_as_uint(o0), ta, __float_as_uint(o1), tb});
      if (lane == 0) st_sc1(ws + WS_MLB + rec * 16u, 0u, v4u{__float_as_uint(M), ta, __float_as_uint(Ls), tb});
    }
    STAMP(10);
    constexpr u32 L_IMG3 = L_S2;
    {
      const u32 bb = (u32)w >> 2, jj = ((u32)lane & 3u) | (((u32)lane >> 5) << 2), gl = ((u32)lane >> 2) & 7u;
      const char* osrc = ws + WS_OB + (bb * 32u + hh_) * 8192u + (u32)(w & 3) * 256u;
      const char* msrc = ws + WS_MLB + (bb * 32u + hh_) * 128u;
      v4u d0, d1, f;
      for (int it = 0;; ++it) {
        ld_sc1<0>(d0, osrc, jj * 1024u + gl * 16u); ld_sc1<128>(d1, osrc, jj * 1024u + gl * 16u); ld_sc1<0>(f, msrc, jj * 16u);
        asm volatile("s_waitcnt vmcnt(0)" : "+v"(d0), "+v"(d1), "+v"(f)::"memory");
        if (wave_ok(tagbad(d0, ta, tb) | tagbad(d1, ta, tb) | tagbad(f, ta, tb)) || it > SPIN_CAP) break;
        __builtin_amdgcn_s_sleep(1);
      }
      STAMP(11);
      float M = __uint_as_float(f.x);
      M = fmaxf(M, dppf<XOR1>(M)); M = fmaxf(M, dppf<XOR2>(M)); M = sw32_max(M);
      const float e = __builtin_amdgcn_exp2f(__uint_as_float(f.x) - M);
      float Ls = e * __uint_as_float(f.z);
      Ls += dppf<XOR1>(Ls); Ls += dppf<XOR2>(Ls); Ls = sw32_add(Ls, Ls);
      const float wi = e * __builtin_amdgcn_rcpf(Ls);
      float v0 = sw32_add(wi * __uint_as_float(d0.x), wi * __uint_as_float(d1.x));
      float v1 = sw32_add(wi * __uint_as_float(d0.z), wi * __uint_as_float(d1.z));
      v0 += dppf<XOR1>(v0); v1 += dppf<XOR1>(v1); v0 += dppf<XOR2>(v0); v1 += dppf<XOR2>(v1);
      *(LAS u32*)(L + L_IMG3 + 272u * bb + 4u * ((u32)(w & 3) * 16u + ((u32)lane >> 5) * 8u + gl)) = pk2(v0, v1);
    }
    BAR();
    {
      v4u a[4], bf[4];
      read_wfrags(L, L_S4, lane, w, a);
      read_bfrags<1>(L, L_IMG3, lane, bf);
      publish_d(ws + WS_OUTP + ((4u + qq_) * 32u + hh_) * 2048u, mv4(a, bf), lane, w, ta, tb);
    }
    STAMP(17);
    if (w < 2) final_reduce(ws, out, 1, idx, lane, w, ta, tb);
    STAMP(18);
  }
  {
    char* sb = ws + (12u << 20);
    const u32 xcc = (u32)__builtin_amdgcn_s_getreg(20 | (3 << 11)) & 0xfu;
    if (w == 0 && lane == 0) st_sc1(sb, (u32)bid * 16u, v4u{cal0, ta, xcc, tb});
    v4u g0, g1, g2, g3;
    for (int it = 0; it < SPIN_CAP; ++it) {
      ld_sc1<0>(g0, sb, 16u * lane); ld_sc1<1024>(g1, sb, 16u * lane); ld_sc1<2048>(g2, sb, 16u * lane); ld_sc1<3072>(g3, sb, 16u * lane);
      asm volatile("s_waitcnt vmcnt(0)" : "+v"(g0), "+v"(g1), "+v"(g2), "+v"(g3)::"memory");
      if (wave_ok(tagbad(g0, ta, tb) | tagbad(g1, ta, tb) | tagbad(g2, ta, tb) | tagbad(g3, ta, tb))) break;
      __builtin_amdgcn_s_sleep(8);
    }
    u32 mn = min(min(g0.x - cal0 + 0x10000u, g1.x - cal0 + 0x10000u), min(g2.x - cal0 + 0x10000u, g3.x - cal0 + 0x10000u));
    u32 best = 0xffffffffu, rank_val = 0;
    u32 gm[8];
    for (int x = 0; x < 8; ++x) { u32 m = 0xffffffffu; for (int i = 0; i < 8; ++i) m = min(m, (u32)__builtin_amdgcn_readlane((int)mn, x + 8 * i)); gm[x] = m; best = min(best, m); }
    for (int x = 0; x < 8; ++x) { int r = 0; for (int y = 0; y < 8; ++y) r += (gm[y] < gm[x]) || (gm[y] == gm[x] && y < x); if (r == CAL_RANK) rank_val = gm[x]; }
    const u32 ref = cal0 + rank_val - 0x10000u;
    for (;;) { u64 t_; asm volatile("s_memrealtime %0\n\ts_waitcnt lgkmcnt(0)" : "=s"(t_)::"memory"); if ((u32)t_ - ref >= 4000u) break; __builtin_amdgcn_s_sleep(1); }
  }
}

extern "C" void kernel_launch(void* const* d_in, const int* in_sizes, int n_in, void* d_out, int out_size, void* d_ws, size_t ws_size,
                              hipStream_t stream) {
  fused_decode<<<256, 512, 0, stream>>>((const u16*)d_in[0], (const u16*)d_in[1], (const u16*)d_in[2], (const u16*)d_in[3], (const u16*)d_in[4],
                                        (const u16*)d_in[5], (const u16*)d_in[6], (const u16*)d_in[7], (const u16*)d_in[8], (const u16*)d_in[9],
                                        (const u16*)d_in[10], (u16*)d_out, (char*)d_ws);
}
```

```cpp
#define CAL_STAMP 98
#define CAL_ROLE 0
#include <hip/hip_runtime.h>
#include <stdint.h>

typedef unsigned u32;
typedef unsigned long long u64;
typedef unsigned short u16;
typedef u32 v4u __attribute__((ext_vector_type(4)));
typedef u32 v2u __attribute__((ext_vector_type(2)));
typedef float f32x2 __attribute__((ext_vector_type(2)));
typedef float f32x4 __attribute__((ext_vector_type(4)));
typedef float f32x16 __attribute__((ext_vector_type(16)));
typedef __bf16 bf16x8 __attribute__((ext_vector_type(8)));
typedef __bf16 bf16x2 __attribute__((ext_vector_type(2)));
typedef short s16x4 __attribute__((ext_vector_type(4)));
#define GAS __attribute__((address_space(1)))
#define LAS __attribute__((address_space(3)))
#define DI __device__ __forceinline__

constexpr u32 WS_E = 0;
constexpr u32 WS_QA = 4096;
constexpr u32 WS_QB = WS_QA + 262144;
constexpr u32 WS_QABS = WS_QB + 262144;
constexpr u32 WS_MLA = WS_QABS + 131072;
constexpr u32 WS_CTX = WS_MLA + 65536;
constexpr u32 WS_OB = WS_CTX + 262144;
constexpr u32 WS_MLB = WS_OB + 524288;
constexpr u32 WS_OUTP = WS_MLB + 8192;
constexpr u32 WS_OA = 2u << 20;
constexpr u32 WS_DBG = 8u << 20;
static_assert(WS_OUTP + 524288 <= WS_OA, "ws map");

constexpr float SC2 = 0.08838834764831845f * 1.4426950408889634f;
constexpr int SPIN_CAP = 1 << 17;

constexpr u32 L_S0 = 0, L_S1 = 32768, L_S2 = 65536, L_S3 = 98304, L_S4 = 131072;

#define STAMP(k) do { if ((k) == 0 || (k) == CAL_STAMP) { u64 t_; asm volatile("s_memrealtime %0\n\ts_waitcnt lgkmcnt(0)" : "=s"(t_)::"memory"); if ((k) == CAL_STAMP && CAL_ROLE == roleB) calx = (u32)t_ - cal0; } } while (0)
#define STAMPV(k, v)

#define WAITVM(n) asm volatile("s_waitcnt vmcnt(%0)" ::"i"(n) : "memory")
#define BAR() asm volatile("s_waitcnt lgkmcnt(0)\n\ts_barrier" ::: "memory")

template <int CTRL> DI float dppf(float x) {
  return __builtin_bit_cast(float, __builtin_amdgcn_mov_dpp(__builtin_bit_cast(int, x), CTRL, 0xf, 0xf, true));
}
constexpr int XOR1 = 0xB1, XOR2 = 0x4E, HMIR = 0x141, ROR4 = 0x124, ROR8 = 0x128;
DI float sw32_add(float a, float b) { auto s = __builtin_amdgcn_permlane32_swap(__float_as_uint(a), __float_as_uint(b), false, false); return __uint_as_float(s[0]) + __uint_as_float(s[1]); }
DI float sw16_add(float a, float b) { auto s = __builtin_amdgcn_permlane16_swap(__float_as_uint(a), __float_as_uint(b), false, false); return __uint_as_float(s[0]) + __uint_as_float(s[1]); }
DI float sw32_max(float a) { auto s = __builtin_amdgcn_permlane32_swap(__float_as_uint(a), __float_as_uint(a), false, false); return fmaxf(__uint_as_float(s[0]), __uint_as_float(s[1])); }
DI float sw16_max(float a) { auto s = __builtin_amdgcn_permlane16_swap(__float_as_uint(a), __float_as_uint(a), false, false); return fmaxf(__uint_as_float(s[0]), __uint_as_float(s[1])); }
DI u32 pk2(float lo, float hi) { f32x2 p = {lo, hi}; return __builtin_bit_cast(u32, __builtin_convertvector(p, bf16x2)); }
DI float bflo(u32 x) { return __uint_as_float(x << 16); }
DI float bfhi(u32 x) { return __uint_as_float(x & 0xffff0000u); }
DI f32x4 mfma16(v4u a, v4u b, f32x4 c) { return __builtin_amdgcn_mfma_f32_16x16x32_bf16(__builtin_bit_cast(bf16x8, a), __builtin_bit_cast(bf16x8, b), c, 0, 0, 0); }
DI f32x16 mfma32(v4u a, v4u b, f32x16 c) { return __builtin_amdgcn_mfma_f32_32x32x16_bf16(__builtin_bit_cast(bf16x8, a), __builtin_bit_cast(bf16x8, b), c, 0, 0, 0); }

DI v4u lds128(LAS char* L, u32 off) { return *(LAS v4u*)(L + off); }
DI v2u tr64(LAS char* L, u32 off) { return __builtin_bit_cast(v2u, __builtin_amdgcn_ds_read_tr16_b64_v4i16((LAS s16x4*)(L + off))); }
DI v4u tr128(LAS char* L, u32 o0, u32 o1) { v2u a = tr64(L, o0), b = tr64(L, o1); return v4u{a.x, a.y, b.x, b.y}; }

DI void dma4(const void* sbase, u32 v0, u32 v1, u32 v2, u32 v3, u32 lds) {
  asm volatile("s_mov_b32 m0, %5\n\ts_nop 0\n\t"
               "global_load_lds_dwordx4 %0, %4 nt\n\t"
               "global_load_lds_dwordx4 %1, %4 offset:1024 nt\n\t"
               "global_load_lds_dwordx4 %2, %4 offset:2048 nt\n\t"
               "global_load_lds_dwordx4 %3, %4 offset:3072 nt"
               ::"v"(v0), "v"(v1), "v"(v2), "v"(v3), "s"(sbase), "s"(lds) : "memory");
}
DI void dma2a(const void* sbase, u32 v0, u32 v1, u32 lds) {
  asm volatile("s_mov_b32 m0, %3\n\ts_nop 0\n\t"
               "global_load_lds_dwordx4 %0, %2 nt\n\t"
               "global_load_lds_dwordx4 %1, %2 offset:1024 nt"
               ::"v"(v0), "v"(v1), "s"(sbase), "s"(lds) : "memory");
}
DI void dma2b(const void* sbase, u32 v2, u32 v3, u32 lds) {
  asm volatile("s_mov_b32 m0, %3\n\ts_nop 0\n\t"
               "global_load_lds_dwordx4 %0, %2 offset:2048 nt\n\t"
               "global_load_lds_dwordx4 %1, %2 offset:3072 nt"
               ::"v"(v2), "v"(v3), "s"(sbase), "s"(lds) : "memory");
}
template <int OFF> DI void ld_sc1(v4u& d, const void* sbase, u32 voff) {
  asm volatile("global_load_dwordx4 %0, %1, %2 offset:%3 sc1" : "=v"(d) : "v"(voff), "s"(sbase), "i"(OFF) : "memory");
}
template <int OFF> DI void ld_plain(v4u& d, const void* sbase, u32 voff) {
  asm volatile("global_load_dwordx4 %0, %1, %2 offset:%3" : "=v"(d) : "v"(voff), "s"(sbase), "i"(OFF) : "memory");
}
DI void st_sc1(void* sbase, u32 voff, v4u d) {
  asm volatile("global_store_dwordx4 %0, %1, %2 sc1\n\ts_nop 1" ::"v"(voff), "v"(d), "s"(sbase) : "memory");
}
#define PIN1(a) asm volatile("" : "+v"(a))
DI u32 tagbad(const v4u& g, u32 ta, u32 tb) { return (g.y ^ ta) | (g.w ^ tb); }
DI bool wave_ok(u32 bad) { return __builtin_amdgcn_ballot_w64(bad != 0u) == 0ull; }

struct Ctx { LAS char* L; char* ws; int lane, w; u32 ta, tb; };

DI void dma_wblock(const char* blk, u32 rstride, u32 lds, int lane, int w) {
  const u32 r = (u32)lane >> 4, c = (u32)lane & 15u;
  const u32 b = (16u * w + r) * rstride;
  const u32 x = c ^ (r << 2);
  dma4(blk, b + 16u * x, b + 4u * rstride - 1024u + 16u * (x ^ 1u), b + 8u * rstride - 2048u + 16u * (x ^ 2u), b + 12u * rstride - 3072u + 16u * (x ^ 3u),
       lds + 4096u * w);
}
DI void read_wfrags(LAS char* L, u32 lds, int lane, int w, v4u (&a)[4]) {
  const u32 g = (u32)lane >> 4, q = ((u32)lane & 15u) >> 2, p = (u32)lane & 3u;
  const u32 ch = 2u * w + (p >> 1);
  const u32 o0 = lds + 256u * (8u * g + q) + 16u * (ch ^ ((q << 2) | ((2u * g) & 3u))) + 8u * (p & 1u);
  const u32 o1 = lds + 256u * (8u * g + 4u + q) + 16u * (ch ^ ((q << 2) | ((2u * g + 1u) & 3u))) + 8u * (p & 1u);
#pragma unroll
  for (int ks = 0; ks < 4; ++ks) a[ks] = tr128(L, o0 + 8192u * ks, o1 + 8192u * ks);
}
template <int MASK> DI void read_bfrags(LAS char* L, u32 img, int lane, v4u (&b)[4]) {
  const u32 o = img + 272u * ((u32)lane & MASK) + 16u * ((u32)lane >> 4);
#pragma unroll
  for (int s = 0; s < 4; ++s) b[s] = lds128(L, o + 64u * s);
}
DI void publish_d(void* dst, f32x4 d, int lane, int w, u32 ta, u32 tb) {
  const u32 col = (u32)lane & 15u, g = (u32)lane >> 4, pr = (col >> 1) & 1u;
  const float v0 = pr ? d[2] : d[0], v1 = pr ? d[3] : d[1];
  if (col < 4u) st_sc1(dst, (col & 1u) * 1024u + (8u * w + 2u * g + pr) * 16u, v4u{__float_as_uint(v0), ta, __float_as_uint(v1), tb});
}
DI f32x4 mv4(const v4u (&a)[4], const v4u (&b)[4]) {
  f32x4 d = {0.f, 0.f, 0.f, 0.f};
#pragma unroll
  for (int s = 0; s < 4; ++s) d = mfma16(a[s], b[s], d);
  return d;
}
DI void gather4(const char* src, LAS char* L, u32 img, int lane, int w, u32 ta, u32 tb) {
  const u32 gi = (16u * w + ((u32)lane >> 2)) & 63u, b = (u32)w >> 2;
  const u32 off = ((u32)lane & 3u) * 2048u + b * 1024u + gi * 16u;
  v4u g;
  for (int it = 0;; ++it) {
    ld_sc1<0>(g, src, off);
    asm volatile("s_waitcnt vmcnt(0)" : "+v"(g)::"memory");
    if (wave_ok(tagbad(g, ta, tb)) || it > SPIN_CAP) break;
    __builtin_amdgcn_s_sleep(1);
  }
  float v0 = __uint_as_float(g.x), v1 = __uint_as_float(g.z);
  v0 += dppf<XOR1>(v0); v1 += dppf<XOR1>(v1);
  v0 += dppf<XOR2>(v0); v1 += dppf<XOR2>(v1);
  *(LAS u32*)(L + img + 272u * b + 4u * gi) = pk2(v0, v1);
}
DI void final_reduce(const char* ws, u16* out, int which, int idx, int lane, int w, u32 ta, u32 tb) {
  const u32 mq = (u32)idx >> 5, sub = (u32)idx & 31u;
  const char* src = ws + WS_OUTP + (which * 4u + mq) * 65536u;
  const u32 off = ((u32)lane >> 1) * 2048u + (u32)w * 1024u + (2u * sub + ((u32)lane & 1u)) * 16u;
  v4u g;
  for (int it = 0;; ++it) {
    ld_sc1<0>(g, src, off);
    asm volatile("s_waitcnt vmcnt(0)" : "+v"(g)::"memory");
    if (wave_ok(tagbad(g, ta, tb)) || it > SPIN_CAP) break;
    __builtin_amdgcn_s_sleep(1);
  }
  float v0 = __uint_as_float(g.x), v1 = __uint_as_float(g.z);
  v0 += dppf<XOR2>(v0); v1 += dppf<XOR2>(v1);
  v0 += dppf<ROR4>(v0); v1 += dppf<ROR4>(v1);
  v0 += dppf<ROR8>(v0); v1 += dppf<ROR8>(v1);
  v0 = sw16_add(v0, v0); v1 = sw16_add(v1, v1);
  v0 = sw32_add(v0, v0); v1 = sw32_add(v1, v1);
  if (lane < 2) *(GAS u32*)((GAS char*)out + ((u32)w * 1024u + which * 512u + 128u * mq + 4u * sub + 2u * lane) * 2u) = pk2(v0, v1);
}

typedef u32 v16u __attribute__((ext_vector_type(16)));
typedef u32 v8u __attribute__((ext_vector_type(8)));
DI const u16* mkp(u32 lo, u32 hi) { return (const u16*)(((u64)hi << 32) | lo); }
__global__ void __launch_bounds__(512, 2)
fused_decode(const u16* p0, const u16* p1, const u16* p2, const u16* p3, const u16* p4, const u16* p5, const u16* p6, const u16* p7,
             const u16* p8, const u16* p9, const u16* p10, u16* p11, char* p12) {
  u64 tt0; asm volatile("s_memrealtime %0\n\ts_waitcnt lgkmcnt(0)" : "=s"(tt0)::"memory");
  v16u ka0; v8u ka1; v2u ka2;
  asm volatile("s_load_dwordx16 %0, %3, 0x0\n\ts_load_dwordx8 %1, %3, 0x40\n\ts_load_dwordx2 %2, %3, 0x60\n\ts_waitcnt lgkmcnt(0)"
               : "=&s"(ka0), "=&s"(ka1), "=&s"(ka2) : "s"(__builtin_amdgcn_kernarg_segment_ptr()) : "memory");
  const u16 *xA = mkp(ka0[0], ka0[1]), *ckv = mkp(ka0[2], ka0[3]), *wq = mkp(ka0[4], ka0[5]), *wuk = mkp(ka0[6], ka0[7]);
  const u16 *wuv = mkp(ka0[8], ka0[9]), *wo = mkp(ka0[10], ka0[11]), *xB = mkp(ka0[12], ka0[13]), *Kc = mkp(ka0[14], ka0[15]);
  const u16 *Vc = mkp(ka1[0], ka1[1]), *wqB = mkp(ka1[2], ka1[3]), *woB = mkp(ka1[4], ka1[5]);
  u16* out = (u16*)mkp(ka1[6], ka1[7]); char* ws = (char*)mkp(ka2[0], ka2[1]);
  __shared__ __attribute__((aligned(16))) char smem[163840];
  LAS char* L = (LAS char*)smem;
  const int lane = threadIdx.x & 63, w = __builtin_amdgcn_readfirstlane(threadIdx.x >> 6);
  const int bid = blockIdx.x, roleB = (bid >> 3) & 1, idx = ((bid >> 4) << 3) | (bid & 7);
  const int hh_ = (idx & 7) + 8 * (idx >> 5), qq_ = (idx >> 3) & 3;
  u32 cal0 = (u32)tt0, calx = 0; u64 c0_; asm volatile("s_memtime %0\n\ts_waitcnt lgkmcnt(0)" : "=s"(c0_)::"memory");
  STAMP(0);
  {
    u64 pc; asm volatile("s_getpc_b64 %0" : "=s"(pc));
    pc &= ~(u64)0x7f;
    if (w >= 5) {
      asm volatile("s_mov_b32 m0, %2\n\ts_nop 0\n\tglobal_load_lds_dword %0, %1"
                   ::"v"((u32)lane * 128u + (u32)(w - 5) * 8192u), "s"(pc), "s"(L_S3 + 4096u * (u32)w) : "memory");
    } else {
      asm volatile("s_mov_b32 m0, %2\n\ts_nop 0\n\tglobal_load_lds_dword %0, %1" ::"v"(0u), "s"(pc), "s"(L_S3 + 4096u * (u32)w) : "memory");
    }
  }
  const u32 xo = ((u32)lane & 1u) * 1024u + (u32)qq_ * 256u + ((u32)lane >> 4) * 16u;
#define LOAD_EX(x) \
  u32 ev; v4u xf[4]; \
  asm volatile("global_load_dword %0, %1, %2 sc1" : "=v"(ev) : "v"(0u), "s"(ws) : "memory"); \
  ld_plain<0>(xf[0], x, xo); ld_plain<64>(xf[1], x, xo); ld_plain<128>(xf[2], x, xo); ld_plain<192>(xf[3], x, xo);
  asm volatile("" ::"s"(xA), "s"(ckv), "s"(wq), "s"(wuk), "s"(wuv), "s"(wo), "s"(xB), "s"(Kc), "s"(Vc), "s"(wqB), "s"(woB), "s"(out), "s"(ws));
  u32 ta, tb, e0;

  if (!roleB) {
    const int b = (idx & 7) >> 2, sp = (idx >> 3) * 4 + (idx & 3);
    const char* kv = (const char*)ckv + ((size_t)b * 8192 + (size_t)sp * 128 + 4u * w) * 1024u;
    const u32 kx = (16u * lane) ^ (16u * (w & 3));
    const u32 k0 = kx, k1 = kx ^ 64u, k2 = kx ^ 128u, k3 = kx ^ 192u;
    const u32 lw = 4096u * w;
    LOAD_EX(xA)
    dma_wblock((const char*)wq + (size_t)qq_ * 128 * 8192 + hh_ * 256, 8192u, L_S4, lane, w);
    dma2a(kv, k0, k1, L_S0 + lw);
    asm volatile("s_waitcnt vmcnt(2)" : "+v"(ev), "+v"(xf[0]), "+v"(xf[1]), "+v"(xf[2]), "+v"(xf[3])::"memory");
    STAMP(1);
    dma2b(kv, k2, k3, L_S0 + lw);
    e0 = __builtin_amdgcn_readfirstlane(ev);
    ta = e0 * 0x9E3779B1u + 0x7F4A7C15u; tb = (e0 ^ (e0 >> 15)) * 0x85EBCA77u + 0x3C6EF372u;
    BAR();
    {
      v4u a[4]; read_wfrags(L, L_S4, lane, w, a);
      f32x4 d = mv4(a, xf);
      publish_d(ws + WS_QA + ((u32)hh_ * 4u + qq_) * 2048u, d, lane, w, ta, tb);
    }
    STAMP(2);
    dma4(kv + 32768, k0, k1, k2, k3, L_S1 + lw);
    dma4(kv + 65536, k0, k1, k2, k3, L_S2 + lw);
    const int kt = w & 3, ch = w >> 2;
    v4u ka[16], vt[8][2];
    const u32 r31 = (u32)lane & 31u, hf = (u32)lane >> 5;
    const u32 fr = ((r31 & 3u) << 2) | ((r31 >> 2) & 3u);
    const u32 rowo = 1024u * r31 + 16u * (hf ^ fr) + 512u * ch;
    const u32 blk = ((u32)lane >> 4) & 1u, q4 = ((u32)lane & 15u) >> 2, p4 = (u32)lane & 3u;
    const u32 chx = 8u * w + 2u * blk + (p4 >> 1);
    const u32 tro0 = 1024u * (8u * hf + q4) + 16u * (chx ^ ((q4 << 2) | (2u * hf))) + 8u * (p4 & 1u);
    const u32 tro1 = 1024u * (8u * hf + 4u + q4) + 16u * (chx ^ ((q4 << 2) | (2u * hf + 1u))) + 8u * (p4 & 1u);
#define READ_TILE(t)                                                                                          \
    {                                                                                                         \
      if (kt == (t)) {                                                                                        \
        _Pragma("unroll") for (int s = 0; s < 16; ++s) ka[s] = lds128(L, 32768u * (t) + ((rowo ^ (32u * (s & 7))) + 256u * (s >> 3))); \
      }                                                                                                       \
      _Pragma("unroll") for (int ks = 0; ks < 2; ++ks)                                                        \
        _Pragma("unroll") for (int u = 0; u < 2; ++u)                                                         \
          vt[2 * (t) + ks][u] = tr128(L, 32768u * (t) + 16384u * ks + (tro0 ^ (64u * u)), 32768u * (t) + 16384u * ks + (tro1 ^ (64u * u))); \
    }
#pragma unroll
    for (int s = 0; s < 16; ++s) ka[s] = v4u{0u, 0u, 0u, 0u};
    WAITVM(8); BAR(); STAMP(3);
    READ_TILE(0)
    dma4(kv + 98304, k0, k1, k2, k3, L_S3 + lw);
    WAITVM(8); BAR(); STAMP(4);
    READ_TILE(1)
    v4u g[8];
    const char* qsrc = ws + WS_QABS + (u32)b * 65536u + (u32)w * 8192u;
    const u32 go = 16u * lane;
#define GATHER_Q() { ld_sc1<0>(g[0], qsrc, go); ld_sc1<1024>(g[1], qsrc, go); ld_sc1<2048>(g[2], qsrc, go); ld_sc1<3072>(g[3], qsrc, go); \
                     ld_sc1<0>(g[4], qsrc + 4096, go); ld_sc1<1024>(g[5], qsrc + 4096, go); ld_sc1<2048>(g[6], qsrc + 4096, go); ld_sc1<3072>(g[7], qsrc + 4096, go); }
    GATHER_Q()
    WAITVM(12); BAR(); STAMP(5);
    READ_TILE(2)
    WAITVM(8); BAR(); STAMP(6);
    READ_TILE(3)
#define WAIT_Q() asm volatile("s_waitcnt vmcnt(0)" : "+v"(g[0]), "+v"(g[1]), "+v"(g[2]), "+v"(g[3]), "+v"(g[4]), "+v"(g[5]), "+v"(g[6]), "+v"(g[7])::"memory")
#define BAD_Q() (tagbad(g[0], ta, tb) | tagbad(g[1], ta, tb) | tagbad(g[2], ta, tb) | tagbad(g[3], ta, tb) | tagbad(g[4], ta, tb) | tagbad(g[5], ta, tb) | tagbad(g[6], ta, tb) | tagbad(g[7], ta, tb))
    WAIT_Q();
    if (!wave_ok(BAD_Q())) {
      for (int it = 0; it < SPIN_CAP; ++it) {
        __builtin_amdgcn_s_sleep(2);
        GATHER_Q()
        WAIT_Q();
        STAMPV(30, it + 1);
        if (wave_ok(BAD_Q())) break;
      }
    }
    STAMP(7);
#pragma unroll
    for (int i = 0; i < 8; ++i) {
      const u32 gi = 64u * (i & 1) + lane, f = ((u32)(i >> 1) << 2) | (u32)(w & 3);
      *(LAS v2u*)(L + L_S4 + 1024u * (4u * w + (i >> 1)) + 16u * ((gi >> 1) ^ f) + 8u * (gi & 1u)) = v2u{g[i].x, g[i].z};
    }
    BAR();
    STAMP(8);
    dma_wblock((const char*)wuv + (size_t)qq_ * 128 * 8192 + hh_ * 256, 8192u, L_S2, lane, w);
    {
      f32x16 acc;
#pragma unroll
      for (int i = 0; i < 16; ++i) acc[i] = 0.f;
#pragma unroll
      for (int s = 0; s < 16; ++s) acc = mfma32(ka[s], lds128(L, L_S4 + ((rowo ^ (32u * (s & 7))) + 256u * (s >> 3))), acc);
      const u32 so = L_S0 + 16896u * ch + 528u * r31 + 128u * kt + 16u * hf;
#pragma unroll
      for (int a = 0; a < 4; ++a) *(LAS f32x4*)(L + so + 32u * a) = f32x4{acc[4 * a], acc[4 * a + 1], acc[4 * a + 2], acc[4 * a + 3]};
    }
    BAR();
    STAMP(9);
    constexpr u32 L_P = L_S1 + 16384;
    float m2, lsum;
    {
      const u32 hd = 4u * w + ((u32)lane >> 4), j = (u32)lane & 15u;
      const u32 so = L_S0 + 528u * hd + 32u * j;
      const f32x4 s0 = *(LAS f32x4*)(L + so) + *(LAS f32x4*)(L + so + 16896u);
      const f32x4 s1 = *(LAS f32x4*)(L + so + 16u) + *(LAS f32x4*)(L + so + 16896u + 16u);
      float m = fmaxf(fmaxf(fmaxf(s0[0], s0[1]), fmaxf(s0[2], s0[3])), fmaxf(fmaxf(s1[0], s1[1]), fmaxf(s1[2], s1[3])));
      m = fmaxf(m, dppf<XOR1>(m)); m = fmaxf(m, dppf<XOR2>(m)); m = fmaxf(m, dppf<ROR4>(m)); m = fmaxf(m, dppf<ROR8>(m));
      m2 = m * SC2;
      float p[8];
#pragma unroll
      for (int i = 0; i < 4; ++i) { p[i] = __builtin_amdgcn_exp2f(fmaf(s0[i], SC2, -m2)); p[4 + i] = __builtin_amdgcn_exp2f(fmaf(s1[i], SC2, -m2)); }
      *(LAS v4u*)(L + L_P + 256u * hd + 16u * (j ^ (hd & 15u))) = v4u{pk2(p[0], p[1]), pk2(p[2], p[3]), pk2(p[4], p[5]), pk2(p[6], p[7])};
      float l = ((p[0] + p[1]) + (p[2] + p[3])) + ((p[4] + p[5]) + (p[6] + p[7]));
      l += dppf<XOR1>(l); l += dppf<XOR2>(l); l += dppf<ROR4>(l); l += dppf<ROR8>(l);
      lsum = l;
    }
    BAR();
    STAMP(10);
    {
      f32x16 o0, o1;
#pragma unroll
      for (int i = 0; i < 16; ++i) { o0[i] = 0.f; o1[i] = 0.f; }
      const u32 po = L_P + 256u * r31 + 16u * (hf ^ (r31 & 15u));
#pragma unroll
      for (int s = 0; s < 8; ++s) {
        const v4u pf = lds128(L, po ^ (32u * s));
        o0 = mfma32(vt[s][0], pf, o0);
        o1 = mfma32(vt[s][1], pf, o1);
      }
      const u32 sb = L_S3 + lw + 128u * r31 + 8u * hf, sx = 16u * (r31 & 7u);
#pragma unroll
      for (int a = 0; a < 4; ++a) {
        *(LAS v2u*)(L + sb + ((16u * a) ^ sx)) = v2u{pk2(o0[4 * a], o0[4 * a + 1]), pk2(o0[4 * a + 2], o0[4 * a + 3])};
        *(LAS v2u*)(L + sb + ((64u + 16u * a) ^ sx)) = v2u{pk2(o1[4 * a], o1[4 * a + 1]), pk2(o1[4 * a + 2], o1[4 * a + 3])};
      }
      const u32 rr = (u32)lane >> 3, kk = (u32)lane & 7u;
      char* od = ws + WS_OA + (((u32)b * 32u * 4u + (u32)(w >> 1)) * 64u + sp) * 256u + (u32)(w & 1) * 128u;
      v4u t[4];
#pragma unroll
      for (int i = 0; i < 4; ++i) t[i] = lds128(L, L_S3 + lw + 1024u * i + 128u * rr + 16u * (kk ^ rr));
#pragma unroll
      for (int i = 0; i < 4; ++i) st_sc1(od, (8u * i + rr) * 65536u + 16u * kk, t[i]);
    }
    STAMP(11);
    WAITVM(0); BAR();
    STAMP(12);
    if ((lane & 15) == 0)
      st_sc1(ws + WS_MLA + (u32)b * 32768u + (u32)sp * 16u, (4u * w + ((u32)lane >> 4)) * 1024u, v4u{__float_as_uint(m2), ta, __float_as_uint(lsum), tb});
    v4u wa[4];
    read_wfrags(L, L_S2, lane, w, wa);
    constexpr u32 L_IMG = L_S4;
    {
      const u32 bb = (u32)w >> 2, cs = (u32)w & 3u;
      const u32 rec = ((u32)lane & 3u) | (((u32)lane >> 4) << 2), chk = ((u32)lane >> 2) & 3u;
      const char* fsrc = ws + WS_MLA + (bb * 32u + hh_) * 1024u;
      v4u f[4];
      for (int it = 0;; ++it) {
        ld_sc1<0>(f[0], fsrc, rec * 16u); ld_sc1<256>(f[1], fsrc, rec * 16u); ld_sc1<512>(f[2], fsrc, rec * 16u); ld_sc1<768>(f[3], fsrc, rec * 16u);
        asm volatile("s_waitcnt vmcnt(0)" : "+v"(f[0]), "+v"(f[1]), "+v"(f[2]), "+v"(f[3])::"memory");
        if (wave_ok(tagbad(f[0], ta, tb) | tagbad(f[1], ta, tb) | tagbad(f[2], ta, tb) | tagbad(f[3], ta, tb)) || it > SPIN_CAP) break;
        __builtin_amdgcn_s_sleep(1);
      }
      STAMP(13);
      const char* psrc = ws + WS_OA + ((bb * 32u + hh_) * 4u + qq_) * 16384u + cs * 64u;
      const u32 po = rec * 256u + chk * 16u;
      v4u d[4];
      ld_sc1<0>(d[0], psrc, po); ld_sc1<0>(d[1], psrc + 4096, po); ld_sc1<0>(d[2], psrc + 8192, po); ld_sc1<0>(d[3], psrc + 12288, po);
      dma_wblock((const char*)wo + (size_t)hh_ * 128 * 1024 + qq_ * 256, 1024u, L_S0, lane, w);
      float M = fmaxf(fmaxf(__uint_as_float(f[0].x), __uint_as_float(f[1].x)), fmaxf(__uint_as_float(f[2].x), __uint_as_float(f[3].x)));
      M = fmaxf(M, dppf<XOR1>(M)); M = fmaxf(M, dppf<XOR2>(M)); M = sw16_max(M); M = sw32_max(M);
      float e[4], Ls = 0.f;
#pragma unroll
      for (int i = 0; i < 4; ++i) { e[i] = __builtin_amdgcn_exp2f(__uint_as_float(f[i].x) - M); Ls = fmaf(e[i], __uint_as_float(f[i].z), Ls); }
      Ls += dppf<XOR1>(Ls); Ls += dppf<XOR2>(Ls); Ls = sw16_add(Ls, Ls); Ls = sw32_add(Ls, Ls);
      const float inv = __builtin_amdgcn_rcpf(Ls);
      asm volatile("s_waitcnt vmcnt(4)" : "+v"(d[0]), "+v"(d[1]), "+v"(d[2]), "+v"(d[3])::"memory");
      STAMP(14);
      float a[8];
#pragma unroll
      for (int k = 0; k < 8; ++k) a[k] = 0.f;
#pragma unroll
      for (int i = 0; i < 4; ++i) {
        const float wi = e[i] * inv;
#pragma unroll
        for (int k = 0; k < 4; ++k) { a[2 * k] = fmaf(wi, bflo(d[i][k]), a[2 * k]); a[2 * k + 1] = fmaf(wi, bfhi(d[i][k]), a[2 * k + 1]); }
      }
      const float r0 = sw32_add(a[0], a[4]), r1 = sw32_add(a[1], a[5]), r2 = sw32_add(a[2], a[6]), r3 = sw32_add(a[3], a[7]);
      float u0 = sw16_add(r0, r2), u1 = sw16_add(r1, r3);
      u0 += dppf<XOR1>(u0); u1 += dppf<XOR1>(u1); u0 += dppf<XOR2>(u0); u1 += dppf<XOR2>(u1);
      const u32 hi = pk2(u0, u1), lo = pk2(u0 - bflo(hi), u1 - bfhi(hi));
      const u32 cl = 32u * cs + 8u * chk + 4u * ((u32)lane >> 5) + 2u * (((u32)lane >> 4) & 1u);
      *(LAS u32*)(L + L_IMG + 272u * (bb + 2u * ((u32)lane & 1u)) + 2u * cl) = (lane & 1) ? lo : hi;
    }
    BAR();
    {
      v4u bf[4]; read_bfrags<3>(L, L_IMG, lane, bf);
      f32x4 d = mv4(wa, bf);
#pragma unroll
      for (int i = 0; i < 4; ++i) d[i] += dppf<XOR2>(d[i]);
      publish_d(ws + WS_CTX + ((u32)hh_ * 4u + qq_) * 2048u, d, lane, w, ta, tb);
    }
    STAMP(15);
    gather4(ws + WS_CTX + (u32)hh_ * 8192u, L, L_IMG + 2048u, lane, w, ta, tb);
    STAMP(16);
    BAR();
    {
      v4u a[4], bf[4];
      read_wfrags(L, L_S0, lane, w, a);
      read_bfrags<1>(L, L_IMG + 2048u, lane, bf);
      publish_d(ws + WS_OUTP + ((u32)qq_ * 32u + hh_) * 2048u, mv4(a, bf), lane, w, ta, tb);
    }
    STAMP(17);
    if (w < 2) {
      final_reduce(ws, out, 0, idx, lane, w, ta, tb);
      if (idx == 0 && w == 0 && lane == 0) __hip_atomic_store((GAS u32*)(ws + WS_E), e0 + 1u, __ATOMIC_RELAXED, __HIP_MEMORY_SCOPE_AGENT);
    }
    STAMP(18);
  } else {
    const u32 r4 = (u32)lane >> 4, c16 = (u32)lane & 15u;
    LOAD_EX(xB)
    {
      const u32 bs = (16u * w + r4) * 8192u, x = c16 ^ r4;
      dma4((const char*)wuk + (size_t)qq_ * 128 * 8192 + hh_ * 256, bs + 16u * x, bs + 32768u - 1024u + 16u * (x ^ 4u), bs + 65536u - 2048u + 16u * (x ^ 8u),
           bs + 98304u - 3072u + 16u * (x ^ 12u), L_S0 + 4096u * w);
    }
    asm volatile("s_waitcnt vmcnt(0)" : "+v"(ev), "+v"(xf[0]), "+v"(xf[1]), "+v"(xf[2]), "+v"(xf[3])::"memory");
    STAMP(1);
    e0 = __builtin_amdgcn_readfirstlane(ev);
    ta = e0 * 0x9E3779B1u + 0x7F4A7C15u; tb = (e0 ^ (e0 >> 15)) * 0x85EBCA77u + 0x3C6EF372u;
    constexpr u32 L_IMG = L_S1;
    v4u uk[4];
    {
      const u32 o = L_S0 + 4096u * w + 256u * c16 + 16u * (r4 ^ c16);
#pragma unroll
      for (int s = 0; s < 4; ++s) uk[s] = lds128(L, o ^ (64u * s));
    }
    gather4(ws + WS_QA + (u32)hh_ * 8192u, L, L_IMG, lane, w, ta, tb);
    STAMP(2);
    dma_wblock((const char*)wqB + (size_t)qq_ * 128 * 8192 + hh_ * 256, 8192u, L_S4, lane, w);
    BAR();
    {
      v4u bf[4]; read_bfrags<1>(L, L_IMG, lane, bf);
      const f32x4 d = mv4(uk, bf);
      if (c16 < 2u)
        st_sc1(ws + WS_QABS + (u32)hh_ * 2048u + (32u * qq_ + 4u * w) * 16u, c16 * 65536u + r4 * 16u, v4u{pk2(d[0], d[1]), ta, pk2(d[2], d[3]), tb});
    }
    STAMP(3);
    WAITVM(0); BAR();
    STAMP(4);
    {
      v4u a[4]; read_wfrags(L, L_S4, lane, w, a);
      publish_d(ws + WS_QB + ((u32)hh_ * 4u + qq_) * 2048u, mv4(a, xf), lane, w, ta, tb);
    }
    STAMP(5);
    const int b = idx >> 6, kvh = (idx >> 3) & 7, j = idx & 7;
    const size_t kvo = (((size_t)b * 8 + kvh) * 8192 + 6144 + 256u * j + 32u * w) * 256u;
    const u32 kb = L_S0 + 8192u * w, vb = L_S2 + 8192u * w;
    {
      const u32 x = c16 ^ r4, bs = 256u * r4;
      dma4((const char*)Kc + kvo, bs + 16u * x, bs + 16u * (x ^ 4u), bs + 16u * (x ^ 8u), bs + 16u * (x ^ 12u), kb);
      dma4((const char*)Kc + kvo + 4096, bs + 16u * x, bs + 16u * (x ^ 4u), bs + 16u * (x ^ 8u), bs + 16u * (x ^ 12u), kb + 4096u);
    }
    v4u g[2];
    const char* qsrc = ws + WS_QB + (4u * kvh + (u32)(w >> 1)) * 8192u + (u32)b * 1024u + (u32)(w & 1) * 512u;
    const u32 go = ((u32)lane & 3u) * 2048u + ((u32)lane >> 2) * 16u;
    ld_sc1<0>(g[0], qsrc, go); ld_sc1<256>(g[1], qsrc, go);
    {
      const u32 bs = 256u * r4 + 16u * (c16 & 1u), x = (c16 >> 1) ^ r4;
      dma4((const char*)Vc + kvo, bs + 32u * x, bs + 32u * (x ^ 4u), bs + 32u * x, bs + 32u * (x ^ 4u), vb);
      dma4((const char*)Vc + kvo + 4096, bs + 32u * x, bs + 32u * (x ^ 4u), bs + 32u * x, bs + 32u * (x ^ 4u), vb + 4096u);
    }
    asm volatile("s_waitcnt vmcnt(8)" : "+v"(g[0]), "+v"(g[1])::"memory");
    STAMP(6);
    v4u kf[2][4];
    {
      const u32 o = kb + 256u * c16 + 16u * (r4 ^ c16);
#pragma unroll
      for (int t = 0; t < 2; ++t)
#pragma unroll
        for (int s = 0; s < 4; ++s) kf[t][s] = lds128(L, (o ^ (64u * s)) + 4096u * t);
    }
    for (int it = 0;; ++it) {
      if (wave_ok(tagbad(g[0], ta, tb) | tagbad(g[1], ta, tb)) || it > SPIN_CAP) { STAMPV(30, it); break; }
      __builtin_amdgcn_s_sleep(1);
      ld_sc1<0>(g[0], qsrc, go); ld_sc1<256>(g[1], qsrc, go);
      asm volatile("s_waitcnt vmcnt(0)" : "+v"(g[0]), "+v"(g[1])::"memory");
    }
    constexpr u32 L_IMG2 = L_S4;
    BAR();
#pragma unroll
    for (int i = 0; i < 2; ++i) {
      float v0 = __uint_as_float(g[i].x), v1 = __uint_as_float(g[i].z);
      v0 += dppf<XOR1>(v0); v1 += dppf<XOR1>(v1); v0 += dppf<XOR2>(v0); v1 += dppf<XOR2>(v1);
      *(LAS u32*)(L + L_IMG2 + 272u * (u32)(w >> 1) + 4u * ((u32)(w & 1) * 32u + 16u * i + ((u32)lane >> 2))) = pk2(v0, v1);
    }
    BAR();
    STAMP(7);
    v4u pb; float m2, lsum;
    {
      v4u bf[4]; read_bfrags<3>(L, L_IMG2, lane, bf);
      const f32x4 s0 = mv4(kf[0], bf), s1 = mv4(kf[1], bf);
      float m = fmaxf(fmaxf(fmaxf(s0[0], s0[1]), fmaxf(s0[2], s0[3])), fmaxf(fmaxf(s1[0], s1[1]), fmaxf(s1[2], s1[3])));
      m = sw16_max(m); m = sw32_max(m);
      m2 = m * SC2;
      float p[8];
#pragma unroll
      for (int i = 0; i < 4; ++i) { p[i] = __builtin_amdgcn_exp2f(fmaf(s0[i], SC2, -m2)); p[4 + i] = __builtin_amdgcn_exp2f(fmaf(s1[i], SC2, -m2)); }
      pb = v4u{pk2(p[0], p[1]), pk2(p[2], p[3]), pk2(p[4], p[5]), pk2(p[6], p[7])};
      float l = ((p[0] + p[1]) + (p[2] + p[3])) + ((p[4] + p[5]) + (p[6] + p[7]));
      l = sw16_add(l, l); lsum = sw32_add(l, l);
    }
    BAR();
    dma_wblock((const char*)woB + (size_t)hh_ * 128 * 1024 + qq_ * 256, 1024u, L_S4, lane, w);
    WAITVM(4);
    STAMP(8);
    {
      const u32 q4 = c16 >> 2, p4 = (u32)lane & 3u, rw = 4u * r4 + q4;
      const u32 o = vb + 256u * rw + 32u * (rw & 7u) + 8u * p4;
#pragma unroll
      for (int c = 0; c < 8; ++c) {
        const v4u a = tr128(L, o ^ (32u * c), (o ^ (32u * c)) + 4096u);
        const f32x4 d = mfma16(a, pb, f32x4{0.f, 0.f, 0.f, 0.f});
        if (c16 < 4u) *(LAS f32x4*)(L + kb + 528u * c16 + 64u * c + 16u * r4) = d;
      }
      if (lane < 4) *(LAS f32x2*)(L + kb + 4096u + 8u * lane) = f32x2{m2, lsum};
    }
    BAR();
    STAMP(9);
    if (w < 4) {
      const f32x2 ml = *(LAS f32x2*)(L + L_S0 + 8192u * ((u32)lane & 7u) + 4096u + 8u * w);
      float M = ml[0];
      M = fmaxf(M, dppf<XOR1>(M)); M = fmaxf(M, dppf<XOR2>(M)); M = fmaxf(M, dppf<HMIR>(M));
      const float e = __builtin_amdgcn_exp2f(ml[0] - M);
      float Ls = e * ml[1];
      Ls += dppf<XOR1>(Ls); Ls += dppf<XOR2>(Ls); Ls += dppf<HMIR>(Ls);
      float o0 = 0.f, o1 = 0.f;
#pragma unroll
      for (int v = 0; v < 8; ++v) {
        const float ev_ = __builtin_bit_cast(float, __builtin_amdgcn_readlane(__builtin_bit_cast(int, e), v));
        const f32x2 ov = *(LAS f32x2*)(L + L_S0 + 8192u * v + 528u * w + 8u * lane);
        o0 = fmaf(ev_, ov[0], o0); o1 = fmaf(ev_, ov[1], o1);
      }
      const u32 rec = ((u32)b * 32u + 4u * kvh + w) * 8u + j;
      st_sc1(ws + WS_OB + rec * 1024u, 16u * lane, v4u{__float_as_uint(o0), ta, __float_as_uint(o1), tb});
      if (lane == 0) st_sc1(ws + WS_MLB + rec * 16u, 0u, v4u{__float_as_uint(M), ta, __float_as_uint(Ls), tb});
    }
    STAMP(10);
    constexpr u32 L_IMG3 = L_S2;
    {
      const u32 bb = (u32)w >> 2, jj = ((u32)lane & 3u) | (((u32)lane >> 5) << 2), gl = ((u32)lane >> 2) & 7u;
      const char* osrc = ws + WS_OB + (bb * 32u + hh_) * 8192u + (u32)(w & 3) * 256u;
      const char* msrc = ws + WS_MLB + (bb * 32u + hh_) * 128u;
      v4u d0, d1, f;
      for (int it = 0;; ++it) {
        ld_sc1<0>(d0, osrc, jj * 1024u + gl * 16u); ld_sc1<128>(d1, osrc, jj * 1024u + gl * 16u); ld_sc1<0>(f, msrc, jj * 16u);
        asm volatile("s_waitcnt vmcnt(0)" : "+v"(d0), "+v"(d1), "+v"(f)::"memory");
        if (wave_ok(tagbad(d0, ta, tb) | tagbad(d1, ta, tb) | tagbad(f, ta, tb)) || it > SPIN_CAP) break;
        __builtin_amdgcn_s_sleep(1);
      }
      STAMP(11);
      float M = __uint_as_float(f.x);
      M = fmaxf(M, dppf<XOR1>(M)); M = fmaxf(M, dppf<XOR2>(M)); M = sw32_max(M);
      const float e = __builtin_amdgcn_exp2f(__uint_as_float(f.x) - M);
      float Ls = e * __uint_as_float(f.z);
      Ls += dppf<XOR1>(Ls); Ls += dppf<XOR2>(Ls); Ls = sw32_add(Ls, Ls);
      const float wi = e * __builtin_amdgcn_rcpf(Ls);
      float v0 = sw32_add(wi * __uint_as_float(d0.x), wi * __uint_as_float(d1.x));
      float v1 = sw32_add(wi * __uint_as_float(d0.z), wi * __uint_as_float(d1.z));
      v0 += dppf<XOR1>(v0); v1 += dppf<XOR1>(v1); v0 += dppf<XOR2>(v0); v1 += dppf<XOR2>(v1);
      *(LAS u32*)(L + L_IMG3 + 272u * bb + 4u * ((u32)(w & 3) * 16u + ((u32)lane >> 5) * 8u + gl)) = pk2(v0, v1);
    }
    BAR();
    {
      v4u a[4], bf[4];
      read_wfrags(L, L_S4, lane, w, a);
      read_bfrags<1>(L, L_IMG3, lane, bf);
      publish_d(ws + WS_OUTP + ((4u + qq_) * 32u + hh_) * 2048u, mv4(a, bf), lane, w, ta, tb);
    }
    STAMP(17);
    if (w < 2) final_reduce(ws, out, 1, idx, lane, w, ta, tb);
    STAMP(18);
  }
  if (bid == 0 && threadIdx.x == 0) *(volatile GAS u32*)(ws + (13u << 20)) = cal0;
}

__global__ void stampA(char* ws) { u64 t_; asm volatile("s_memrealtime %0\n\ts_waitcnt lgkmcnt(0)" : "=s"(t_)::"memory"); if (threadIdx.x == 0 && blockIdx.x == 0) *(volatile u32*)(ws + (13u << 20)) = (u32)t_; }
__global__ void spinB(char* ws) { const u32 ref = *(volatile u32*)(ws + (13u << 20));
  for (;;) { u64 t_; asm volatile("s_memrealtime %0\n\ts_waitcnt lgkmcnt(0)" : "=s"(t_)::"memory"); if ((u32)t_ - ref >= 4000u) break; __builtin_amdgcn_s_sleep(1); } }
extern "C" void kernel_launch(void* const* d_in, const int* in_sizes, int n_in, void* d_out, int out_size, void* d_ws, size_t ws_size,
                              hipStream_t stream) {
  fused_decode<<<256, 512, 0, stream>>>((const u16*)d_in[0], (const u16*)d_in[1], (const u16*)d_in[2], (const u16*)d_in[3], (const u16*)d_in[4],
                                        (const u16*)d_in[5], (const u16*)d_in[6], (const u16*)d_in[7], (const u16*)d_in[8], (const u16*)d_in[9],
                                        (const u16*)d_in[10], (u16*)d_out, (char*)d_ws);
  spinB<<<256,64,0,stream>>>((char*)d_ws);
}
```

```cpp
#define A_DELAY 12
#define G_NAP 16
#define NTS ""
#include <hip/hip_runtime.h>
#include <stdint.h>

typedef unsigned u32;
typedef unsigned long long u64;
typedef unsigned short u16;
typedef u32 v4u __attribute__((ext_vector_type(4)));
typedef u32 v2u __attribute__((ext_vector_type(2)));
typedef float f32x2 __attribute__((ext_vector_type(2)));
typedef float f32x4 __attribute__((ext_vector_type(4)));
typedef float f32x16 __attribute__((ext_vector_type(16)));
typedef __bf16 bf16x8 __attribute__((ext_vector_type(8)));
typedef __bf16 bf16x2 __attribute__((ext_vector_type(2)));
typedef short s16x4 __attribute__((ext_vector_type(4)));
#define GAS __attribute__((address_space(1)))
#define LAS __attribute__((address_space(3)))
#define DI __device__ __forceinline__

constexpr u32 WS_E = 0;
constexpr u32 WS_QA = 4096;
constexpr u32 WS_QB = WS_QA + 262144;
constexpr u32 WS_QABS = WS_QB + 262144;
constexpr u32 WS_QCK = WS_QABS + 65536;
constexpr u32 WS_MLA = WS_QABS + 131072;
constexpr u32 WS_CTX = WS_MLA + 65536;
constexpr u32 WS_OB = WS_CTX + 262144;
constexpr u32 WS_MLB = WS_OB + 524288;
constexpr u32 WS_OUTP = WS_MLB + 65536;
constexpr u32 WS_OA = 3u << 20;
constexpr u32 WS_DBG = 8u << 20;
static_assert(WS_OUTP + 524288 <= WS_OA, "ws map");

constexpr float SC2 = 0.08838834764831845f * 1.4426950408889634f;
constexpr int SPIN_CAP = 1 << 17;

constexpr u32 L_S0 = 0, L_S1 = 32768, L_S2 = 65536, L_S3 = 98304, L_S4 = 131072;

#define STAMP(k)
#define STAMPV(k, v)

#define WAITVM(n) asm volatile("s_waitcnt vmcnt(%0)" ::"i"(n) : "memory")
#define BAR() asm volatile("s_waitcnt lgkmcnt(0)\n\ts_barrier" ::: "memory")

template <int CTRL> DI float dppf(float x) {
  return __builtin_bit_cast(float, __builtin_amdgcn_mov_dpp(__builtin_bit_cast(int, x), CTRL, 0xf, 0xf, true));
}
constexpr int XOR1 = 0xB1, XOR2 = 0x4E, HMIR = 0x141, ROR4 = 0x124, ROR8 = 0x128;
DI float sw32_add(float a, float b) { auto s = __builtin_amdgcn_permlane32_swap(__float_as_uint(a), __float_as_uint(b), false, false); return __uint_as_float(s[0]) + __uint_as_float(s[1]); }
DI float sw16_add(float a, float b) { auto s = __builtin_amdgcn_permlane16_swap(__float_as_uint(a), __float_as_uint(b), false, false); return __uint_as_float(s[0]) + __uint_as_float(s[1]); }
DI float sw32_max(float a) { auto s = __builtin_amdgcn_permlane32_swap(__float_as_uint(a), __float_as_uint(a), false, false); return fmaxf(__uint_as_float(s[0]), __uint_as_float(s[1])); }
DI float sw16_max(float a) { auto s = __builtin_amdgcn_permlane16_swap(__float_as_uint(a), __float_as_uint(a), false, false); return fmaxf(__uint_as_float(s[0]), __uint_as_float(s[1])); }
DI u32 pk2(float lo, float hi) { f32x2 p = {lo, hi}; return __builtin_bit_cast(u32, __builtin_convertvector(p, bf16x2)); }
DI float bflo(u32 x) { return __uint_as_float(x << 16); }
DI float bfhi(u32 x) { return __uint_as_float(x & 0xffff0000u); }
DI f32x4 mfma16(v4u a, v4u b, f32x4 c) { return __builtin_amdgcn_mfma_f32_16x16x32_bf16(__builtin_bit_cast(bf16x8, a), __builtin_bit_cast(bf16x8, b), c, 0, 0, 0); }
DI f32x16 mfma32(v4u a, v4u b, f32x16 c) { return __builtin_amdgcn_mfma_f32_32x32x16_bf16(__builtin_bit_cast(bf16x8, a), __builtin_bit_cast(bf16x8, b), c, 0, 0, 0); }

DI v4u lds128(LAS char* L, u32 off) { return *(LAS v4u*)(L + off); }
DI v2u tr64(LAS char* L, u32 off) { return __builtin_bit_cast(v2u, __builtin_amdgcn_ds_read_tr16_b64_v4i16((LAS s16x4*)(L + off))); }
DI v4u tr128(LAS char* L, u32 o0, u32 o1) { v2u a = tr64(L, o0), b = tr64(L, o1); return v4u{a.x, a.y, b.x, b.y}; }

DI void dma4(const void* sbase, u32 v0, u32 v1, u32 v2, u32 v3, u32 lds) {
  asm volatile("s_mov_b32 m0, %5\n\ts_nop 0\n\t"
               "global_load_lds_dwordx4 %0, %4 " NTS "\n\t"
               "global_load_lds_dwordx4 %1, %4 offset:1024 " NTS "\n\t"
               "global_load_lds_dwordx4 %2, %4 offset:2048 " NTS "\n\t"
               "global_load_lds_dwordx4 %3, %4 offset:3072 " NTS
               ::"v"(v0), "v"(v1), "v"(v2), "v"(v3), "s"(sbase), "s"(lds) : "memory");
}
DI void dma2a(const void* sbase, u32 v0, u32 v1, u32 lds) {
  asm volatile("s_mov_b32 m0, %3\n\ts_nop 0\n\t"
               "global_load_lds_dwordx4 %0, %2 " NTS "\n\t"
               "global_load_lds_dwordx4 %1, %2 offset:1024 " NTS
               ::"v"(v0), "v"(v1), "s"(sbase), "s"(lds) : "memory");
}
DI void dma2b(const void* sbase, u32 v2, u32 v3, u32 lds) {
  asm volatile("s_mov_b32 m0, %3\n\ts_nop 0\n\t"
               "global_load_lds_dwordx4 %0, %2 offset:2048 " NTS "\n\t"
               "global_load_lds_dwordx4 %1, %2 offset:3072 " NTS
               ::"v"(v2), "v"(v3), "s"(sbase), "s"(lds) : "memory");
}
template <int I> DI void dma1(const void* sbase, u32 v, u32 lds) {
  asm volatile("s_mov_b32 m0, %2\n\ts_nop 0\n\tglobal_load_lds_dwordx4 %0, %1 offset:%3 " NTS ::"v"(v), "s"(sbase), "s"(lds), "i"(1024 * I) : "memory");
}
template <int OFF> DI void ld_sc1(v4u& d, const void* sbase, u32 voff) {
  asm volatile("global_load_dwordx4 %0, %1, %2 offset:%3 sc1" : "=v"(d) : "v"(voff), "s"(sbase), "i"(OFF) : "memory");
}
template <int OFF> DI void ld_plain(v4u& d, const void* sbase, u32 voff) {
  asm volatile("global_load_dwordx4 %0, %1, %2 offset:%3" : "=v"(d) : "v"(voff), "s"(sbase), "i"(OFF) : "memory");
}
DI void st_sc1(void* sbase, u32 voff, v4u d) {
  asm volatile("global_store_dwordx4 %0, %1, %2 sc1\n\ts_nop 1" ::"v"(voff), "v"(d), "s"(sbase) : "memory");
}
#define PIN1(a) asm volatile("" : "+v"(a))
DI u32 orx(u32 acc, u32 a, u32 b) { return __builtin_amdgcn_bitop3_b32(acc, a, b, 0xF6); }
DI u32 tagbad(const v4u& g, u32 ta, u32 tb, u32 acc = 0u) { return orx(orx(acc, g.y, ta), g.w, tb); }
DI u32 rowsum(u32 x) {
  x += (u32)__builtin_amdgcn_mov_dpp((int)x, 0xB1, 0xf, 0xf, true); x += (u32)__builtin_amdgcn_mov_dpp((int)x, 0x4E, 0xf, 0xf, true);
  x += (u32)__builtin_amdgcn_mov_dpp((int)x, 0x124, 0xf, 0xf, true); x += (u32)__builtin_amdgcn_mov_dpp((int)x, 0x128, 0xf, 0xf, true);
  return x;
}
DI u32 sum4(const v4u& g) { return (g.x + g.y) + (g.z + g.w); }
DI bool wave_ok(u32 bad) { return __builtin_amdgcn_ballot_w64(bad != 0u) == 0ull; }

#define NAP(n) __builtin_amdgcn_s_sleep(n)
#define SB() __builtin_amdgcn_sched_barrier(0)
#define RBAR() asm volatile("s_barrier" ::: "memory")
#define NAP_H1 0
#define NAP_HOP 11
#define NAP_RETRY 6
DI v4u poll1(const char* src, u32 off, u32 ta, u32 tb) {
  v4u g;
  ld_sc1<0>(g, src, off);
  asm volatile("s_waitcnt vmcnt(0)" : "+v"(g)::"memory");
  for (int it = 0; it < SPIN_CAP && !wave_ok(tagbad(g, ta, tb)); ++it) {
    NAP(NAP_RETRY);
    if (tagbad(g, ta, tb)) asm volatile("global_load_dwordx4 %0, %1, %2 sc1\n\ts_waitcnt vmcnt(0)" : "+v"(g) : "v"(off), "s"(src) : "memory");
  }
  return g;
}

DI void dma_wblock(const char* blk, u32 rstride, u32 lds, int lane, int w, u32 row0) {
  const u32 r = (u32)lane >> 4, c = (u32)lane & 15u;
  const u32 b = (row0 + r) * rstride;
  const u32 x = c ^ (r << 2);
  dma4(blk, b + 16u * x, b + 4u * rstride - 1024u + 16u * (x ^ 1u), b + 8u * rstride - 2048u + 16u * (x ^ 2u), b + 12u * rstride - 3072u + 16u * (x ^ 3u),
       lds + 4096u * w);
}
template <int OFF> DI void ld_nt(v4u& d, const void* sbase, u32 voff) {
  asm volatile("global_load_dwordx4 %0, %1, %2 offset:%3 " NTS : "=v"(d) : "v"(voff), "s"(sbase), "i"(OFF) : "memory");
}
DI void reg_wblock(const char* blk, u32 rstride, int lane, u32 row0, v4u (&t)[4]) {
  const u32 o = (row0 + ((u32)lane >> 4)) * rstride + 16u * ((u32)lane & 15u);
  ld_nt<0>(t[0], blk, o); ld_nt<0>(t[1], blk + 4u * rstride, o); ld_nt<0>(t[2], blk + 8u * rstride, o); ld_nt<0>(t[3], blk + 12u * rstride, o);
}
DI void put_wblock(LAS char* L, u32 lds, int lane, int w, const v4u (&t)[4]) {
  const u32 r = (u32)lane >> 4, o = lds + 4096u * w + 256u * r + ((16u * ((u32)lane & 15u)) ^ (64u * r));
#pragma unroll
  for (int i = 0; i < 4; ++i) *(LAS v4u*)(L + (o ^ (16u * i)) + 1024u * i) = t[i];
}
#define PIN4(t) "+v"(t[0]), "+v"(t[1]), "+v"(t[2]), "+v"(t[3])
DI void read_wfrags(LAS char* L, u32 lds, int lane, int w, v4u (&a)[4]) {
  const u32 g = (u32)lane >> 4, q = ((u32)lane & 15u) >> 2, p = (u32)lane & 3u;
  const u32 ch = 2u * w + (p >> 1);
  const u32 o0 = lds + 256u * (8u * g + q) + 16u * (ch ^ ((q << 2) | ((2u * g) & 3u))) + 8u * (p & 1u);
  const u32 o1 = lds + 256u * (8u * g + 4u + q) + 16u * (ch ^ ((q << 2) | ((2u * g + 1u) & 3u))) + 8u * (p & 1u);
#pragma unroll
  for (int ks = 0; ks < 4; ++ks) a[ks] = tr128(L, o0 + 8192u * ks, o1 + 8192u * ks);
}
template <int MASK> DI void read_bfrags(LAS char* L, u32 img, int lane, v4u (&b)[4]) {
  const u32 o = img + 272u * ((u32)lane & MASK) + 16u * ((u32)lane >> 4);
#pragma unroll
  for (int s = 0; s < 4; ++s) b[s] = lds128(L, o + 64u * s);
}
DI void publish_d(void* dst, f32x4 d, int lane, int w, u32 ta, u32 tb) {
  const u32 col = (u32)lane & 15u, g = (u32)lane >> 4, pr = (col >> 1) & 1u;
  const float v0 = pr ? d[2] : d[0], v1 = pr ? d[3] : d[1];
  if (col < 4u) st_sc1(dst, (col & 1u) * 1024u + (8u * w + 2u * g + pr) * 16u, v4u{__float_as_uint(v0), ta, __float_as_uint(v1), tb});
}
DI f32x4 mv4(const v4u (&a)[4], const v4u (&b)[4]) {
  f32x4 d = {0.f, 0.f, 0.f, 0.f};
#pragma unroll
  for (int s = 0; s < 4; ++s) d = mfma16(a[s], b[s], d);
  return d;
}
DI void gather4(const char* src, LAS char* L, u32 img, int lane, int w, u32 ta, u32 tb) {
  const u32 gi = (16u * w + ((u32)lane >> 2)) & 63u, b = (u32)w >> 2;
  const u32 off = ((u32)lane & 3u) * 2048u + b * 1024u + gi * 16u;
  const v4u g = poll1(src, off, ta, tb);
  float v0 = __uint_as_float(g.x), v1 = __uint_as_float(g.z);
  v0 += dppf<XOR1>(v0); v1 += dppf<XOR1>(v1);
  v0 += dppf<XOR2>(v0); v1 += dppf<XOR2>(v1);
  *(LAS u32*)(L + img + 272u * b + 4u * gi) = pk2(v0, v1);
}
DI void final_reduce(const char* ws, u16* out, LAS char* L, u32 scr, int which, int idx, int lane, int w, u32 ta, u32 tb) {
  const u32 mq = (u32)idx >> 3, t = (u32)idx & 7u;
  const char* src = ws + WS_OUTP + (which * 4u + mq) * 65536u + (u32)w * 8192u + t * 128u;
  const v4u g = poll1(src, ((u32)lane >> 3) * 1024u + ((u32)lane & 7u) * 16u, ta, tb);
  float v0 = __uint_as_float(g.x), v1 = __uint_as_float(g.z);
  v0 = sw32_add(v0, v1);
  v0 = sw16_add(v0, v0);
  if (((u32)lane & 16u) == 0u) *(LAS float*)(L + scr + 128u * w + 8u * ((u32)lane & 15u) + 4u * ((u32)lane >> 5)) = v0;
  BAR();
  if (w == 0 && lane < 16) {
    f32x2 a = {0.f, 0.f};
#pragma unroll
    for (int i = 0; i < 8; ++i) a += *(LAS f32x2*)(L + scr + 128u * i + 8u * lane);
    *(GAS u32*)((GAS char*)out + (((u32)lane >> 3) * 1024u + which * 512u + 128u * mq + 16u * t + 2u * ((u32)lane & 7u)) * 2u) = pk2(a[0], a[1]);
  }
}

#define NB1 0
#define G_POS 2
#define A_EARLY 6
#define G_WAIT 0
#define K_NAP 0
#define B_PACE 0
#define K_EARLY 0
#define NAP_H3 11

__global__ void __launch_bounds__(512, 2)
fused_decode(const u16* __restrict__ xA, const u16* __restrict__ ckv, const u16* __restrict__ wq, const u16* __restrict__ wuk,
             const u16* __restrict__ wuv, const u16* __restrict__ wo, const u16* __restrict__ xB, const u16* __restrict__ Kc,
             const u16* __restrict__ Vc, const u16* __restrict__ wqB, const u16* __restrict__ woB, u16* __restrict__ out, char* __restrict__ ws) {
  __shared__ __attribute__((aligned(16))) char smem[163840];
  LAS char* L = (LAS char*)smem;
  const int lane = threadIdx.x & 63, w = __builtin_amdgcn_readfirstlane(threadIdx.x >> 6);
  const int bid = blockIdx.x, roleB = (bid >> 3) & 1, idx = ((bid >> 4) << 3) | (bid & 7);
  const int hh_ = (idx & 7) + 8 * (idx >> 5), qq_ = (idx >> 3) & 3;
  u64 kstart; asm volatile("s_getpc_b64 %0" : "=s"(kstart)); kstart &= ~(u64)0x7f;
  STAMP(0);
  asm volatile("" ::"s"(xA), "s"(ckv), "s"(wq), "s"(wuk), "s"(wuv), "s"(wo), "s"(xB), "s"(Kc), "s"(Vc), "s"(wqB), "s"(woB), "s"(out), "s"(ws));
  const u32 blkoff = (u32)qq_ * (128u * 8192u) + (u32)hh_ * 256u;
  const u32 r4 = (u32)lane >> 4, c16 = (u32)lane & 15u, lw = 4096u * w;
  const int bA = (idx & 7) >> 2, sp = (idx >> 3) * 4 + (idx & 3);
  const char* kv = (const char*)ckv + ((u32)bA * 8192u + (u32)sp * 128u) * 1024u;
  const u32 rsh = roleB ? 13u : 10u, rstep = (4u << rsh) - 1024u;
  const u32 k0 = ((16u * w + r4) << rsh) + ((16u * c16) ^ (64u * r4));
  const u32 k1 = (k0 ^ 16u) + rstep, k2 = (k0 ^ 32u) + 2u * rstep, k3 = (k0 ^ 48u) + 3u * rstep;
  if (A_DELAY && !roleB) NAP(A_DELAY);
  dma4(roleB ? (const char*)wq + blkoff : kv, k0, k1, k2, k3, (roleB ? L_S4 : L_S0) + lw);
#define CODE_PF() { \
    asm volatile("s_mov_b32 m0, %2\n\ts_nop 0\n\tglobal_load_lds_dword %0, %1" \
                 ::"v"(w >= 5 ? (u32)lane * 128u + (u32)(w - 5) * 8192u : 0u), "s"(kstart), "s"(L_S3 + lw) : "memory"); }
  STAMP(25);
  u32 ta, tb, e0;
  const u32 xo = ((u32)lane & 1u) * 1024u + (u32)qq_ * 256u + r4 * 16u;
#define LOAD_E() \
  asm volatile("s_load_dword %0, %1, 0x0\n\ts_waitcnt lgkmcnt(0)" : "=s"(e0) : "s"(ws) : "memory"); \
  ta = e0 * 0x9E3779B1u + 0x7F4A7C15u; tb = (e0 ^ (e0 >> 15)) * 0x85EBCA77u + 0x3C6EF372u;

  if (!roleB) {
    const int b = bA;
    CODE_PF()
    RBAR();
    dma4(kv + 256, k0, k1, k2, k3, L_S1 + lw);
    LOAD_E()
    RBAR();
    dma4(kv + 512, k0, k1, k2, k3, L_S2 + lw);
    RBAR();
    dma4(kv + 768, k0, k1, k2, k3, L_S3 + lw);
    STAMP(1);
    v4u g[4], gc;
    const char* qsrc = ws + WS_QABS + (u32)b * 32768u + lw;
    const char* csrc = ws + WS_QCK + (u32)b * 2048u + (u32)w * 256u;
    const u32 go = 16u * lane, co = 64u * (c16 & 3u) + 16u * r4;
#define GATHER_Q() { ld_sc1<0>(g[0], qsrc, go); ld_sc1<1024>(g[1], qsrc, go); ld_sc1<2048>(g[2], qsrc, go); ld_sc1<3072>(g[3], qsrc, go); ld_sc1<0>(gc, csrc, co); }
#define WAIT_Q(n) asm volatile("s_waitcnt vmcnt(%5)" : PIN4(g), "+v"(gc) : "i"(n) : "memory")
    auto bad_q = [&]() {
      const u32 c0 = rowsum(sum4(g[0])), c1 = rowsum(sum4(g[1])), c2 = rowsum(sum4(g[2])), c3 = rowsum(sum4(g[3]));
      const u32 lo = (c16 & 1u) ? c1 : c0, hi = (c16 & 1u) ? c3 : c2;
      return orx(gc.y ^ tb, gc.x ^ ta, (c16 & 2u) ? hi : lo);
    };
    const int kt = w & 3, ch = w >> 2;
    const u32 r31 = (u32)lane & 31u, hf = (u32)lane >> 5;
    const u32 fr = ((r31 & 3u) << 2) | ((r31 >> 2) & 3u);
    const u32 qo = 256u * r31 + 16u * ((8u * ch + hf) ^ fr);
    const u32 blk = r4 & 1u, q4 = c16 >> 2, p4 = (u32)lane & 3u;
    const u32 chx = 4u * kt + 2u * blk + (p4 >> 1);
    const u32 tro0 = 32768u * ch + 256u * (8u * hf + q4) + 16u * (chx ^ ((q4 << 2) | (2u * hf))) + 8u * (p4 & 1u);
    const u32 tro1 = 32768u * ch + 256u * (8u * hf + 4u + q4) + 16u * (chx ^ ((q4 << 2) | (2u * hf + 1u))) + 8u * (p4 & 1u);
    v4u vt[8][2];
    WAITVM(8); BAR();
    STAMP(2);
#pragma unroll
    for (int ks = 0; ks < 8; ++ks) vt[ks][0] = tr128(L, tro0 + 4096u * ks, tro1 + 4096u * ks);
    u32 qw = L_S4 + 8192u * r4 + 1024u * w + ((16u * c16) ^ (16u * (w & 3)));
    asm volatile("s_waitcnt vmcnt(%1)" : "+v"(qw) : "i"(G_WAIT) : "memory");
    if (G_NAP) NAP(G_NAP);
    STAMP(3);
    GATHER_Q()
    v4u uv[4];
    RBAR();
    reg_wblock((const char*)wuv + (u32)hh_ * 256u, 8192u, lane, 256u * ((w >> 1) & 1) + 64u * qq_ + 32u * (w >> 2) + 16u * (w & 1), uv);
    STAMP(4);
    WAIT_Q(4);
    STAMP(5);
    if (!wave_ok(bad_q())) {
      for (int it = 0; it < SPIN_CAP; ++it) {
        NAP(8);
        GATHER_Q()
        WAIT_Q(0);
        STAMPV(30, it + 1);
        if (wave_ok(bad_q())) break;
      }
    }
    STAMP(6);
#pragma unroll
    for (int i = 0; i < 4; ++i) *(LAS v4u*)(L + (qw ^ (64u * i)) + 256u * i) = g[i];
    BAR();
    STAMP(7);
    f32x16 acc;
#pragma unroll
    for (int i = 0; i < 16; ++i) acc[i] = 0.f;
#define QK_READ(t) _Pragma("unroll") for (int s = 0; s < 4; ++s) { fa[t][s] = lds128(L, 32768u * (t) + 8192u * kt + (qo ^ (32u * s))); fb[t][s] = lds128(L, L_S4 + 8192u * (t) + (qo ^ (32u * s))); }
#define QK_MMA(t) _Pragma("unroll") for (int s = 0; s < 4; ++s) acc = mfma32(fa[t][s], fb[t][s], acc);
    v4u fa[4][4], fb[4][4];
    QK_READ(0) QK_READ(1)
    SB();
    QK_MMA(0)
    QK_READ(2)
    SB();
    QK_MMA(1)
    QK_READ(3)
    SB();
    QK_MMA(2)
    BAR();
    QK_MMA(3)
    {
      const u32 so = L_S0 + 16896u * ch + 528u * r31 + 128u * kt + 16u * hf;
      if (true) {
#pragma unroll
        for (int a = 0; a < 4; ++a) *(LAS f32x4*)(L + so + 32u * a) = f32x4{acc[4 * a], acc[4 * a + 1], acc[4 * a + 2], acc[4 * a + 3]};
      }
    }
    BAR();
    STAMP(9);
    constexpr u32 L_P = L_S1 + 16384;
    constexpr u32 L_ML = L_S1 + 28672;
    {
      const u32 hd = 4u * w + r4, j = c16;
      const u32 so = L_S0 + 528u * hd + 32u * j;
      const f32x4 sa = *(LAS f32x4*)(L + so), sb_ = *(LAS f32x4*)(L + so + 16896u), sc = *(LAS f32x4*)(L + so + 16u), sd = *(LAS f32x4*)(L + so + 16896u + 16u);
      SB();
#pragma unroll
      for (int ks = 0; ks < 8; ++ks) vt[ks][1] = tr128(L, tro0 + 65536u + 4096u * ks, tro1 + 65536u + 4096u * ks);
      SB();
      const f32x4 s0 = sa + sb_, s1 = sc + sd;
      float m = fmaxf(fmaxf(fmaxf(s0[0], s0[1]), fmaxf(s0[2], s0[3])), fmaxf(fmaxf(s1[0], s1[1]), fmaxf(s1[2], s1[3])));
      m = fmaxf(m, dppf<XOR1>(m)); m = fmaxf(m, dppf<XOR2>(m)); m = fmaxf(m, dppf<ROR4>(m)); m = fmaxf(m, dppf<ROR8>(m));
      const float m2 = m * SC2;
      float p[8];
#pragma unroll
      for (int i = 0; i < 4; ++i) { p[i] = __builtin_amdgcn_exp2f(fmaf(s0[i], SC2, -m2)); p[4 + i] = __builtin_amdgcn_exp2f(fmaf(s1[i], SC2, -m2)); }
      *(LAS v4u*)(L + L_P + 256u * hd + 16u * (j ^ (hd & 15u))) = v4u{pk2(p[0], p[1]), pk2(p[2], p[3]), pk2(p[4], p[5]), pk2(p[6], p[7])};
      float l = ((p[0] + p[1]) + (p[2] + p[3])) + ((p[4] + p[5]) + (p[6] + p[7]));
      l += dppf<XOR1>(l); l += dppf<XOR2>(l); l += dppf<ROR4>(l); l += dppf<ROR8>(l);
      if (j == 0u) *(LAS f32x2*)(L + L_ML + 8u * hd) = f32x2{m2, l};
    }
    BAR();
    STAMP(10);
    {
      f32x16 o0, o1;
#pragma unroll
      for (int i = 0; i < 16; ++i) { o0[i] = 0.f; o1[i] = 0.f; }
      const u32 po = L_P + 256u * r31 + 16u * (hf ^ (r31 & 15u));
      v4u pf[8];
#pragma unroll
      for (int s = 0; s < 8; ++s) pf[s] = lds128(L, po ^ (32u * s));
      SB();
#pragma unroll
      for (int s = 0; s < 8; ++s) o0 = mfma32(vt[s][0], pf[s], o0);
#pragma unroll
      for (int s = 0; s < 8; ++s) o1 = mfma32(vt[s][1], pf[s], o1);
      const u32 sb = L_S3 + lw + 128u * r31 + 8u * hf, sx = 16u * (r31 & 7u);
#pragma unroll
      for (int a = 0; a < 4; ++a) *(LAS v2u*)(L + sb + ((16u * a) ^ sx)) = v2u{pk2(o0[4 * a], o0[4 * a + 1]), pk2(o0[4 * a + 2], o0[4 * a + 3])};
#pragma unroll
      for (int a = 0; a < 4; ++a) *(LAS v2u*)(L + sb + ((64u + 16u * a) ^ sx)) = v2u{pk2(o1[4 * a], o1[4 * a + 1]), pk2(o1[4 * a + 2], o1[4 * a + 3])};
      const u32 rr = (u32)lane >> 3, kk = (u32)lane & 7u;
      char* od = ws + WS_OA + (((u32)b * 32u * 4u + (u32)(w >> 1)) * 64u + sp) * 256u + (u32)(w & 1) * 128u;
      v4u t[4];
#pragma unroll
      for (int i = 0; i < 4; ++i) t[i] = lds128(L, L_S3 + lw + 1024u * i + 128u * rr + 16u * (kk ^ rr));
#pragma unroll
      for (int i = 0; i < 4; ++i) st_sc1(od, (8u * i + rr) * 65536u + 16u * kk, t[i]);
    }
    STAMP(11);
    asm volatile("s_waitcnt vmcnt(4)" : PIN4(uv)::"memory");
    put_wblock(L, L_S2, lane, w, uv);
    WAITVM(0); BAR();
    STAMP(12);
    if (w == 0 && lane < 32) {
      const f32x2 ml = *(LAS f32x2*)(L + L_ML + 8u * lane);
      st_sc1(ws + WS_MLA + ((u32)b * 64u + sp) * 512u, 16u * lane, v4u{__float_as_uint(ml[0]), ta, __float_as_uint(ml[1]), tb});
    }
    v4u wa[4];
    read_wfrags(L, L_S2, lane, w, wa);
    constexpr u32 L_IMG = L_S0, L_WO = L_S4;
    {
      const u32 bb = (u32)w >> 2, half = ((u32)w >> 1) & 1u, rh = (u32)w & 1u;
      const u32 r8 = (u32)lane >> 3, k8 = (u32)lane & 7u;
      const char* fsrc = ws + WS_MLA + bb * 32768u + hh_ * 16u;
      const char* psrc = ws + WS_OA + ((bb * 32u + hh_) * 4u + qq_) * 16384u + rh * 8192u + half * 128u;
      const u32 po = r8 * 256u + k8 * 16u, fo = 512u * lane;
      constexpr u32 L_W = L_ML + 256;
      if ((w & 3) == 0) {
        NAP(NAP_H3);
        const v4u f = poll1(fsrc, fo, ta, tb);
        float M = __uint_as_float(f.x);
        M = fmaxf(M, dppf<XOR1>(M)); M = fmaxf(M, dppf<XOR2>(M)); M = fmaxf(M, dppf<ROR4>(M)); M = fmaxf(M, dppf<ROR8>(M)); M = sw16_max(M); M = sw32_max(M);
        const float e = __builtin_amdgcn_exp2f(__uint_as_float(f.x) - M);
        float Ls = e * __uint_as_float(f.z);
        Ls += dppf<XOR1>(Ls); Ls += dppf<XOR2>(Ls); Ls += dppf<ROR4>(Ls); Ls += dppf<ROR8>(Ls); Ls = sw16_add(Ls, Ls); Ls = sw32_add(Ls, Ls);
        *(LAS float*)(L + L_W + 256u * bb + 4u * lane) = e * __builtin_amdgcn_rcpf(Ls);
      }
      BAR();
      STAMP(13);
      v4u d0, d1, d2, d3;
      ld_sc1<0>(d0, psrc, po); ld_sc1<2048>(d1, psrc, po); ld_sc1<0>(d2, psrc + 4096, po); ld_sc1<2048>(d3, psrc + 4096, po);
      RBAR();
      dma_wblock((const char*)wo + (u32)hh_ * (128u * 1024u) + qq_ * 256u, 1024u, L_WO, lane, w, 16u * w);
      float wi[4];
#pragma unroll
      for (int i = 0; i < 4; ++i) wi[i] = *(LAS float*)(L + L_W + 256u * bb + 128u * rh + 32u * i + 4u * r8);
      asm volatile("s_waitcnt vmcnt(4)" : "+v"(d0), "+v"(d1), "+v"(d2), "+v"(d3)::"memory");
      STAMP(14);
      float a[8];
#pragma unroll
      for (int k = 0; k < 4; ++k) {
        a[2 * k] = fmaf(wi[3], bflo(d3[k]), fmaf(wi[2], bflo(d2[k]), fmaf(wi[1], bflo(d1[k]), wi[0] * bflo(d0[k]))));
        a[2 * k + 1] = fmaf(wi[3], bfhi(d3[k]), fmaf(wi[2], bfhi(d2[k]), fmaf(wi[1], bfhi(d1[k]), wi[0] * bfhi(d0[k]))));
      }
      const float r0 = sw32_add(a[0], a[4]), r1 = sw32_add(a[1], a[5]), r2 = sw32_add(a[2], a[6]), r3 = sw32_add(a[3], a[7]);
      float u0 = sw16_add(r0, r2), u1 = sw16_add(r1, r3);
      u0 += dppf<ROR8>(u0); u1 += dppf<ROR8>(u1);
      const u32 hi = pk2(u0, u1), lo = pk2(u0 - bflo(hi), u1 - bfhi(hi));
      const u32 cl = 64u * half + 8u * k8 + 4u * ((u32)lane >> 5) + 2u * (r4 & 1u);
      *(LAS u32*)(L + L_IMG + 272u * (bb + 2u * (r8 & 1u) + 4u * rh) + 2u * cl) = (r8 & 1u) ? lo : hi;
    }
    BAR();
    {
      v4u bf[4]; read_bfrags<7>(L, L_IMG, lane, bf);
      f32x4 d = mv4(wa, bf);
#pragma unroll
      for (int i = 0; i < 4; ++i) { d[i] += dppf<XOR2>(d[i]); d[i] += dppf<ROR4>(d[i]); }
      publish_d(ws + WS_CTX + ((u32)hh_ * 4u + qq_) * 2048u, d, lane, w, ta, tb);
    }
    STAMP(15);
    NAP(NAP_HOP);
    gather4(ws + WS_CTX + (u32)hh_ * 8192u, L, L_IMG + 4096u, lane, w, ta, tb);
    STAMP(16);
    BAR();
    {
      v4u a[4], bf[4];
      read_wfrags(L, L_WO, lane, w, a);
      read_bfrags<1>(L, L_IMG + 4096u, lane, bf);
      publish_d(ws + WS_OUTP + ((u32)qq_ * 32u + hh_) * 2048u, mv4(a, bf), lane, w, ta, tb);
    }
    STAMP(17);
    if (idx < 32) {
      NAP(NAP_HOP);
      final_reduce(ws, out, L, L_IMG + 8192u, 0, idx, lane, w, ta, tb);
      if (idx == 0 && w == 0 && lane == 0) __hip_atomic_store((GAS u32*)(ws + WS_E), e0 + 1u, __ATOMIC_RELAXED, __HIP_MEMORY_SCOPE_AGENT);
    }
    STAMP(18);
  } else {
    v4u xf[4], xg[4];
    ld_plain<0>(xg[0], xA, xo); ld_plain<64>(xg[1], xA, xo); ld_plain<128>(xg[2], xA, xo); ld_plain<192>(xg[3], xA, xo);
    RBAR();
    {
      const u32 bs = (16u * w + r4) * 8192u + 16u * (c16 ^ r4);
      dma4((const char*)wuk + blkoff, bs, (bs ^ 64u) + 31744u, (bs ^ 128u) + 63488u, (bs ^ 192u) + 95232u, L_S0 + lw);
    }
    ld_plain<0>(xf[0], xB, xo); ld_plain<64>(xf[1], xB, xo); ld_plain<128>(xf[2], xB, xo); ld_plain<192>(xf[3], xB, xo);
    CODE_PF()
    LOAD_E()
    asm volatile("s_waitcnt vmcnt(9)" : PIN4(xg)::"memory");
    STAMP(1);
    BAR();
    {
      v4u a[4]; read_wfrags(L, L_S4, lane, w, a);
      publish_d(ws + WS_QA + ((u32)hh_ * 4u + qq_) * 2048u, mv4(a, xg), lane, w, ta, tb);
    }
    RBAR();
    STAMP(19);
    const char* wqb = (const char*)wqB + blkoff;
#define WQB(i) dma1<i>(wqb, (i) == 0 ? k0 : (i) == 1 ? k1 : (i) == 2 ? k2 : k3, L_S4 + lw)
    if (NB1 >= 1) WQB(0);
    if (NB1 >= 2) WQB(1);
    asm volatile("s_waitcnt vmcnt(%4)" : PIN4(xf) : "i"(NB1) : "memory");
    STAMP(20);
    constexpr u32 L_IMG = L_S1;
    v4u uk[4];
    {
      const u32 o = L_S0 + lw + 256u * c16 + 16u * (r4 ^ c16);
#pragma unroll
      for (int s = 0; s < 4; ++s) uk[s] = lds128(L, o ^ (64u * s));
    }
    if (NAP_H1) NAP(NAP_H1);
    gather4(ws + WS_QA + (u32)hh_ * 8192u, L, L_IMG, lane, w, ta, tb);
    STAMP(2);
    BAR();
    {
      v4u bf[4]; read_bfrags<1>(L, L_IMG, lane, bf);
      const f32x4 d = mv4(uk, bf);
      if (c16 < 2u) *(LAS v2u*)(L + L_IMG + 1024u + 256u * c16 + 32u * w + 8u * r4) = v2u{pk2(d[0], d[1]), pk2(d[2], d[3])};
    }
    BAR();
    if (w == 0) {
      v4u q = lds128(L, L_IMG + 1024u + 16u * ((u32)lane & 31u));
      const u32 ck = rowsum(sum4(q));
      u32 o = WS_QABS + (r4 & 1u) * 32768u + (u32)hh_ * 1024u + (u32)qq_ * 256u + 16u * c16;
      if (lane >= 32) { q = v4u{ck ^ ta, tb, 0u, 0u}; o = WS_QCK + (r4 & 1u) * 2048u + (u32)hh_ * 64u + (u32)qq_ * 16u; }
      if (lane < 32 || c16 == 0u) st_sc1(ws, o, q);
    }
    RBAR();
    STAMP(3);
    if (NB1 < 1) { WQB(0); if (B_PACE) WAITVM(B_PACE - 1); }
    if (NB1 < 2) { WQB(1); if (B_PACE) WAITVM(B_PACE - 1); }
    WQB(2); if (B_PACE) WAITVM(B_PACE - 1);
    WQB(3);
    const int b = idx >> 6, kvh = (idx >> 3) & 7, j = idx & 7;
    const size_t kvo = (((size_t)b * 8 + kvh) * 8192 + 6144 + 256u * j + 32u * w) * 256u;
    const u32 kb = L_S0 + 8192u * w, vb = L_S2 + 8192u * w;
    const u32 kx = 256u * r4 + 16u * (c16 ^ r4);
    if (K_EARLY >= 2) dma2a((const char*)Kc + kvo, kx, kx ^ 64u, kb);
    if (K_EARLY >= 4) dma2b((const char*)Kc + kvo, kx ^ 128u, kx ^ 192u, kb);
    WAITVM(K_EARLY); BAR();
    STAMP(4);
    {
      v4u a[4]; read_wfrags(L, L_S4, lane, w, a);
      publish_d(ws + WS_QB + ((u32)hh_ * 4u + qq_) * 2048u, mv4(a, xf), lane, w, ta, tb);
    }
    if (K_NAP) NAP(K_NAP);
    RBAR();
    STAMP(5);
    if (K_EARLY < 2) dma2a((const char*)Kc + kvo, kx, kx ^ 64u, kb);
    if (K_EARLY < 4) dma2b((const char*)Kc + kvo, kx ^ 128u, kx ^ 192u, kb);
    dma4((const char*)Kc + kvo + 4096, kx, kx ^ 64u, kx ^ 128u, kx ^ 192u, kb + 4096u);
    RBAR();
    v4u g[2];
    const char* qsrc = ws + WS_QB + (4u * kvh + (u32)(w >> 1)) * 8192u + (u32)b * 1024u + (u32)(w & 1) * 512u;
    const u32 go = ((u32)lane & 3u) * 2048u + ((u32)lane >> 2) * 16u;
    {
      const u32 vx = 256u * r4 + 16u * (c16 & 1u) + 32u * ((c16 >> 1) ^ r4);
      dma4((const char*)Vc + kvo, vx, vx ^ 128u, vx, vx ^ 128u, vb);
      ld_sc1<0>(g[0], qsrc, go); ld_sc1<256>(g[1], qsrc, go);
      RBAR();
      dma4((const char*)Vc + kvo + 4096, vx, vx ^ 128u, vx, vx ^ 128u, vb + 4096u);
    }
    RBAR();
    dma_wblock((const char*)woB + (u32)hh_ * (128u * 1024u) + qq_ * 256u, 1024u, L_S4, lane, w, 16u * w);
    asm volatile("s_waitcnt vmcnt(8)" : "+v"(g[0]), "+v"(g[1])::"memory");
    STAMP(6);
    v4u kf[2][4];
    {
      const u32 o = kb + 256u * c16 + 16u * (r4 ^ c16);
#pragma unroll
      for (int t = 0; t < 2; ++t)
#pragma unroll
        for (int s = 0; s < 4; ++s) kf[t][s] = lds128(L, (o ^ (64u * s)) + 4096u * t);
    }
    for (int it = 0;; ++it) {
      if (wave_ok(tagbad(g[0], ta, tb, tagbad(g[1], ta, tb))) || it > SPIN_CAP) { STAMPV(30, it); break; }
      NAP(NAP_RETRY);
      ld_sc1<0>(g[0], qsrc, go); ld_sc1<256>(g[1], qsrc, go);
      asm volatile("s_waitcnt vmcnt(0)" : "+v"(g[0]), "+v"(g[1])::"memory");
    }
    constexpr u32 L_IMG2 = L_S0 + 6144;
    BAR();
#pragma unroll
    for (int i = 0; i < 2; ++i) {
      float v0 = __uint_as_float(g[i].x), v1 = __uint_as_float(g[i].z);
      v0 += dppf<XOR1>(v0); v1 += dppf<XOR1>(v1); v0 += dppf<XOR2>(v0); v1 += dppf<XOR2>(v1);
      *(LAS u32*)(L + L_IMG2 + 272u * (u32)(w >> 1) + 4u * ((u32)(w & 1) * 32u + 16u * i + ((u32)lane >> 2))) = pk2(v0, v1);
    }
    BAR();
    STAMP(7);
    v4u pb; float m2, lsum;
    {
      v4u bf[4]; read_bfrags<3>(L, L_IMG2, lane, bf);
      const f32x4 s0 = mv4(kf[0], bf), s1 = mv4(kf[1], bf);
      float m = fmaxf(fmaxf(fmaxf(s0[0], s0[1]), fmaxf(s0[2], s0[3])), fmaxf(fmaxf(s1[0], s1[1]), fmaxf(s1[2], s1[3])));
      m = sw16_max(m); m = sw32_max(m);
      m2 = m * SC2;
      float p[8];
#pragma unroll
      for (int i = 0; i < 4; ++i) { p[i] = __builtin_amdgcn_exp2f(fmaf(s0[i], SC2, -m2)); p[4 + i] = __builtin_amdgcn_exp2f(fmaf(s1[i], SC2, -m2)); }
      pb = v4u{pk2(p[0], p[1]), pk2(p[2], p[3]), pk2(p[4], p[5]), pk2(p[6], p[7])};
      float l = ((p[0] + p[1]) + (p[2] + p[3])) + ((p[4] + p[5]) + (p[6] + p[7]));
      l = sw16_add(l, l); lsum = sw32_add(l, l);
      if (lane < 4) *(LAS f32x2*)(L + kb + 4096u + 8u * lane) = f32x2{m2, lsum};
    }
    {
      const u32 q4 = c16 >> 2, p4 = (u32)lane & 3u, rw = 4u * r4 + q4;
      u32 o = vb + 256u * rw + 32u * (rw & 7u) + 8u * p4;
      u32 mo = kb + 528u * c16 + 16u * r4;
      asm volatile("s_waitcnt vmcnt(4)" : "+v"(pb), "+v"(o), "+v"(mo)::"memory");
      STAMP(8);
      v4u a[8]; f32x4 d[8];
#pragma unroll
      for (int c = 0; c < 8; ++c) a[c] = tr128(L, o ^ (32u * c), (o ^ (32u * c)) + 4096u);
      SB();
#pragma unroll
      for (int c = 0; c < 8; ++c) d[c] = mfma16(a[c], pb, f32x4{0.f, 0.f, 0.f, 0.f});
      SB();
      if (c16 < 4u) {
#pragma unroll
        for (int c = 0; c < 8; ++c) *(LAS f32x4*)(L + mo + 64u * c) = d[c];
      }
    }
    BAR();
    STAMP(9);
    if (w < 4) {
      const f32x2 ml = *(LAS f32x2*)(L + L_S0 + 8192u * ((u32)lane & 7u) + 4096u + 8u * w);
      f32x2 ov[8];
#pragma unroll
      for (int v = 0; v < 8; ++v) ov[v] = *(LAS f32x2*)(L + L_S0 + 8192u * v + 528u * w + 8u * lane);
      SB();
      float M = ml[0];
      M = fmaxf(M, dppf<XOR1>(M)); M = fmaxf(M, dppf<XOR2>(M)); M = fmaxf(M, dppf<HMIR>(M));
      const float e = __builtin_amdgcn_exp2f(ml[0] - M);
      float Ls = e * ml[1];
      Ls += dppf<XOR1>(Ls); Ls += dppf<XOR2>(Ls); Ls += dppf<HMIR>(Ls);
      float o0 = 0.f, o1 = 0.f;
#pragma unroll
      for (int v = 0; v < 8; ++v) {
        const float ev_ = __builtin_bit_cast(float, __builtin_amdgcn_readlane(__builtin_bit_cast(int, e), v));
        o0 = fmaf(ev_, ov[v][0], o0); o1 = fmaf(ev_, ov[v][1], o1);
      }
      const u32 rec = ((u32)b * 32u + 4u * kvh + w) * 8u + j;
      st_sc1(ws + WS_OB + rec * 1024u, 16u * lane, v4u{__float_as_uint(o0), ta, __float_as_uint(o1), tb});
      if (lane == 0) st_sc1(ws + WS_MLB + rec * 128u, 0u, v4u{__float_as_uint(M), ta, __float_as_uint(Ls), tb});
    }
    STAMP(10);
    constexpr u32 L_IMG3 = L_S2;
    {
      const u32 bb = (u32)w >> 2, jj = ((u32)lane & 3u) | (((u32)lane >> 5) << 2), gl = ((u32)lane >> 2) & 7u;
      const char* osrc = ws + WS_OB + (bb * 32u + hh_) * 8192u + (u32)(w & 3) * 256u;
      const char* msrc = ws + WS_MLB + (bb * 32u + hh_) * 1024u;
      const u32 oo = jj * 1024u + gl * 16u, mo = jj * 128u;
      v4u f = {0u, ~ta, 0u, ~tb}, d0 = f, d1 = f;
      NAP(NAP_HOP);
      for (int it = 0; it < SPIN_CAP; ++it) {
        if (tagbad(d0, ta, tb, tagbad(d1, ta, tb, tagbad(f, ta, tb))))
          asm volatile("global_load_dwordx4 %0, %3, %5 sc1\n\tglobal_load_dwordx4 %1, %3, %5 offset:128 sc1\n\tglobal_load_dwordx4 %2, %4, %6 sc1\n\ts_waitcnt vmcnt(0)"
                       : "+v"(d0), "+v"(d1), "+v"(f) : "v"(oo), "v"(mo), "s"(osrc), "s"(msrc) : "memory");
        if (wave_ok(tagbad(d0, ta, tb, tagbad(d1, ta, tb, tagbad(f, ta, tb))))) break;
        NAP(NAP_RETRY);
      }
      STAMP(11);
      float M = __uint_as_float(f.x);
      M = fmaxf(M, dppf<XOR1>(M)); M = fmaxf(M, dppf<XOR2>(M)); M = sw32_max(M);
      const float e = __builtin_amdgcn_exp2f(__uint_as_float(f.x) - M);
      float Ls = e * __uint_as_float(f.z);
      Ls += dppf<XOR1>(Ls); Ls += dppf<XOR2>(Ls); Ls = sw32_add(Ls, Ls);
      const float wi = e * __builtin_amdgcn_rcpf(Ls);
      float v0 = sw32_add(wi * __uint_as_float(d0.x), wi * __uint_as_float(d1.x));
      float v1 = sw32_add(wi * __uint_as_float(d0.z), wi * __uint_as_float(d1.z));
      v0 += dppf<XOR1>(v0); v1 += dppf<XOR1>(v1); v0 += dppf<XOR2>(v0); v1 += dppf<XOR2>(v1);
      *(LAS u32*)(L + L_IMG3 + 272u * bb + 4u * ((u32)(w & 3) * 16u + ((u32)lane >> 5) * 8u + gl)) = pk2(v0, v1);
    }
    BAR();
    {
      v4u a[4], bf[4];
      read_wfrags(L, L_S4, lane, w, a);
      read_bfrags<1>(L, L_IMG3, lane, bf);
      publish_d(ws + WS_OUTP + ((4u + qq_) * 32u + hh_) * 2048u, mv4(a, bf), lane, w, ta, tb);
    }
    STAMP(17);
    if (idx < 32) { NAP(NAP_HOP); final_reduce(ws, out, L, L_IMG3 + 4096u, 1, idx, lane, w, ta, tb); }
    STAMP(18);
  }
}

extern "C" void kernel_launch(void* const* d_in, const int* in_sizes, int n_in, void* d_out, int out_size, void* d_ws, size_t ws_size,
                              hipStream_t stream) {
  fused_decode<<<256, 512, 0, stream>>>((const u16*)d_in[0], (const u16*)d_in[1], (const u16*)d_in[2], (const u16*)d_in[3], (const u16*)d_in[4],
                                        (const u16*)d_in[5], (const u16*)d_in[6], (const u16*)d_in[7], (const u16*)d_in[8], (const u16*)d_in[9],
                                        (const u16*)d_in[10], (u16*)d_out, (char*)d_ws);
}
```

```cpp
#define A_DELAY 12
#define G_NAP 16
#define NTS ""
#define CAL_STAMP 97
#define CAL_ROLE 0
#define PROBE_PTR woB
#include <hip/hip_runtime.h>
#include <stdint.h>

typedef unsigned u32;
typedef unsigned long long u64;
typedef unsigned short u16;
typedef u32 v4u __attribute__((ext_vector_type(4)));
typedef u32 v2u __attribute__((ext_vector_type(2)));
typedef float f32x2 __attribute__((ext_vector_type(2)));
typedef float f32x4 __attribute__((ext_vector_type(4)));
typedef float f32x16 __attribute__((ext_vector_type(16)));
typedef __bf16 bf16x8 __attribute__((ext_vector_type(8)));
typedef __bf16 bf16x2 __attribute__((ext_vector_type(2)));
typedef short s16x4 __attribute__((ext_vector_type(4)));
#define GAS __attribute__((address_space(1)))
#define LAS __attribute__((address_space(3)))
#define DI __device__ __forceinline__

constexpr u32 WS_E = 0;
constexpr u32 WS_QA = 4096;
constexpr u32 WS_QB = WS_QA + 262144;
constexpr u32 WS_QABS = WS_QB + 262144;
constexpr u32 WS_QCK = WS_QABS + 65536;
constexpr u32 WS_MLA = WS_QABS + 131072;
constexpr u32 WS_CTX = WS_MLA + 65536;
constexpr u32 WS_OB = WS_CTX + 262144;
constexpr u32 WS_MLB = WS_OB + 524288;
constexpr u32 WS_OUTP = WS_MLB + 65536;
constexpr u32 WS_OA = 3u << 20;
constexpr u32 WS_DBG = 8u << 20;
static_assert(WS_OUTP + 524288 <= WS_OA, "ws map");

constexpr float SC2 = 0.08838834764831845f * 1.4426950408889634f;
constexpr int SPIN_CAP = 1 << 17;

constexpr u32 L_S0 = 0, L_S1 = 32768, L_S2 = 65536, L_S3 = 98304, L_S4 = 131072;

#define STAMP(k) do { if ((k) == 0 || (k) == CAL_STAMP) { u64 t_; asm volatile("s_memrealtime %0\n\ts_waitcnt lgkmcnt(0)" : "=s"(t_)::"memory"); if ((k) == 0) cal0 = (u32)t_; else if (CAL_ROLE == roleB) calx = (u32)t_ - cal0; } } while (0)
#define STAMPV(k, v)

#define WAITVM(n) asm volatile("s_waitcnt vmcnt(%0)" ::"i"(n) : "memory")
#define BAR() asm volatile("s_waitcnt lgkmcnt(0)\n\ts_barrier" ::: "memory")

template <int CTRL> DI float dppf(float x) {
  return __builtin_bit_cast(float, __builtin_amdgcn_mov_dpp(__builtin_bit_cast(int, x), CTRL, 0xf, 0xf, true));
}
constexpr int XOR1 = 0xB1, XOR2 = 0x4E, HMIR = 0x141, ROR4 = 0x124, ROR8 = 0x128;
DI float sw32_add(float a, float b) { auto s = __builtin_amdgcn_permlane32_swap(__float_as_uint(a), __float_as_uint(b), false, false); return __uint_as_float(s[0]) + __uint_as_float(s[1]); }
DI float sw16_add(float a, float b) { auto s = __builtin_amdgcn_permlane16_swap(__float_as_uint(a), __float_as_uint(b), false, false); return __uint_as_float(s[0]) + __uint_as_float(s[1]); }
DI float sw32_max(float a) { auto s = __builtin_amdgcn_permlane32_swap(__float_as_uint(a), __float_as_uint(a), false, false); return fmaxf(__uint_as_float(s[0]), __uint_as_float(s[1])); }
DI float sw16_max(float a) { auto s = __builtin_amdgcn_permlane16_swap(__float_as_uint(a), __float_as_uint(a), false, false); return fmaxf(__uint_as_float(s[0]), __uint_as_float(s[1])); }
DI u32 pk2(float lo, float hi) { f32x2 p = {lo, hi}; return __builtin_bit_cast(u32, __builtin_convertvector(p, bf16x2)); }
DI float bflo(u32 x) { return __uint_as_float(x << 16); }
DI float bfhi(u32 x) { return __uint_as_float(x & 0xffff0000u); }
DI f32x4 mfma16(v4u a, v4u b, f32x4 c) { return __builtin_amdgcn_mfma_f32_16x16x32_bf16(__builtin_bit_cast(bf16x8, a), __builtin_bit_cast(bf16x8, b), c, 0, 0, 0); }
DI f32x16 mfma32(v4u a, v4u b, f32x16 c) { return __builtin_amdgcn_mfma_f32_32x32x16_bf16(__builtin_bit_cast(bf16x8, a), __builtin_bit_cast(bf16x8, b), c, 0, 0, 0); }

DI v4u lds128(LAS char* L, u32 off) { return *(LAS v4u*)(L + off); }
DI v2u tr64(LAS char* L, u32 off) { return __builtin_bit_cast(v2u, __builtin_amdgcn_ds_read_tr16_b64_v4i16((LAS s16x4*)(L + off))); }
DI v4u tr128(LAS char* L, u32 o0, u32 o1) { v2u a = tr64(L, o0), b = tr64(L, o1); return v4u{a.x, a.y, b.x, b.y}; }

DI void dma4(const void* sbase, u32 v0, u32 v1, u32 v2, u32 v3, u32 lds) {
  asm volatile("s_mov_b32 m0, %5\n\ts_nop 0\n\t"
               "global_load_lds_dwordx4 %0, %4 " NTS "\n\t"
               "global_load_lds_dwordx4 %1, %4 offset:1024 " NTS "\n\t"
               "global_load_lds_dwordx4 %2, %4 offset:2048 " NTS "\n\t"
               "global_load_lds_dwordx4 %3, %4 offset:3072 " NTS
               ::"v"(v0), "v"(v1), "v"(v2), "v"(v3), "s"(sbase), "s"(lds) : "memory");
}
DI void dma2a(const void* sbase, u32 v0, u32 v1, u32 lds) {
  asm volatile("s_mov_b32 m0, %3\n\ts_nop 0\n\t"
               "global_load_lds_dwordx4 %0, %2 " NTS "\n\t"
               "global_load_lds_dwordx4 %1, %2 offset:1024 " NTS
               ::"v"(v0), "v"(v1), "s"(sbase), "s"(lds) : "memory");
}
DI void dma2b(const void* sbase, u32 v2, u32 v3, u32 lds) {
  asm volatile("s_mov_b32 m0, %3\n\ts_nop 0\n\t"
               "global_load_lds_dwordx4 %0, %2 offset:2048 " NTS "\n\t"
               "global_load_lds_dwordx4 %1, %2 offset:3072 " NTS
               ::"v"(v2), "v"(v3), "s"(sbase), "s"(lds) : "memory");
}
template <int I> DI void dma1(const void* sbase, u32 v, u32 lds) {
  asm volatile("s_mov_b32 m0, %2\n\ts_nop 0\n\tglobal_load_lds_dwordx4 %0, %1 offset:%3 " NTS ::"v"(v), "s"(sbase), "s"(lds), "i"(1024 * I) : "memory");
}
template <int OFF> DI void ld_sc1(v4u& d, const void* sbase, u32 voff) {
  asm volatile("global_load_dwordx4 %0, %1, %2 offset:%3 sc1" : "=v"(d) : "v"(voff), "s"(sbase), "i"(OFF) : "memory");
}
template <int OFF> DI void ld_plain(v4u& d, const void* sbase, u32 voff) {
  asm volatile("global_load_dwordx4 %0, %1, %2 offset:%3" : "=v"(d) : "v"(voff), "s"(sbase), "i"(OFF) : "memory");
}
DI void st_sc1(void* sbase, u32 voff, v4u d) {
  asm volatile("global_store_dwordx4 %0, %1, %2 sc1\n\ts_nop 1" ::"v"(voff), "v"(d), "s"(sbase) : "memory");
}
#define PIN1(a) asm volatile("" : "+v"(a))
DI u32 orx(u32 acc, u32 a, u32 b) { return __builtin_amdgcn_bitop3_b32(acc, a, b, 0xF6); }
DI u32 tagbad(const v4u& g, u32 ta, u32 tb, u32 acc = 0u) { return orx(orx(acc, g.y, ta), g.w, tb); }
DI u32 rowsum(u32 x) {
  x += (u32)__builtin_amdgcn_mov_dpp((int)x, 0xB1, 0xf, 0xf, true); x += (u32)__builtin_amdgcn_mov_dpp((int)x, 0x4E, 0xf, 0xf, true);
  x += (u32)__builtin_amdgcn_mov_dpp((int)x, 0x124, 0xf, 0xf, true); x += (u32)__builtin_amdgcn_mov_dpp((int)x, 0x128, 0xf, 0xf, true);
  return x;
}
DI u32 sum4(const v4u& g) { return (g.x + g.y) + (g.z + g.w); }
DI bool wave_ok(u32 bad) { return __builtin_amdgcn_ballot_w64(bad != 0u) == 0ull; }

#define NAP(n) __builtin_amdgcn_s_sleep(n)
#define SB() __builtin_amdgcn_sched_barrier(0)
#define RBAR() asm volatile("s_barrier" ::: "memory")
#define NAP_H1 0
#define NAP_HOP 11
#define NAP_RETRY 6
DI v4u poll1(const char* src, u32 off, u32 ta, u32 tb) {
  v4u g;
  ld_sc1<0>(g, src, off);
  asm volatile("s_waitcnt vmcnt(0)" : "+v"(g)::"memory");
  for (int it = 0; it < SPIN_CAP && !wave_ok(tagbad(g, ta, tb)); ++it) {
    NAP(NAP_RETRY);
    if (tagbad(g, ta, tb)) asm volatile("global_load_dwordx4 %0, %1, %2 sc1\n\ts_waitcnt vmcnt(0)" : "+v"(g) : "v"(off), "s"(src) : "memory");
  }
  return g;
}

DI void dma_wblock(const char* blk, u32 rstride, u32 lds, int lane, int w, u32 row0) {
  const u32 r = (u32)lane >> 4, c = (u32)lane & 15u;
  const u32 b = (row0 + r) * rstride;
  const u32 x = c ^ (r << 2);
  dma4(blk, b + 16u * x, b + 4u * rstride - 1024u + 16u * (x ^ 1u), b + 8u * rstride - 2048u + 16u * (x ^ 2u), b + 12u * rstride - 3072u + 16u * (x ^ 3u),
       lds + 4096u * w);
}
template <int OFF> DI void ld_nt(v4u& d, const void* sbase, u32 voff) {
  asm volatile("global_load_dwordx4 %0, %1, %2 offset:%3 " NTS : "=v"(d) : "v"(voff), "s"(sbase), "i"(OFF) : "memory");
}
DI void reg_wblock(const char* blk, u32 rstride, int lane, u32 row0, v4u (&t)[4]) {
  const u32 o = (row0 + ((u32)lane >> 4)) * rstride + 16u * ((u32)lane & 15u);
  ld_nt<0>(t[0], blk, o); ld_nt<0>(t[1], blk + 4u * rstride, o); ld_nt<0>(t[2], blk + 8u * rstride, o); ld_nt<0>(t[3], blk + 12u * rstride, o);
}
DI void put_wblock(LAS char* L, u32 lds, int lane, int w, const v4u (&t)[4]) {
  const u32 r = (u32)lane >> 4, o = lds + 4096u * w + 256u * r + ((16u * ((u32)lane & 15u)) ^ (64u * r));
#pragma unroll
  for (int i = 0; i < 4; ++i) *(LAS v4u*)(L + (o ^ (16u * i)) + 1024u * i) = t[i];
}
#define PIN4(t) "+v"(t[0]), "+v"(t[1]), "+v"(t[2]), "+v"(t[3])
DI void read_wfrags(LAS char* L, u32 lds, int lane, int w, v4u (&a)[4]) {
  const u32 g = (u32)lane >> 4, q = ((u32)lane & 15u) >> 2, p = (u32)lane & 3u;
  const u32 ch = 2u * w + (p >> 1);
  const u32 o0 = lds + 256u * (8u * g + q) + 16u * (ch ^ ((q << 2) | ((2u * g) & 3u))) + 8u * (p & 1u);
  const u32 o1 = lds + 256u * (8u * g + 4u + q) + 16u * (ch ^ ((q << 2) | ((2u * g + 1u) & 3u))) + 8u * (p & 1u);
#pragma unroll
  for (int ks = 0; ks < 4; ++ks) a[ks] = tr128(L, o0 + 8192u * ks, o1 + 8192u * ks);
}
template <int MASK> DI void read_bfrags(LAS char* L, u32 img, int lane, v4u (&b)[4]) {
  const u32 o = img + 272u * ((u32)lane & MASK) + 16u * ((u32)lane >> 4);
#pragma unroll
  for (int s = 0; s < 4; ++s) b[s] = lds128(L, o + 64u * s);
}
DI void publish_d(void* dst, f32x4 d, int lane, int w, u32 ta, u32 tb) {
  const u32 col = (u32)lane & 15u, g = (u32)lane >> 4, pr = (col >> 1) & 1u;
  const float v0 = pr ? d[2] : d[0], v1 = pr ? d[3] : d[1];
  if (col < 4u) st_sc1(dst, (col & 1u) * 1024u + (8u * w + 2u * g + pr) * 16u, v4u{__float_as_uint(v0), ta, __float_as_uint(v1), tb});
}
DI f32x4 mv4(const v4u (&a)[4], const v4u (&b)[4]) {
  f32x4 d = {0.f, 0.f, 0.f, 0.f};
#pragma unroll
  for (int s = 0; s < 4; ++s) d = mfma16(a[s], b[s], d);
  return d;
}
DI void gather4(const char* src, LAS char* L, u32 img, int lane, int w, u32 ta, u32 tb) {
  const u32 gi = (16u * w + ((u32)lane >> 2)) & 63u, b = (u32)w >> 2;
  const u32 off = ((u32)lane & 3u) * 2048u + b * 1024u + gi * 16u;
  const v4u g = poll1(src, off, ta, tb);
  float v0 = __uint_as_float(g.x), v1 = __uint_as_float(g.z);
  v0 += dppf<XOR1>(v0); v1 += dppf<XOR1>(v1);
  v0 += dppf<XOR2>(v0); v1 += dppf<XOR2>(v1);
  *(LAS u32*)(L + img + 272u * b + 4u * gi) = pk2(v0, v1);
}
DI void final_reduce(const char* ws, u16* out, LAS char* L, u32 scr, int which, int idx, int lane, int w, u32 ta, u32 tb) {
  const u32 mq = (u32)idx >> 3, t = (u32)idx & 7u;
  const char* src = ws + WS_OUTP + (which * 4u + mq) * 65536u + (u32)w * 8192u + t * 128u;
  const v4u g = poll1(src, ((u32)lane >> 3) * 1024u + ((u32)lane & 7u) * 16u, ta, tb);
  float v0 = __uint_as_float(g.x), v1 = __uint_as_float(g.z);
  v0 = sw32_add(v0, v1);
  v0 = sw16_add(v0, v0);
  if (((u32)lane & 16u) == 0u) *(LAS float*)(L + scr + 128u * w + 8u * ((u32)lane & 15u) + 4u * ((u32)lane >> 5)) = v0;
  BAR();
  if (w == 0 && lane < 16) {
    f32x2 a = {0.f, 0.f};
#pragma unroll
    for (int i = 0; i < 8; ++i) a += *(LAS f32x2*)(L + scr + 128u * i + 8u * lane);
    *(GAS u32*)((GAS char*)out + (((u32)lane >> 3) * 1024u + which * 512u + 128u * mq + 16u * t + 2u * ((u32)lane & 7u)) * 2u) = pk2(a[0], a[1]);
  }
}

#define NB1 0
#define G_POS 2
#define A_EARLY 6
#define G_WAIT 0
#define K_NAP 0
#define B_PACE 0
#define K_EARLY 0
#define NAP_H3 11

__global__ void __launch_bounds__(512, 2)
fused_decode(const u16* __restrict__ xA, const u16* __restrict__ ckv, const u16* __restrict__ wq, const u16* __restrict__ wuk,
             const u16* __restrict__ wuv, const u16* __restrict__ wo, const u16* __restrict__ xB, const u16* __restrict__ Kc,
             const u16* __restrict__ Vc, const u16* __restrict__ wqB, const u16* __restrict__ woB, u16* __restrict__ out, char* __restrict__ ws) {
  __shared__ __attribute__((aligned(16))) char smem[163840];
  LAS char* L = (LAS char*)smem;
  const int lane = threadIdx.x & 63, w = __builtin_amdgcn_readfirstlane(threadIdx.x >> 6);
  const int bid = blockIdx.x, roleB = (bid >> 3) & 1, idx = ((bid >> 4) << 3) | (bid & 7);
  const int hh_ = (idx & 7) + 8 * (idx >> 5), qq_ = (idx >> 3) & 3;
  u32 cal0 = 0, calx = 0; u64 c0_; asm volatile("s_memtime %0\n\ts_waitcnt lgkmcnt(0)" : "=s"(c0_)::"memory");
  u64 kstart; asm volatile("s_getpc_b64 %0" : "=s"(kstart)); kstart &= ~(u64)0x7f;
  STAMP(0);
  asm volatile("" ::"s"(xA), "s"(ckv), "s"(wq), "s"(wuk), "s"(wuv), "s"(wo), "s"(xB), "s"(Kc), "s"(Vc), "s"(wqB), "s"(woB), "s"(out), "s"(ws));

  {
    if (bid == 0 && w == 0) {
      const char* pb = (const char*)(PROBE_PTR);
      u32 v = 0; u64 t0_, t1_;
      asm volatile("global_load_dword %0, %1, %2\n\ts_waitcnt vmcnt(0)" : "=v"(v) : "v"(64u), "s"(pb) : "memory");
      asm volatile("s_memrealtime %0\n\ts_waitcnt lgkmcnt(0)" : "=s"(t0_)::"memory");
      for (int i = 1; i <= 32; ++i) asm volatile("global_load_dword %0, %1, %2\n\ts_waitcnt vmcnt(0)" : "=v"(v) : "v"((u32)i * 4096u + 1024u), "s"(pb) : "memory");
      asm volatile("s_memrealtime %0\n\ts_waitcnt lgkmcnt(0)" : "=s"(t1_)::"memory");
      calx = (u32)t1_ - (u32)t0_;
    }
    for (;;) { u64 t_; asm volatile("s_memrealtime %0\n\ts_waitcnt lgkmcnt(0)" : "=s"(t_)::"memory"); if ((u32)t_ - cal0 >= 1600u) break; __builtin_amdgcn_s_sleep(1); }
  }
  const u32 blkoff = (u32)qq_ * (128u * 8192u) + (u32)hh_ * 256u;
  const u32 r4 = (u32)lane >> 4, c16 = (u32)lane & 15u, lw = 4096u * w;
  const int bA = (idx & 7) >> 2, sp = (idx >> 3) * 4 + (idx & 3);
  const char* kv = (const char*)ckv + ((u32)bA * 8192u + (u32)sp * 128u) * 1024u;
  const u32 rsh = roleB ? 13u : 10u, rstep = (4u << rsh) - 1024u;
  const u32 k0 = ((16u * w + r4) << rsh) + ((16u * c16) ^ (64u * r4));
  const u32 k1 = (k0 ^ 16u) + rstep, k2 = (k0 ^ 32u) + 2u * rstep, k3 = (k0 ^ 48u) + 3u * rstep;
  if (A_DELAY && !roleB) NAP(A_DELAY);
  dma4(roleB ? (const char*)wq + blkoff : kv, k0, k1, k2, k3, (roleB ? L_S4 : L_S0) + lw);
#define CODE_PF() { \
    asm volatile("s_mov_b32 m0, %2\n\ts_nop 0\n\tglobal_load_lds_dword %0, %1" \
                 ::"v"(w >= 5 ? (u32)lane * 128u + (u32)(w - 5) * 8192u : 0u), "s"(kstart), "s"(L_S3 + lw) : "memory"); }
  STAMP(25);
  u32 ta, tb, e0;
  const u32 xo = ((u32)lane & 1u) * 1024u + (u32)qq_ * 256u + r4 * 16u;
#define LOAD_E() \
  asm volatile("s_load_dword %0, %1, 0x0\n\ts_waitcnt lgkmcnt(0)" : "=s"(e0) : "s"(ws) : "memory"); \
  ta = e0 * 0x9E3779B1u + 0x7F4A7C15u; tb = (e0 ^ (e0 >> 15)) * 0x85EBCA77u + 0x3C6EF372u;

  if (!roleB) {
    const int b = bA;
    CODE_PF()
    RBAR();
    dma4(kv + 256, k0, k1, k2, k3, L_S1 + lw);
    LOAD_E()
    RBAR();
    dma4(kv + 512, k0, k1, k2, k3, L_S2 + lw);
    RBAR();
    dma4(kv + 768, k0, k1, k2, k3, L_S3 + lw);
    STAMP(1);
    v4u g[4], gc;
    const char* qsrc = ws + WS_QABS + (u32)b * 32768u + lw;
    const char* csrc = ws + WS_QCK + (u32)b * 2048u + (u32)w * 256u;
    const u32 go = 16u * lane, co = 64u * (c16 & 3u) + 16u * r4;
#define GATHER_Q() { ld_sc1<0>(g[0], qsrc, go); ld_sc1<1024>(g[1], qsrc, go); ld_sc1<2048>(g[2], qsrc, go); ld_sc1<3072>(g[3], qsrc, go); ld_sc1<0>(gc, csrc, co); }
#define WAIT_Q(n) asm volatile("s_waitcnt vmcnt(%5)" : PIN4(g), "+v"(gc) : "i"(n) : "memory")
    auto bad_q = [&]() {
      const u32 c0 = rowsum(sum4(g[0])), c1 = rowsum(sum4(g[1])), c2 = rowsum(sum4(g[2])), c3 = rowsum(sum4(g[3]));
      const u32 lo = (c16 & 1u) ? c1 : c0, hi = (c16 & 1u) ? c3 : c2;
      return orx(gc.y ^ tb, gc.x ^ ta, (c16 & 2u) ? hi : lo);
    };
    const int kt = w & 3, ch = w >> 2;
    const u32 r31 = (u32)lane & 31u, hf = (u32)lane >> 5;
    const u32 fr = ((r31 & 3u) << 2) | ((r31 >> 2) & 3u);
    const u32 qo = 256u * r31 + 16u * ((8u * ch + hf) ^ fr);
    const u32 blk = r4 & 1u, q4 = c16 >> 2, p4 = (u32)lane & 3u;
    const u32 chx = 4u * kt + 2u * blk + (p4 >> 1);
    const u32 tro0 = 32768u * ch + 256u * (8u * hf + q4) + 16u * (chx ^ ((q4 << 2) | (2u * hf))) + 8u * (p4 & 1u);
    const u32 tro1 = 32768u * ch + 256u * (8u * hf + 4u + q4) + 16u * (chx ^ ((q4 << 2) | (2u * hf + 1u))) + 8u * (p4 & 1u);
    v4u vt[8][2];
    WAITVM(8); BAR();
    STAMP(2);
#pragma unroll
    for (int ks = 0; ks < 8; ++ks) vt[ks][0] = tr128(L, tro0 + 4096u * ks, tro1 + 4096u * ks);
    u32 qw = L_S4 + 8192u * r4 + 1024u * w + ((16u * c16) ^ (16u * (w & 3)));
    asm volatile("s_waitcnt vmcnt(%1)" : "+v"(qw) : "i"(G_WAIT) : "memory");
    if (G_NAP) NAP(G_NAP);
    STAMP(3);
    GATHER_Q()
    v4u uv[4];
    RBAR();
    reg_wblock((const char*)wuv + (u32)hh_ * 256u, 8192u, lane, 256u * ((w >> 1) & 1) + 64u * qq_ + 32u * (w >> 2) + 16u * (w & 1), uv);
    STAMP(4);
    WAIT_Q(4);
    STAMP(5);
    if (!wave_ok(bad_q())) {
      for (int it = 0; it < SPIN_CAP; ++it) {
        NAP(8);
        GATHER_Q()
        WAIT_Q(0);
        STAMPV(30, it + 1);
        if (wave_ok(bad_q())) break;
      }
    }
    STAMP(6);
#pragma unroll
    for (int i = 0; i < 4; ++i) *(LAS v4u*)(L + (qw ^ (64u * i)) + 256u * i) = g[i];
    BAR();
    STAMP(7);
    f32x16 acc;
#pragma unroll
    for (int i = 0; i < 16; ++i) acc[i] = 0.f;
#define QK_READ(t) _Pragma("unroll") for (int s = 0; s < 4; ++s) { fa[t][s] = lds128(L, 32768u * (t) + 8192u * kt + (qo ^ (32u * s))); fb[t][s] = lds128(L, L_S4 + 8192u * (t) + (qo ^ (32u * s))); }
#define QK_MMA(t) _Pragma("unroll") for (int s = 0; s < 4; ++s) acc = mfma32(fa[t][s], fb[t][s], acc);
    v4u fa[4][4], fb[4][4];
    QK_READ(0) QK_READ(1)
    SB();
    QK_MMA(0)
    QK_READ(2)
    SB();
    QK_MMA(1)
    QK_READ(3)
    SB();
    QK_MMA(2)
    BAR();
    QK_MMA(3)
    {
      const u32 so = L_S0 + 16896u * ch + 528u * r31 + 128u * kt + 16u * hf;
      if (true) {
#pragma unroll
        for (int a = 0; a < 4; ++a) *(LAS f32x4*)(L + so + 32u * a) = f32x4{acc[4 * a], acc[4 * a + 1], acc[4 * a + 2], acc[4 * a + 3]};
      }
    }
    BAR();
    STAMP(9);
    constexpr u32 L_P = L_S1 + 16384;
    constexpr u32 L_ML = L_S1 + 28672;
    {
      const u32 hd = 4u * w + r4, j = c16;
      const u32 so = L_S0 + 528u * hd + 32u * j;
      const f32x4 sa = *(LAS f32x4*)(L + so), sb_ = *(LAS f32x4*)(L + so + 16896u), sc = *(LAS f32x4*)(L + so + 16u), sd = *(LAS f32x4*)(L + so + 16896u + 16u);
      SB();
#pragma unroll
      for (int ks = 0; ks < 8; ++ks) vt[ks][1] = tr128(L, tro0 + 65536u + 4096u * ks, tro1 + 65536u + 4096u * ks);
      SB();
      const f32x4 s0 = sa + sb_, s1 = sc + sd;
      float m = fmaxf(fmaxf(fmaxf(s0[0], s0[1]), fmaxf(s0[2], s0[3])), fmaxf(fmaxf(s1[0], s1[1]), fmaxf(s1[2], s1[3])));
      m = fmaxf(m, dppf<XOR1>(m)); m = fmaxf(m, dppf<XOR2>(m)); m = fmaxf(m, dppf<ROR4>(m)); m = fmaxf(m, dppf<ROR8>(m));
      const float m2 = m * SC2;
      float p[8];
#pragma unroll
      for (int i = 0; i < 4; ++i) { p[i] = __builtin_amdgcn_exp2f(fmaf(s0[i], SC2, -m2)); p[4 + i] = __builtin_amdgcn_exp2f(fmaf(s1[i], SC2, -m2)); }
      *(LAS v4u*)(L + L_P + 256u * hd + 16u * (j ^ (hd & 15u))) = v4u{pk2(p[0], p[1]), pk2(p[2], p[3]), pk2(p[4], p[5]), pk2(p[6], p[7])};
      float l = ((p[0] + p[1]) + (p[2] + p[3])) + ((p[4] + p[5]) + (p[6] + p[7]));
      l += dppf<XOR1>(l); l += dppf<XOR2>(l); l += dppf<ROR4>(l); l += dppf<ROR8>(l);
      if (j == 0u) *(LAS f32x2*)(L + L_ML + 8u * hd) = f32x2{m2, l};
    }
    BAR();
    STAMP(10);
    {
      f32x16 o0, o1;
#pragma unroll
      for (int i = 0; i < 16; ++i) { o0[i] = 0.f; o1[i] = 0.f; }
      const u32 po = L_P + 256u * r31 + 16u * (hf ^ (r31 & 15u));
      v4u pf[8];
#pragma unroll
      for (int s = 0; s < 8; ++s) pf[s] = lds128(L, po ^ (32u * s));
      SB();
#pragma unroll
      for (int s = 0; s < 8; ++s) o0 = mfma32(vt[s][0], pf[s], o0);
#pragma unroll
      for (int s = 0; s < 8; ++s) o1 = mfma32(vt[s][1], pf[s], o1);
      const u32 sb = L_S3 + lw + 128u * r31 + 8u * hf, sx = 16u * (r31 & 7u);
#pragma unroll
      for (int a = 0; a < 4; ++a) *(LAS v2u*)(L + sb + ((16u * a) ^ sx)) = v2u{pk2(o0[4 * a], o0[4 * a + 1]), pk2(o0[4 * a + 2], o0[4 * a + 3])};
#pragma unroll
      for (int a = 0; a < 4; ++a) *(LAS v2u*)(L + sb + ((64u + 16u * a) ^ sx)) = v2u{pk2(o1[4 * a], o1[4 * a + 1]), pk2(o1[4 * a + 2], o1[4 * a + 3])};
      const u32 rr = (u32)lane >> 3, kk = (u32)lane & 7u;
      char* od = ws + WS_OA + (((u32)b * 32u * 4u + (u32)(w >> 1)) * 64u + sp) * 256u + (u32)(w & 1) * 128u;
      v4u t[4];
#pragma unroll
      for (int i = 0; i < 4; ++i) t[i] = lds128(L, L_S3 + lw + 1024u * i + 128u * rr + 16u * (kk ^ rr));
#pragma unroll
      for (int i = 0; i < 4; ++i) st_sc1(od, (8u * i + rr) * 65536u + 16u * kk, t[i]);
    }
    STAMP(11);
    asm volatile("s_waitcnt vmcnt(4)" : PIN4(uv)::"memory");
    put_wblock(L, L_S2, lane, w, uv);
    WAITVM(0); BAR();
    STAMP(12);
    if (w == 0 && lane < 32) {
      const f32x2 ml = *(LAS f32x2*)(L + L_ML + 8u * lane);
      st_sc1(ws + WS_MLA + ((u32)b * 64u + sp) * 512u, 16u * lane, v4u{__float_as_uint(ml[0]), ta, __float_as_uint(ml[1]), tb});
    }
    v4u wa[4];
    read_wfrags(L, L_S2, lane, w, wa);
    constexpr u32 L_IMG = L_S0, L_WO = L_S4;
    {
      const u32 bb = (u32)w >> 2, half = ((u32)w >> 1) & 1u, rh = (u32)w & 1u;
      const u32 r8 = (u32)lane >> 3, k8 = (u32)lane & 7u;
      const char* fsrc = ws + WS_MLA + bb * 32768u + hh_ * 16u;
      const char* psrc = ws + WS_OA + ((bb * 32u + hh_) * 4u + qq_) * 16384u + rh * 8192u + half * 128u;
      const u32 po = r8 * 256u + k8 * 16u, fo = 512u * lane;
      constexpr u32 L_W = L_ML + 256;
      if ((w & 3) == 0) {
        NAP(NAP_H3);
        const v4u f = poll1(fsrc, fo, ta, tb);
        float M = __uint_as_float(f.x);
        M = fmaxf(M, dppf<XOR1>(M)); M = fmaxf(M, dppf<XOR2>(M)); M = fmaxf(M, dppf<ROR4>(M)); M = fmaxf(M, dppf<ROR8>(M)); M = sw16_max(M); M = sw32_max(M);
        const float e = __builtin_amdgcn_exp2f(__uint_as_float(f.x) - M);
        float Ls = e * __uint_as_float(f.z);
        Ls += dppf<XOR1>(Ls); Ls += dppf<XOR2>(Ls); Ls += dppf<ROR4>(Ls); Ls += dppf<ROR8>(Ls); Ls = sw16_add(Ls, Ls); Ls = sw32_add(Ls, Ls);
        *(LAS float*)(L + L_W + 256u * bb + 4u * lane) = e * __builtin_amdgcn_rcpf(Ls);
      }
      BAR();
      STAMP(13);
      v4u d0, d1, d2, d3;
      ld_sc1<0>(d0, psrc, po); ld_sc1<2048>(d1, psrc, po); ld_sc1<0>(d2, psrc + 4096, po); ld_sc1<2048>(d3, psrc + 4096, po);
      RBAR();
      dma_wblock((const char*)wo + (u32)hh_ * (128u * 1024u) + qq_ * 256u, 1024u, L_WO, lane, w, 16u * w);
      float wi[4];
#pragma unroll
      for (int i = 0; i < 4; ++i) wi[i] = *(LAS float*)(L + L_W + 256u * bb + 128u * rh + 32u * i + 4u * r8);
      asm volatile("s_waitcnt vmcnt(4)" : "+v"(d0), "+v"(d1), "+v"(d2), "+v"(d3)::"memory");
      STAMP(14);
      float a[8];
#pragma unroll
      for (int k = 0; k < 4; ++k) {
        a[2 * k] = fmaf(wi[3], bflo(d3[k]), fmaf(wi[2], bflo(d2[k]), fmaf(wi[1], bflo(d1[k]), wi[0] * bflo(d0[k]))));
        a[2 * k + 1] = fmaf(wi[3], bfhi(d3[k]), fmaf(wi[2], bfhi(d2[k]), fmaf(wi[1], bfhi(d1[k]), wi[0] * bfhi(d0[k]))));
      }
      const float r0 = sw32_add(a[0], a[4]), r1 = sw32_add(a[1], a[5]), r2 = sw32_add(a[2], a[6]), r3 = sw32_add(a[3], a[7]);
      float u0 = sw16_add(r0, r2), u1 = sw16_add(r1, r3);
      u0 += dppf<ROR8>(u0); u1 += dppf<ROR8>(u1);
      const u32 hi = pk2(u0, u1), lo = pk2(u0 - bflo(hi), u1 - bfhi(hi));
      const u32 cl = 64u * half + 8u * k8 + 4u * ((u32)lane >> 5) + 2u * (r4 & 1u);
      *(LAS u32*)(L + L_IMG + 272u * (bb + 2u * (r8 & 1u) + 4u * rh) + 2u * cl) = (r8 & 1u) ? lo : hi;
    }
    BAR();
    {
      v4u bf[4]; read_bfrags<7>(L, L_IMG, lane, bf);
      f32x4 d = mv4(wa, bf);
#pragma unroll
      for (int i = 0; i < 4; ++i) { d[i] += dppf<XOR2>(d[i]); d[i] += dppf<ROR4>(d[i]); }
      publish_d(ws + WS_CTX + ((u32)hh_ * 4u + qq_) * 2048u, d, lane, w, ta, tb);
    }
    STAMP(15);
    NAP(NAP_HOP);
    gather4(ws + WS_CTX + (u32)hh_ * 8192u, L, L_IMG + 4096u, lane, w, ta, tb);
    STAMP(16);
    BAR();
    {
      v4u a[4], bf[4];
      read_wfrags(L, L_WO, lane, w, a);
      read_bfrags<1>(L, L_IMG + 4096u, lane, bf);
      publish_d(ws + WS_OUTP + ((u32)qq_ * 32u + hh_) * 2048u, mv4(a, bf), lane, w, ta, tb);
    }
    STAMP(17);
    if (idx < 32) {
      NAP(NAP_HOP);
      final_reduce(ws, out, L, L_IMG + 8192u, 0, idx, lane, w, ta, tb);
      if (idx == 0 && w == 0 && lane == 0) __hip_atomic_store((GAS u32*)(ws + WS_E), e0 + 1u, __ATOMIC_RELAXED, __HIP_MEMORY_SCOPE_AGENT);
    }
    STAMP(18);
  } else {
    v4u xf[4], xg[4];
    ld_plain<0>(xg[0], xA, xo); ld_plain<64>(xg[1], xA, xo); ld_plain<128>(xg[2], xA, xo); ld_plain<192>(xg[3], xA, xo);
    RBAR();
    {
      const u32 bs = (16u * w + r4) * 8192u + 16u * (c16 ^ r4);
      dma4((const char*)wuk + blkoff, bs, (bs ^ 64u) + 31744u, (bs ^ 128u) + 63488u, (bs ^ 192u) + 95232u, L_S0 + lw);
    }
    ld_plain<0>(xf[0], xB, xo); ld_plain<64>(xf[1], xB, xo); ld_plain<128>(xf[2], xB, xo); ld_plain<192>(xf[3], xB, xo);
    CODE_PF()
    LOAD_E()
    asm volatile("s_waitcnt vmcnt(9)" : PIN4(xg)::"memory");
    STAMP(1);
    BAR();
    {
      v4u a[4]; read_wfrags(L, L_S4, lane, w, a);
      publish_d(ws + WS_QA + ((u32)hh_ * 4u + qq_) * 2048u, mv4(a, xg), lane, w, ta, tb);
    }
    RBAR();
    STAMP(19);
    const char* wqb = (const char*)wqB + blkoff;
#define WQB(i) dma1<i>(wqb, (i) == 0 ? k0 : (i) == 1 ? k1 : (i) == 2 ? k2 : k3, L_S4 + lw)
    if (NB1 >= 1) WQB(0);
    if (NB1 >= 2) WQB(1);
    asm volatile("s_waitcnt vmcnt(%4)" : PIN4(xf) : "i"(NB1) : "memory");
    STAMP(20);
    constexpr u32 L_IMG = L_S1;
    v4u uk[4];
    {
      const u32 o = L_S0 + lw + 256u * c16 + 16u * (r4 ^ c16);
#pragma unroll
      for (int s = 0; s < 4; ++s) uk[s] = lds128(L, o ^ (64u * s));
    }
    if (NAP_H1) NAP(NAP_H1);
    gather4(ws + WS_QA + (u32)hh_ * 8192u, L, L_IMG, lane, w, ta, tb);
    STAMP(2);
    BAR();
    {
      v4u bf[4]; read_bfrags<1>(L, L_IMG, lane, bf);
      const f32x4 d = mv4(uk, bf);
      if (c16 < 2u) *(LAS v2u*)(L + L_IMG + 1024u + 256u * c16 + 32u * w + 8u * r4) = v2u{pk2(d[0], d[1]), pk2(d[2], d[3])};
    }
    BAR();
    if (w == 0) {
      v4u q = lds128(L, L_IMG + 1024u + 16u * ((u32)lane & 31u));
      const u32 ck = rowsum(sum4(q));
      u32 o = WS_QABS + (r4 & 1u) * 32768u + (u32)hh_ * 1024u + (u32)qq_ * 256u + 16u * c16;
      if (lane >= 32) { q = v4u{ck ^ ta, tb, 0u, 0u}; o = WS_QCK + (r4 & 1u) * 2048u + (u32)hh_ * 64u + (u32)qq_ * 16u; }
      if (lane < 32 || c16 == 0u) st_sc1(ws, o, q);
    }
    RBAR();
    STAMP(3);
    if (NB1 < 1) { WQB(0); if (B_PACE) WAITVM(B_PACE - 1); }
    if (NB1 < 2) { WQB(1); if (B_PACE) WAITVM(B_PACE - 1); }
    WQB(2); if (B_PACE) WAITVM(B_PACE - 1);
    WQB(3);
    const int b = idx >> 6, kvh = (idx >> 3) & 7, j = idx & 7;
    const size_t kvo = (((size_t)b * 8 + kvh) * 8192 + 6144 + 256u * j + 32u * w) * 256u;
    const u32 kb = L_S0 + 8192u * w, vb = L_S2 + 8192u * w;
    const u32 kx = 256u * r4 + 16u * (c16 ^ r4);
    if (K_EARLY >= 2) dma2a((const char*)Kc + kvo, kx, kx ^ 64u, kb);
    if (K_EARLY >= 4) dma2b((const char*)Kc + kvo, kx ^ 128u, kx ^ 192u, kb);
    WAITVM(K_EARLY); BAR();
    STAMP(4);
    {
      v4u a[4]; read_wfrags(L, L_S4, lane, w, a);
      publish_d(ws + WS_QB + ((u32)hh_ * 4u + qq_) * 2048u, mv4(a, xf), lane, w, ta, tb);
    }
    if (K_NAP) NAP(K_NAP);
    RBAR();
    STAMP(5);
    if (K_EARLY < 2) dma2a((const char*)Kc + kvo, kx, kx ^ 64u, kb);
    if (K_EARLY < 4) dma2b((const char*)Kc + kvo, kx ^ 128u, kx ^ 192u, kb);
    dma4((const char*)Kc + kvo + 4096, kx, kx ^ 64u, kx ^ 128u, kx ^ 192u, kb + 4096u);
    RBAR();
    v4u g[2];
    const char* qsrc = ws + WS_QB + (4u * kvh + (u32)(w >> 1)) * 8192u + (u32)b * 1024u + (u32)(w & 1) * 512u;
    const u32 go = ((u32)lane & 3u) * 2048u + ((u32)lane >> 2) * 16u;
    {
      const u32 vx = 256u * r4 + 16u * (c16 & 1u) + 32u * ((c16 >> 1) ^ r4);
      dma4((const char*)Vc + kvo, vx, vx ^ 128u, vx, vx ^ 128u, vb);
      ld_sc1<0>(g[0], qsrc, go); ld_sc1<256>(g[1], qsrc, go);
      RBAR();
      dma4((const char*)Vc + kvo + 4096, vx, vx ^ 128u, vx, vx ^ 128u, vb + 4096u);
    }
    RBAR();
    dma_wblock((const char*)woB + (u32)hh_ * (128u * 1024u) + qq_ * 256u, 1024u, L_S4, lane, w, 16u * w);
    asm volatile("s_waitcnt vmcnt(8)" : "+v"(g[0]), "+v"(g[1])::"memory");
    STAMP(6);
    v4u kf[2][4];
    {
      const u32 o = kb + 256u * c16 + 16u * (r4 ^ c16);
#pragma unroll
      for (int t = 0; t < 2; ++t)
#pragma unroll
        for (int s = 0; s < 4; ++s) kf[t][s] = lds128(L, (o ^ (64u * s)) + 4096u * t);
    }
    for (int it = 0;; ++it) {
      if (wave_ok(tagbad(g[0], ta, tb, tagbad(g[1], ta, tb))) || it > SPIN_CAP) { STAMPV(30, it); break; }
      NAP(NAP_RETRY);
      ld_sc1<0>(g[0], qsrc, go); ld_sc1<256>(g[1], qsrc, go);
      asm volatile("s_waitcnt vmcnt(0)" : "+v"(g[0]), "+v"(g[1])::"memory");
    }
    constexpr u32 L_IMG2 = L_S0 + 6144;
    BAR();
#pragma unroll
    for (int i = 0; i < 2; ++i) {
      float v0 = __uint_as_float(g[i].x), v1 = __uint_as_float(g[i].z);
      v0 += dppf<XOR1>(v0); v1 += dppf<XOR1>(v1); v0 += dppf<XOR2>(v0); v1 += dppf<XOR2>(v1);
      *(LAS u32*)(L + L_IMG2 + 272u * (u32)(w >> 1) + 4u * ((u32)(w & 1) * 32u + 16u * i + ((u32)lane >> 2))) = pk2(v0, v1);
    }
    BAR();
    STAMP(7);
    v4u pb; float m2, lsum;
    {
      v4u bf[4]; read_bfrags<3>(L, L_IMG2, lane, bf);
      const f32x4 s0 = mv4(kf[0], bf), s1 = mv4(kf[1], bf);
      float m = fmaxf(fmaxf(fmaxf(s0[0], s0[1]), fmaxf(s0[2], s0[3])), fmaxf(fmaxf(s1[0], s1[1]), fmaxf(s1[2], s1[3])));
      m = sw16_max(m); m = sw32_max(m);
      m2 = m * SC2;
      float p[8];
#pragma unroll
      for (int i = 0; i < 4; ++i) { p[i] = __builtin_amdgcn_exp2f(fmaf(s0[i], SC2, -m2)); p[4 + i] = __builtin_amdgcn_exp2f(fmaf(s1[i], SC2, -m2)); }
      pb = v4u{pk2(p[0], p[1]), pk2(p[2], p[3]), pk2(p[4], p[5]), pk2(p[6], p[7])};
      float l = ((p[0] + p[1]) + (p[2] + p[3])) + ((p[4] + p[5]) + (p[6] + p[7]));
      l = sw16_add(l, l); lsum = sw32_add(l, l);
      if (lane < 4) *(LAS f32x2*)(L + kb + 4096u + 8u * lane) = f32x2{m2, lsum};
    }
    {
      const u32 q4 = c16 >> 2, p4 = (u32)lane & 3u, rw = 4u * r4 + q4;
      u32 o = vb + 256u * rw + 32u * (rw & 7u) + 8u * p4;
      u32 mo = kb + 528u * c16 + 16u * r4;
      asm volatile("s_waitcnt vmcnt(4)" : "+v"(pb), "+v"(o), "+v"(mo)::"memory");
      STAMP(8);
      v4u a[8]; f32x4 d[8];
#pragma unroll
      for (int c = 0; c < 8; ++c) a[c] = tr128(L, o ^ (32u * c), (o ^ (32u * c)) + 4096u);
      SB();
#pragma unroll
      for (int c = 0; c < 8; ++c) d[c] = mfma16(a[c], pb, f32x4{0.f, 0.f, 0.f, 0.f});
      SB();
      if (c16 < 4u) {
#pragma unroll
        for (int c = 0; c < 8; ++c) *(LAS f32x4*)(L + mo + 64u * c) = d[c];
      }
    }
    BAR();
    STAMP(9);
    if (w < 4) {
      const f32x2 ml = *(LAS f32x2*)(L + L_S0 + 8192u * ((u32)lane & 7u) + 4096u + 8u * w);
      f32x2 ov[8];
#pragma unroll
      for (int v = 0; v < 8; ++v) ov[v] = *(LAS f32x2*)(L + L_S0 + 8192u * v + 528u * w + 8u * lane);
      SB();
      float M = ml[0];
      M = fmaxf(M, dppf<XOR1>(M)); M = fmaxf(M, dppf<XOR2>(M)); M = fmaxf(M, dppf<HMIR>(M));
      const float e = __builtin_amdgcn_exp2f(ml[0] - M);
      float Ls = e * ml[1];
      Ls += dppf<XOR1>(Ls); Ls += dppf<XOR2>(Ls); Ls += dppf<HMIR>(Ls);
      float o0 = 0.f, o1 = 0.f;
#pragma unroll
      for (int v = 0; v < 8; ++v) {
        const float ev_ = __builtin_bit_cast(float, __builtin_amdgcn_readlane(__builtin_bit_cast(int, e), v));
        o0 = fmaf(ev_, ov[v][0], o0); o1 = fmaf(ev_, ov[v][1], o1);
      }
      const u32 rec = ((u32)b * 32u + 4u * kvh + w) * 8u + j;
      st_sc1(ws + WS_OB + rec * 1024u, 16u * lane, v4u{__float_as_uint(o0), ta, __float_as_uint(o1), tb});
      if (lane == 0) st_sc1(ws + WS_MLB + rec * 128u, 0u, v4u{__float_as_uint(M), ta, __float_as_uint(Ls), tb});
    }
    STAMP(10);
    constexpr u32 L_IMG3 = L_S2;
    {
      const u32 bb = (u32)w >> 2, jj = ((u32)lane & 3u) | (((u32)lane >> 5) << 2), gl = ((u32)lane >> 2) & 7u;
      const char* osrc = ws + WS_OB + (bb * 32u + hh_) * 8192u + (u32)(w & 3) * 256u;
      const char* msrc = ws + WS_MLB + (bb * 32u + hh_) * 1024u;
      const u32 oo = jj * 1024u + gl * 16u, mo = jj * 128u;
      v4u f = {0u, ~ta, 0u, ~tb}, d0 = f, d1 = f;
      NAP(NAP_HOP);
      for (int it = 0; it < SPIN_CAP; ++it) {
        if (tagbad(d0, ta, tb, tagbad(d1, ta, tb, tagbad(f, ta, tb))))
          asm volatile("global_load_dwordx4 %0, %3, %5 sc1\n\tglobal_load_dwordx4 %1, %3, %5 offset:128 sc1\n\tglobal_load_dwordx4 %2, %4, %6 sc1\n\ts_waitcnt vmcnt(0)"
                       : "+v"(d0), "+v"(d1), "+v"(f) : "v"(oo), "v"(mo), "s"(osrc), "s"(msrc) : "memory");
        if (wave_ok(tagbad(d0, ta, tb, tagbad(d1, ta, tb, tagbad(f, ta, tb))))) break;
        NAP(NAP_RETRY);
      }
      STAMP(11);
      float M = __uint_as_float(f.x);
      M = fmaxf(M, dppf<XOR1>(M)); M = fmaxf(M, dppf<XOR2>(M)); M = sw32_max(M);
      const float e = __builtin_amdgcn_exp2f(__uint_as_float(f.x) - M);
      float Ls = e * __uint_as_float(f.z);
      Ls += dppf<XOR1>(Ls); Ls += dppf<XOR2>(Ls); Ls = sw32_add(Ls, Ls);
      const float wi = e * __builtin_amdgcn_rcpf(Ls);
      float v0 = sw32_add(wi * __uint_as_float(d0.x), wi * __uint_as_float(d1.x));
      float v1 = sw32_add(wi * __uint_as_float(d0.z), wi * __uint_as_float(d1.z));
      v0 += dppf<XOR1>(v0); v1 += dppf<XOR1>(v1); v0 += dppf<XOR2>(v0); v1 += dppf<XOR2>(v1);
      *(LAS u32*)(L + L_IMG3 + 272u * bb + 4u * ((u32)(w & 3) * 16u + ((u32)lane >> 5) * 8u + gl)) = pk2(v0, v1);
    }
    BAR();
    {
      v4u a[4], bf[4];
      read_wfrags(L, L_S4, lane, w, a);
      read_bfrags<1>(L, L_IMG3, lane, bf);
      publish_d(ws + WS_OUTP + ((4u + qq_) * 32u + hh_) * 2048u, mv4(a, bf), lane, w, ta, tb);
    }
    STAMP(17);
    if (idx < 32) { NAP(NAP_HOP); final_reduce(ws, out, L, L_IMG3 + 4096u, 1, idx, lane, w, ta, tb); }
    STAMP(18);
  }
  if (CAL_STAMP == 99) { for (;;) { u64 t_; asm volatile("s_memtime %0\n\ts_waitcnt lgkmcnt(0)" : "=s"(t_)::"memory"); if (t_ - c0_ >= 96000ull) break; __builtin_amdgcn_s_sleep(1); } }
  else { for (;;) { u64 t_; asm volatile("s_memrealtime %0\n\ts_waitcnt lgkmcnt(0)" : "=s"(t_)::"memory"); if ((u32)t_ - cal0 >= 4000u + calx) break; __builtin_amdgcn_s_sleep(1); } }
}

extern "C" void kernel_launch(void* const* d_in, const int* in_sizes, int n_in, void* d_out, int out_size, void* d_ws, size_t ws_size,
                              hipStream_t stream) {
  fused_decode<<<256, 512, 0, stream>>>((const u16*)d_in[0], (const u16*)d_in[1], (const u16*)d_in[2], (const u16*)d_in[3], (const u16*)d_in[4],
                                        (const u16*)d_in[5], (const u16*)d_in[6], (const u16*)d_in[7], (const u16*)d_in[8], (const u16*)d_in[9],
                                        (const u16*)d_in[10], (u16*)d_out, (char*)d_ws);
}
```

```cpp
#define CAL_STAMP 98
#define CAL_ROLE 0
#define CAL_RANK 0
#include <hip/hip_runtime.h>
#include <stdint.h>

typedef unsigned u32;
typedef unsigned long long u64;
typedef unsigned short u16;
typedef u32 v4u __attribute__((ext_vector_type(4)));
typedef u32 v2u __attribute__((ext_vector_type(2)));
typedef float f32x2 __attribute__((ext_vector_type(2)));
typedef float f32x4 __attribute__((ext_vector_type(4)));
typedef float f32x16 __attribute__((ext_vector_type(16)));
typedef __bf16 bf16x8 __attribute__((ext_vector_type(8)));
typedef __bf16 bf16x2 __attribute__((ext_vector_type(2)));
typedef short s16x4 __attribute__((ext_vector_type(4)));
#define GAS __attribute__((address_space(1)))
#define LAS __attribute__((address_space(3)))
#define DI __device__ __forceinline__

constexpr u32 WS_E = 0;
constexpr u32 WS_QA = 4096;
constexpr u32 WS_QB = WS_QA + 262144;
constexpr u32 WS_QABS = WS_QB + 262144;
constexpr u32 WS_MLA = WS_QABS + 131072;
constexpr u32 WS_CTX = WS_MLA + 65536;
constexpr u32 WS_OB = WS_CTX + 262144;
constexpr u32 WS_MLB = WS_OB + 524288;
constexpr u32 WS_OUTP = WS_MLB + 8192;
constexpr u32 WS_OA = 2u << 20;
constexpr u32 WS_DBG = 8u << 20;
static_assert(WS_OUTP + 524288 <= WS_OA, "ws map");

constexpr float SC2 = 0.08838834764831845f * 1.4426950408889634f;
constexpr int SPIN_CAP = 1 << 17;

constexpr u32 L_S0 = 0, L_S1 = 32768, L_S2 = 65536, L_S3 = 98304, L_S4 = 131072;

#define STAMP(k) do { if ((k) == 0 || (k) == CAL_STAMP) { u64 t_; asm volatile("s_memrealtime %0\n\ts_waitcnt lgkmcnt(0)" : "=s"(t_)::"memory"); if ((k) == CAL_STAMP && CAL_ROLE == roleB) calx = (u32)t_ - cal0; } } while (0)
#define STAMPV(k, v)

#define WAITVM(n) asm volatile("s_waitcnt vmcnt(%0)" ::"i"(n) : "memory")
#define BAR() asm volatile("s_waitcnt lgkmcnt(0)\n\ts_barrier" ::: "memory")

template <int CTRL> DI float dppf(float x) {
  return __builtin_bit_cast(float, __builtin_amdgcn_mov_dpp(__builtin_bit_cast(int, x), CTRL, 0xf, 0xf, true));
}
constexpr int XOR1 = 0xB1, XOR2 = 0x4E, HMIR = 0x141, ROR4 = 0x124, ROR8 = 0x128;
DI float sw32_add(float a, float b) { auto s = __builtin_amdgcn_permlane32_swap(__float_as_uint(a), __float_as_uint(b), false, false); return __uint_as_float(s[0]) + __uint_as_float(s[1]); }
DI float sw16_add(float a, float b) { auto s = __builtin_amdgcn_permlane16_swap(__float_as_uint(a), __float_as_uint(b), false, false); return __uint_as_float(s[0]) + __uint_as_float(s[1]); }
DI float sw32_max(float a) { auto s = __builtin_amdgcn_permlane32_swap(__float_as_uint(a), __float_as_uint(a), false, false); return fmaxf(__uint_as_float(s[0]), __uint_as_float(s[1])); }
DI float sw16_max(float a) { auto s = __builtin_amdgcn_permlane16_swap(__float_as_uint(a), __float_as_uint(a), false, false); return fmaxf(__uint_as_float(s[0]), __uint_as_float(s[1])); }
DI u32 pk2(float lo, float hi) { f32x2 p = {lo, hi}; return __builtin_bit_cast(u32, __builtin_convertvector(p, bf16x2)); }
DI float bflo(u32 x) { return __uint_as_float(x << 16); }
DI float bfhi(u32 x) { return __uint_as_float(x & 0xffff0000u); }
DI f32x4 mfma16(v4u a, v4u b, f32x4 c) { return __builtin_amdgcn_mfma_f32_16x16x32_bf16(__builtin_bit_cast(bf16x8, a), __builtin_bit_cast(bf16x8, b), c, 0, 0, 0); }
DI f32x16 mfma32(v4u a, v4u b, f32x16 c) { return __builtin_amdgcn_mfma_f32_32x32x16_bf16(__builtin_bit_cast(bf16x8, a), __builtin_bit_cast(bf16x8, b), c, 0, 0, 0); }

DI v4u lds128(LAS char* L, u32 off) { return *(LAS v4u*)(L + off); }
DI v2u tr64(LAS char* L, u32 off) { return __builtin_bit_cast(v2u, __builtin_amdgcn_ds_read_tr16_b64_v4i16((LAS s16x4*)(L + off))); }
DI v4u tr128(LAS char* L, u32 o0, u32 o1) { v2u a = tr64(L, o0), b = tr64(L, o1); return v4u{a.x, a.y, b.x, b.y}; }

DI void dma4(const void* sbase, u32 v0, u32 v1, u32 v2, u32 v3, u32 lds) {
  asm volatile("s_mov_b32 m0, %5\n\ts_nop 0\n\t"
               "global_load_lds_dwordx4 %0, %4 nt\n\t"
               "global_load_lds_dwordx4 %1, %4 offset:1024 nt\n\t"
               "global_load_lds_dwordx4 %2, %4 offset:2048 nt\n\t"
               "global_load_lds_dwordx4 %3, %4 offset:3072 nt"
               ::"v"(v0), "v"(v1), "v"(v2), "v"(v3), "s"(sbase), "s"(lds) : "memory");
}
DI void dma2a(const void* sbase, u32 v0, u32 v1, u32 lds) {
  asm volatile("s_mov_b32 m0, %3\n\ts_nop 0\n\t"
               "global_load_lds_dwordx4 %0, %2 nt\n\t"
               "global_load_lds_dwordx4 %1, %2 offset:1024 nt"
               ::"v"(v0), "v"(v1), "s"(sbase), "s"(lds) : "memory");
}
DI void dma2b(const void* sbase, u32 v2, u32 v3, u32 lds) {
  asm volatile("s_mov_b32 m0, %3\n\ts_nop 0\n\t"
               "global_load_lds_dwordx4 %0, %2 offset:2048 nt\n\t"
               "global_load_lds_dwordx4 %1, %2 offset:3072 nt"
               ::"v"(v2), "v"(v3), "s"(sbase), "s"(lds) : "memory");
}
template <int OFF> DI void ld_sc1(v4u& d, const void* sbase, u32 voff) {
  asm volatile("global_load_dwordx4 %0, %1, %2 offset:%3 sc1" : "=v"(d) : "v"(voff), "s"(sbase), "i"(OFF) : "memory");
}
template <int OFF> DI void ld_plain(v4u& d, const void* sbase, u32 voff) {
  asm volatile("global_load_dwordx4 %0, %1, %2 offset:%3" : "=v"(d) : "v"(voff), "s"(sbase), "i"(OFF) : "memory");
}
DI void st_sc1(void* sbase, u32 voff, v4u d) {
  asm volatile("global_store_dwordx4 %0, %1, %2 sc1\n\ts_nop 1" ::"v"(voff), "v"(d), "s"(sbase) : "memory");
}
#define PIN1(a) asm volatile("" : "+v"(a))
DI u32 tagbad(const v4u& g, u32 ta, u32 tb) { return (g.y ^ ta) | (g.w ^ tb); }
DI bool wave_ok(u32 bad) { return __builtin_amdgcn_ballot_w64(bad != 0u) == 0ull; }

struct Ctx { LAS char* L; char* ws; int lane, w; u32 ta, tb; };

DI void dma_wblock(const char* blk, u32 rstride, u32 lds, int lane, int w) {
  const u32 r = (u32)lane >> 4, c = (u32)lane & 15u;
  const u32 b = (16u * w + r) * rstride;
  const u32 x = c ^ (r << 2);
  dma4(blk, b + 16u * x, b + 4u * rstride - 1024u + 16u * (x ^ 1u), b + 8u * rstride - 2048u + 16u * (x ^ 2u), b + 12u * rstride - 3072u + 16u * (x ^ 3u),
       lds + 4096u * w);
}
DI void read_wfrags(LAS char* L, u32 lds, int lane, int w, v4u (&a)[4]) {
  const u32 g = (u32)lane >> 4, q = ((u32)lane & 15u) >> 2, p = (u32)lane & 3u;
  const u32 ch = 2u * w + (p >> 1);
  const u32 o0 = lds + 256u * (8u * g + q) + 16u * (ch ^ ((q << 2) | ((2u * g) & 3u))) + 8u * (p & 1u);
  const u32 o1 = lds + 256u * (8u * g + 4u + q) + 16u * (ch ^ ((q << 2) | ((2u * g + 1u) & 3u))) + 8u * (p & 1u);
#pragma unroll
  for (int ks = 0; ks < 4; ++ks) a[ks] = tr128(L, o0 + 8192u * ks, o1 + 8192u * ks);
}
template <int MASK> DI void read_bfrags(LAS char* L, u32 img, int lane, v4u (&b)[4]) {
  const u32 o = img + 272u * ((u32)lane & MASK) + 16u * ((u32)lane >> 4);
#pragma unroll
  for (int s = 0; s < 4; ++s) b[s] = lds128(L, o + 64u * s);
}
DI void publish_d(void* dst, f32x4 d, int lane, int w, u32 ta, u32 tb) {
  const u32 col = (u32)lane & 15u, g = (u32)lane >> 4, pr = (col >> 1) & 1u;
  const float v0 = pr ? d[2] : d[0], v1 = pr ? d[3] : d[1];
  if (col < 4u) st_sc1(dst, (col & 1u) * 1024u + (8u * w + 2u * g + pr) * 16u, v4u{__float_as_uint(v0), ta, __float_as_uint(v1), tb});
}
DI f32x4 mv4(const v4u (&a)[4], const v4u (&b)[4]) {
  f32x4 d = {0.f, 0.f, 0.f, 0.f};
#pragma unroll
  for (int s = 0; s < 4; ++s) d = mfma16(a[s], b[s], d);
  return d;
}
DI void gather4(const char* src, LAS char* L, u32 img, int lane, int w, u32 ta, u32 tb) {
  const u32 gi = (16u * w + ((u32)lane >> 2)) & 63u, b = (u32)w >> 2;
  const u32 off = ((u32)lane & 3u) * 2048u + b * 1024u + gi * 16u;
  v4u g;
  for (int it = 0;; ++it) {
    ld_sc1<0>(g, src, off);
    asm volatile("s_waitcnt vmcnt(0)" : "+v"(g)::"memory");
    if (wave_ok(tagbad(g, ta, tb)) || it > SPIN_CAP) break;
    __builtin_amdgcn_s_sleep(1);
  }
  float v0 = __uint_as_float(g.x), v1 = __uint_as_float(g.z);
  v0 += dppf<XOR1>(v0); v1 += dppf<XOR1>(v1);
  v0 += dppf<XOR2>(v0); v1 += dppf<XOR2>(v1);
  *(LAS u32*)(L + img + 272u * b + 4u * gi) = pk2(v0, v1);
}
DI void final_reduce(const char* ws, u16* out, int which, int idx, int lane, int w, u32 ta, u32 tb) {
  const u32 mq = (u32)idx >> 5, sub = (u32)idx & 31u;
  const char* src = ws + WS_OUTP + (which * 4u + mq) * 65536u;
  const u32 off = ((u32)lane >> 1) * 2048u + (u32)w * 1024u + (2u * sub + ((u32)lane & 1u)) * 16u;
  v4u g;
  for (int it = 0;; ++it) {
    ld_sc1<0>(g, src, off);
    asm volatile("s_waitcnt vmcnt(0)" : "+v"(g)::"memory");
    if (wave_ok(tagbad(g, ta, tb)) || it > SPIN_CAP) break;
    __builtin_amdgcn_s_sleep(1);
  }
  float v0 = __uint_as_float(g.x), v1 = __uint_as_float(g.z);
  v0 += dppf<XOR2>(v0); v1 += dppf<XOR2>(v1);
  v0 += dppf<ROR4>(v0); v1 += dppf<ROR4>(v1);
  v0 += dppf<ROR8>(v0); v1 += dppf<ROR8>(v1);
  v0 = sw16_add(v0, v0); v1 = sw16_add(v1, v1);
  v0 = sw32_add(v0, v0); v1 = sw32_add(v1, v1);
  if (lane < 2) *(GAS u32*)((GAS char*)out + ((u32)w * 1024u + which * 512u + 128u * mq + 4u * sub + 2u * lane) * 2u) = pk2(v0, v1);
}

typedef u32 v16u __attribute__((ext_vector_type(16)));
typedef u32 v8u __attribute__((ext_vector_type(8)));
DI const u16* mkp(u32 lo, u32 hi) { return (const u16*)(((u64)hi << 32) | lo); }
__global__ void __launch_bounds__(512, 2)
fused_decode(const u16* p0, const u16* p1, const u16* p2, const u16* p3, const u16* p4, const u16* p5, const u16* p6, const u16* p7,
             const u16* p8, const u16* p9, const u16* p10, u16* p11, char* p12) {
  u64 tt0; asm volatile("s_memrealtime %0\n\ts_waitcnt lgkmcnt(0)" : "=s"(tt0)::"memory");
  v16u ka0; v8u ka1; v2u ka2;
  asm volatile("s_load_dwordx16 %0, %3, 0x0\n\ts_load_dwordx8 %1, %3, 0x40\n\ts_load_dwordx2 %2, %3, 0x60\n\ts_waitcnt lgkmcnt(0)"
               : "=&s"(ka0), "=&s"(ka1), "=&s"(ka2) : "s"(__builtin_amdgcn_kernarg_segment_ptr()) : "memory");
  const u16 *xA = mkp(ka0[0], ka0[1]), *ckv = mkp(ka0[2], ka0[3]), *wq = mkp(ka0[4], ka0[5]), *wuk = mkp(ka0[6], ka0[7]);
  const u16 *wuv = mkp(ka0[8], ka0[9]), *wo = mkp(ka0[10], ka0[11]), *xB = mkp(ka0[12], ka0[13]), *Kc = mkp(ka0[14], ka0[15]);
  const u16 *Vc = mkp(ka1[0], ka1[1]), *wqB = mkp(ka1[2], ka1[3]), *woB = mkp(ka1[4], ka1[5]);
  u16* out = (u16*)mkp(ka1[6], ka1[7]); char* ws = (char*)mkp(ka2[0], ka2[1]);
  __shared__ __attribute__((aligned(16))) char smem[163840];
  LAS char* L = (LAS char*)smem;
  const int lane = threadIdx.x & 63, w = __builtin_amdgcn_readfirstlane(threadIdx.x >> 6);
  const int bid = blockIdx.x, roleB = (bid >> 3) & 1, idx = ((bid >> 4) << 3) | (bid & 7);
  const int hh_ = (idx & 7) + 8 * (idx >> 5), qq_ = (idx >> 3) & 3;
  u32 cal0 = (u32)tt0, calx = 0; u64 c0_; asm volatile("s_memtime %0\n\ts_waitcnt lgkmcnt(0)" : "=s"(c0_)::"memory");
  STAMP(0);
  {
    u64 pc; asm volatile("s_getpc_b64 %0" : "=s"(pc));
    pc &= ~(u64)0x7f;
    if (w >= 5) {
      asm volatile("s_mov_b32 m0, %2\n\ts_nop 0\n\tglobal_load_lds_dword %0, %1"
                   ::"v"((u32)lane * 128u + (u32)(w - 5) * 8192u), "s"(pc), "s"(L_S3 + 4096u * (u32)w) : "memory");
    } else {
      asm volatile("s_mov_b32 m0, %2\n\ts_nop 0\n\tglobal_load_lds_dword %0, %1" ::"v"(0u), "s"(pc), "s"(L_S3 + 4096u * (u32)w) : "memory");
    }
  }
  const u32 xo = ((u32)lane & 1u) * 1024u + (u32)qq_ * 256u + ((u32)lane >> 4) * 16u;
#define LOAD_EX(x) \
  u32 ev; v4u xf[4]; \
  asm volatile("global_load_dword %0, %1, %2 sc1" : "=v"(ev) : "v"(0u), "s"(ws) : "memory"); \
  ld_plain<0>(xf[0], x, xo); ld_plain<64>(xf[1], x, xo); ld_plain<128>(xf[2], x, xo); ld_plain<192>(xf[3], x, xo);
  asm volatile("" ::"s"(xA), "s"(ckv), "s"(wq), "s"(wuk), "s"(wuv), "s"(wo), "s"(xB), "s"(Kc), "s"(Vc), "s"(wqB), "s"(woB), "s"(out), "s"(ws));
  u32 ta, tb, e0;

  if (!roleB) {
    const int b = (idx & 7) >> 2, sp = (idx >> 3) * 4 + (idx & 3);
    const char* kv = (const char*)ckv + ((size_t)b * 8192 + (size_t)sp * 128 + 4u * w) * 1024u;
    const u32 kx = (16u * lane) ^ (16u * (w & 3));
    const u32 k0 = kx, k1 = kx ^ 64u, k2 = kx ^ 128u, k3 = kx ^ 192u;
    const u32 lw = 4096u * w;
    LOAD_EX(xA)
    dma_wblock((const char*)wq + (size_t)qq_ * 128 * 8192 + hh_ * 256, 8192u, L_S4, lane, w);
    dma2a(kv, k0, k1, L_S0 + lw);
    asm volatile("s_waitcnt vmcnt(2)" : "+v"(ev), "+v"(xf[0]), "+v"(xf[1]), "+v"(xf[2]), "+v"(xf[3])::"memory");
    STAMP(1);
    dma2b(kv, k2, k3, L_S0 + lw);
    e0 = __builtin_amdgcn_readfirstlane(ev);
    ta = e0 * 0x9E3779B1u + 0x7F4A7C15u; tb = (e0 ^ (e0 >> 15)) * 0x85EBCA77u + 0x3C6EF372u;
    BAR();
    {
      v4u a[4]; read_wfrags(L, L_S4, lane, w, a);
      f32x4 d = mv4(a, xf);
      publish_d(ws + WS_QA + ((u32)hh_ * 4u + qq_) * 2048u, d, lane, w, ta, tb);
    }
    STAMP(2);
    dma4(kv + 32768, k0, k1, k2, k3, L_S1 + lw);
    dma4(kv + 65536, k0, k1, k2, k3, L_S2 + lw);
    const int kt = w & 3, ch = w >> 2;
    v4u ka[16], vt[8][2];
    const u32 r31 = (u32)lane & 31u, hf = (u32)lane >> 5;
    const u32 fr = ((r31 & 3u) << 2) | ((r31 >> 2) & 3u);
    const u32 rowo = 1024u * r31 + 16u * (hf ^ fr) + 512u * ch;
    const u32 blk = ((u32)lane >> 4) & 1u, q4 = ((u32)lane & 15u) >> 2, p4 = (u32)lane & 3u;
    const u32 chx = 8u * w + 2u * blk + (p4 >> 1);
    const u32 tro0 = 1024u * (8u * hf + q4) + 16u * (chx ^ ((q4 << 2) | (2u * hf))) + 8u * (p4 & 1u);
    const u32 tro1 = 1024u * (8u * hf + 4u + q4) + 16u * (chx ^ ((q4 << 2) | (2u * hf + 1u))) + 8u * (p4 & 1u);
#define READ_TILE(t)                                                                                          \
    {                                                                                                         \
      if (kt == (t)) {                                                                                        \
        _Pragma("unroll") for (int s = 0; s < 16; ++s) ka[s] = lds128(L, 32768u * (t) + ((rowo ^ (32u * (s & 7))) + 256u * (s >> 3))); \
      }                                                                                                       \
      _Pragma("unroll") for (int ks = 0; ks < 2; ++ks)                                                        \
        _Pragma("unroll") for (int u = 0; u < 2; ++u)                                                         \
          vt[2 * (t) + ks][u] = tr128(L, 32768u * (t) + 16384u * ks + (tro0 ^ (64u * u)), 32768u * (t) + 16384u * ks + (tro1 ^ (64u * u))); \
    }
#pragma unroll
    for (int s = 0; s < 16; ++s) ka[s] = v4u{0u, 0u, 0u, 0u};
    WAITVM(8); BAR(); STAMP(3);
    READ_TILE(0)
    dma4(kv + 98304, k0, k1, k2, k3, L_S3 + lw);
    WAITVM(8); BAR(); STAMP(4);
    READ_TILE(1)
    v4u g[8];
    const char* qsrc = ws + WS_QABS + (u32)b * 65536u + (u32)w * 8192u;
    const u32 go = 16u * lane;
#define GATHER_Q() { ld_sc1<0>(g[0], qsrc, go); ld_sc1<1024>(g[1], qsrc, go); ld_sc1<2048>(g[2], qsrc, go); ld_sc1<3072>(g[3], qsrc, go); \
                     ld_sc1<0>(g[4], qsrc + 4096, go); ld_sc1<1024>(g[5], qsrc + 4096, go); ld_sc1<2048>(g[6], qsrc + 4096, go); ld_sc1<3072>(g[7], qsrc + 4096, go); }
    GATHER_Q()
    WAITVM(12); BAR(); STAMP(5);
    READ_TILE(2)
    WAITVM(8); BAR(); STAMP(6);
    READ_TILE(3)
#define WAIT_Q() asm volatile("s_waitcnt vmcnt(0)" : "+v"(g[0]), "+v"(g[1]), "+v"(g[2]), "+v"(g[3]), "+v"(g[4]), "+v"(g[5]), "+v"(g[6]), "+v"(g[7])::"memory")
#define BAD_Q() (tagbad(g[0], ta, tb) | tagbad(g[1], ta, tb) | tagbad(g[2], ta, tb) | tagbad(g[3], ta, tb) | tagbad(g[4], ta, tb) | tagbad(g[5], ta, tb) | tagbad(g[6], ta, tb) | tagbad(g[7], ta, tb))
    WAIT_Q();
    if (!wave_ok(BAD_Q())) {
      for (int it = 0; it < SPIN_CAP; ++it) {
        __builtin_amdgcn_s_sleep(2);
        GATHER_Q()
        WAIT_Q();
        STAMPV(30, it + 1);
        if (wave_ok(BAD_Q())) break;
      }
    }
    STAMP(7);
#pragma unroll
    for (int i = 0; i < 8; ++i) {
      const u32 gi = 64u * (i & 1) + lane, f = ((u32)(i >> 1) << 2) | (u32)(w & 3);
      *(LAS v2u*)(L + L_S4 + 1024u * (4u * w + (i >> 1)) + 16u * ((gi >> 1) ^ f) + 8u * (gi & 1u)) = v2u{g[i].x, g[i].z};
    }
    BAR();
    STAMP(8);
    dma_wblock((const char*)wuv + (size_t)qq_ * 128 * 8192 + hh_ * 256, 8192u, L_S2, lane, w);
    {
      f32x16 acc;
#pragma unroll
      for (int i = 0; i < 16; ++i) acc[i] = 0.f;
#pragma unroll
      for (int s = 0; s < 16; ++s) acc = mfma32(ka[s], lds128(L, L_S4 + ((rowo ^ (32u * (s & 7))) + 256u * (s >> 3))), acc);
      const u32 so = L_S0 + 16896u * ch + 528u * r31 + 128u * kt + 16u * hf;
#pragma unroll
      for (int a = 0; a < 4; ++a) *(LAS f32x4*)(L + so + 32u * a) = f32x4{acc[4 * a], acc[4 * a + 1], acc[4 * a + 2], acc[4 * a + 3]};
    }
    BAR();
    STAMP(9);
    constexpr u32 L_P = L_S1 + 16384;
    float m2, lsum;
    {
      const u32 hd = 4u * w + ((u32)lane >> 4), j = (u32)lane & 15u;
      const u32 so = L_S0 + 528u * hd + 32u * j;
      const f32x4 s0 = *(LAS f32x4*)(L + so) + *(LAS f32x4*)(L + so + 16896u);
      const f32x4 s1 = *(LAS f32x4*)(L + so + 16u) + *(LAS f32x4*)(L + so + 16896u + 16u);
      float m = fmaxf(fmaxf(fmaxf(s0[0], s0[1]), fmaxf(s0[2], s0[3])), fmaxf(fmaxf(s1[0], s1[1]), fmaxf(s1[2], s1[3])));
      m = fmaxf(m, dppf<XOR1>(m)); m = fmaxf(m, dppf<XOR2>(m)); m = fmaxf(m, dppf<ROR4>(m)); m = fmaxf(m, dppf<ROR8>(m));
      m2 = m * SC2;
      float p[8];
#pragma unroll
      for (int i = 0; i < 4; ++i) { p[i] = __builtin_amdgcn_exp2f(fmaf(s0[i], SC2, -m2)); p[4 + i] = __builtin_amdgcn_exp2f(fmaf(s1[i], SC2, -m2)); }
      *(LAS v4u*)(L + L_P + 256u * hd + 16u * (j ^ (hd & 15u))) = v4u{pk2(p[0], p[1]), pk2(p[2], p[3]), pk2(p[4], p[5]), pk2(p[6], p[7])};
      float l = ((p[0] + p[1]) + (p[2] + p[3])) + ((p[4] + p[5]) + (p[6] + p[7]));
      l += dppf<XOR1>(l); l += dppf<XOR2>(l); l += dppf<ROR4>(l); l += dppf<ROR8>(l);
      lsum = l;
    }
    BAR();
    STAMP(10);
    {
      f32x16 o0, o1;
#pragma unroll
      for (int i = 0; i < 16; ++i) { o0[i] = 0.f; o1[i] = 0.f; }
      const u32 po = L_P + 256u * r31 + 16u * (hf ^ (r31 & 15u));
#pragma unroll
      for (int s = 0; s < 8; ++s) {
        const v4u pf = lds128(L, po ^ (32u * s));
        o0 = mfma32(vt[s][0], pf, o0);
        o1 = mfma32(vt[s][1], pf, o1);
      }
      const u32 sb = L_S3 + lw + 128u * r31 + 8u * hf, sx = 16u * (r31 & 7u);
#pragma unroll
      for (int a = 0; a < 4; ++a) {
        *(LAS v2u*)(L + sb + ((16u * a) ^ sx)) = v2u{pk2(o0[4 * a], o0[4 * a + 1]), pk2(o0[4 * a + 2], o0[4 * a + 3])};
        *(LAS v2u*)(L + sb + ((64u + 16u * a) ^ sx)) = v2u{pk2(o1[4 * a], o1[4 * a + 1]), pk2(o1[4 * a + 2], o1[4 * a + 3])};
      }
      const u32 rr = (u32)lane >> 3, kk = (u32)lane & 7u;
      char* od = ws + WS_OA + (((u32)b * 32u * 4u + (u32)(w >> 1)) * 64u + sp) * 256u + (u32)(w & 1) * 128u;
      v4u t[4];
#pragma unroll
      for (int i = 0; i < 4; ++i) t[i] = lds128(L, L_S3 + lw + 1024u * i + 128u * rr + 16u * (kk ^ rr));
#pragma unroll
      for (int i = 0; i < 4; ++i) st_sc1(od, (8u * i + rr) * 65536u + 16u * kk, t[i]);
    }
    STAMP(11);
    WAITVM(0); BAR();
    STAMP(12);
    if ((lane & 15) == 0)
      st_sc1(ws + WS_MLA + (u32)b * 32768u + (u32)sp * 16u, (4u * w + ((u32)lane >> 4)) * 1024u, v4u{__float_as_uint(m2), ta, __float_as_uint(lsum), tb});
    v4u wa[4];
    read_wfrags(L, L_S2, lane, w, wa);
    constexpr u32 L_IMG = L_S4;
    {
      const u32 bb = (u32)w >> 2, cs = (u32)w & 3u;
      const u32 rec = ((u32)lane & 3u) | (((u32)lane >> 4) << 2), chk = ((u32)lane >> 2) & 3u;
      const char* fsrc = ws + WS_MLA + (bb * 32u + hh_) * 1024u;
      v4u f[4];
      for (int it = 0;; ++it) {
        ld_sc1<0>(f[0], fsrc, rec * 16u); ld_sc1<256>(f[1], fsrc, rec * 16u); ld_sc1<512>(f[2], fsrc, rec * 16u); ld_sc1<768>(f[3], fsrc, rec * 16u);
        asm volatile("s_waitcnt vmcnt(0)" : "+v"(f[0]), "+v"(f[1]), "+v"(f[2]), "+v"(f[3])::"memory");
        if (wave_ok(tagbad(f[0], ta, tb) | tagbad(f[1], ta, tb) | tagbad(f[2], ta, tb) | tagbad(f[3], ta, tb)) || it > SPIN_CAP) break;
        __builtin_amdgcn_s_sleep(1);
      }
      STAMP(13);
      const char* psrc = ws + WS_OA + ((bb * 32u + hh_) * 4u + qq_) * 16384u + cs * 64u;
      const u32 po = rec * 256u + chk * 16u;
      v4u d[4];
      ld_sc1<0>(d[0], psrc, po); ld_sc1<0>(d[1], psrc + 4096, po); ld_sc1<0>(d[2], psrc + 8192, po); ld_sc1<0>(d[3], psrc + 12288, po);
      dma_wblock((const char*)wo + (size_t)hh_ * 128 * 1024 + qq_ * 256, 1024u, L_S0, lane, w);
      float M = fmaxf(fmaxf(__uint_as_float(f[0].x), __uint_as_float(f[1].x)), fmaxf(__uint_as_float(f[2].x), __uint_as_float(f[3].x)));
      M = fmaxf(M, dppf<XOR1>(M)); M = fmaxf(M, dppf<XOR2>(M)); M = sw16_max(M); M = sw32_max(M);
      float e[4], Ls = 0.f;
#pragma unroll
      for (int i = 0; i < 4; ++i) { e[i] = __builtin_amdgcn_exp2f(__uint_as_float(f[i].x) - M); Ls = fmaf(e[i], __uint_as_float(f[i].z), Ls); }
      Ls += dppf<XOR1>(Ls); Ls += dppf<XOR2>(Ls); Ls = sw16_add(Ls, Ls); Ls = sw32_add(Ls, Ls);
      const float inv = __builtin_amdgcn_rcpf(Ls);
      asm volatile("s_waitcnt vmcnt(4)" : "+v"(d[0]), "+v"(d[1]), "+v"(d[2]), "+v"(d[3])::"memory");
      STAMP(14);
      float a[8];
#pragma unroll
      for (int k = 0; k < 8; ++k) a[k] = 0.f;
#pragma unroll
      for (int i = 0; i < 4; ++i) {
        const float wi = e[i] * inv;
#pragma unroll
        for (int k = 0; k < 4; ++k) { a[2 * k] = fmaf(wi, bflo(d[i][k]), a[2 * k]); a[2 * k + 1] = fmaf(wi, bfhi(d[i][k]), a[2 * k + 1]); }
      }
      const float r0 = sw32_add(a[0], a[4]), r1 = sw32_add(a[1], a[5]), r2 = sw32_add(a[2], a[6]), r3 = sw32_add(a[3], a[7]);
      float u0 = sw16_add(r0, r2), u1 = sw16_add(r1, r3);
      u0 += dppf<XOR1>(u0); u1 += dppf<XOR1>(u1); u0 += dppf<XOR2>(u0); u1 += dppf<XOR2>(u1);
      const u32 hi = pk2(u0, u1), lo = pk2(u0 - bflo(hi), u1 - bfhi(hi));
      const u32 cl = 32u * cs + 8u * chk + 4u * ((u32)lane >> 5) + 2u * (((u32)lane >> 4) & 1u);
      *(LAS u32*)(L + L_IMG + 272u * (bb + 2u * ((u32)lane & 1u)) + 2u * cl) = (lane & 1) ? lo : hi;
    }
    BAR();
    {
      v4u bf[4]; read_bfrags<3>(L, L_IMG, lane, bf);
      f32x4 d = mv4(wa, bf);
#pragma unroll
      for (int i = 0; i < 4; ++i) d[i] += dppf<XOR2>(d[i]);
      publish_d(ws + WS_CTX + ((u32)hh_ * 4u + qq_) * 2048u, d, lane, w, ta, tb);
    }
    STAMP(15);
    gather4(ws + WS_CTX + (u32)hh_ * 8192u, L, L_IMG + 2048u, lane, w, ta, tb);
    STAMP(16);
    BAR();
    {
      v4u a[4], bf[4];
      read_wfrags(L, L_S0, lane, w, a);
      read_bfrags<1>(L, L_IMG + 2048u, lane, bf);
      publish_d(ws + WS_OUTP + ((u32)qq_ * 32u + hh_) * 2048u, mv4(a, bf), lane, w, ta, tb);
    }
    STAMP(17);
    if (w < 2) {
      final_reduce(ws, out, 0, idx, lane, w, ta, tb);
      if (idx == 0 && w == 0 && lane == 0) __hip_atomic_store((GAS u32*)(ws + WS_E), e0 + 1u, __ATOMIC_RELAXED, __HIP_MEMORY_SCOPE_AGENT);
    }
    STAMP(18);
  } else {
    const u32 r4 = (u32)lane >> 4, c16 = (u32)lane & 15u;
    LOAD_EX(xB)
    {
      const u32 bs = (16u * w + r4) * 8192u, x = c16 ^ r4;
      dma4((const char*)wuk + (size_t)qq_ * 128 * 8192 + hh_ * 256, bs + 16u * x, bs + 32768u - 1024u + 16u * (x ^ 4u), bs + 65536u - 2048u + 16u * (x ^ 8u),
           bs + 98304u - 3072u + 16u * (x ^ 12u), L_S0 + 4096u * w);
    }
    asm volatile("s_waitcnt vmcnt(0)" : "+v"(ev), "+v"(xf[0]), "+v"(xf[1]), "+v"(xf[2]), "+v"(xf[3])::"memory");
    STAMP(1);
    e0 = __builtin_amdgcn_readfirstlane(ev);
    ta = e0 * 0x9E3779B1u + 0x7F4A7C15u; tb = (e0 ^ (e0 >> 15)) * 0x85EBCA77u + 0x3C6EF372u;
    constexpr u32 L_IMG = L_S1;
    v4u uk[4];
    {
      const u32 o = L_S0 + 4096u * w + 256u * c16 + 16u * (r4 ^ c16);
#pragma unroll
      for (int s = 0; s < 4; ++s) uk[s] = lds128(L, o ^ (64u * s));
    }
    gather4(ws + WS_QA + (u32)hh_ * 8192u, L, L_IMG, lane, w, ta, tb);
    STAMP(2);
    dma_wblock((const char*)wqB + (size_t)qq_ * 128 * 8192 + hh_ * 256, 8192u, L_S4, lane, w);
    BAR();
    {
      v4u bf[4]; read_bfrags<1>(L, L_IMG, lane, bf);
      const f32x4 d = mv4(uk, bf);
      if (c16 < 2u)
        st_sc1(ws + WS_QABS + (u32)hh_ * 2048u + (32u * qq_ + 4u * w) * 16u, c16 * 65536u + r4 * 16u, v4u{pk2(d[0], d[1]), ta, pk2(d[2], d[3]), tb});
    }
    STAMP(3);
    WAITVM(0); BAR();
    STAMP(4);
    {
      v4u a[4]; read_wfrags(L, L_S4, lane, w, a);
      publish_d(ws + WS_QB + ((u32)hh_ * 4u + qq_) * 2048u, mv4(a, xf), lane, w, ta, tb);
    }
    STAMP(5);
    const int b = idx >> 6, kvh = (idx >> 3) & 7, j = idx & 7;
    const size_t kvo = (((size_t)b * 8 + kvh) * 8192 + 6144 + 256u * j + 32u * w) * 256u;
    const u32 kb = L_S0 + 8192u * w, vb = L_S2 + 8192u * w;
    {
      const u32 x = c16 ^ r4, bs = 256u * r4;
      dma4((const char*)Kc + kvo, bs + 16u * x, bs + 16u * (x ^ 4u), bs + 16u * (x ^ 8u), bs + 16u * (x ^ 12u), kb);
      dma4((const char*)Kc + kvo + 4096, bs + 16u * x, bs + 16u * (x ^ 4u), bs + 16u * (x ^ 8u), bs + 16u * (x ^ 12u), kb + 4096u);
    }
    v4u g[2];
    const char* qsrc = ws + WS_QB + (4u * kvh + (u32)(w >> 1)) * 8192u + (u32)b * 1024u + (u32)(w & 1) * 512u;
    const u32 go = ((u32)lane & 3u) * 2048u + ((u32)lane >> 2) * 16u;
    ld_sc1<0>(g[0], qsrc, go); ld_sc1<256>(g[1], qsrc, go);
    {
      const u32 bs = 256u * r4 + 16u * (c16 & 1u), x = (c16 >> 1) ^ r4;
      dma4((const char*)Vc + kvo, bs + 32u * x, bs + 32u * (x ^ 4u), bs + 32u * x, bs + 32u * (x ^ 4u), vb);
      dma4((const char*)Vc + kvo + 4096, bs + 32u * x, bs + 32u * (x ^ 4u), bs + 32u * x, bs + 32u * (x ^ 4u), vb + 4096u);
    }
    asm volatile("s_waitcnt vmcnt(8)" : "+v"(g[0]), "+v"(g[1])::"memory");
    STAMP(6);
    v4u kf[2][4];
    {
      const u32 o = kb + 256u * c16 + 16u * (r4 ^ c16);
#pragma unroll
      for (int t = 0; t < 2; ++t)
#pragma unroll
        for (int s = 0; s < 4; ++s) kf[t][s] = lds128(L, (o ^ (64u * s)) + 4096u * t);
    }
    for (int it = 0;; ++it) {
      if (wave_ok(tagbad(g[0], ta, tb) | tagbad(g[1], ta, tb)) || it > SPIN_CAP) { STAMPV(30, it); break; }
      __builtin_amdgcn_s_sleep(1);
      ld_sc1<0>(g[0], qsrc, go); ld_sc1<256>(g[1], qsrc, go);
      asm volatile("s_waitcnt vmcnt(0)" : "+v"(g[0]), "+v"(g[1])::"memory");
    }
    constexpr u32 L_IMG2 = L_S4;
    BAR();
#pragma unroll
    for (int i = 0; i < 2; ++i) {
      float v0 = __uint_as_float(g[i].x), v1 = __uint_as_float(g[i].z);
      v0 += dppf<XOR1>(v0); v1 += dppf<XOR1>(v1); v0 += dppf<XOR2>(v0); v1 += dppf<XOR2>(v1);
      *(LAS u32*)(L + L_IMG2 + 272u * (u32)(w >> 1) + 4u * ((u32)(w & 1) * 32u + 16u * i + ((u32)lane >> 2))) = pk2(v0, v1);
    }
    BAR();
    STAMP(7);
    v4u pb; float m2, lsum;
    {
      v4u bf[4]; read_bfrags<3>(L, L_IMG2, lane, bf);
      const f32x4 s0 = mv4(kf[0], bf), s1 = mv4(kf[1], bf);
      float m = fmaxf(fmaxf(fmaxf(s0[0], s0[1]), fmaxf(s0[2], s0[3])), fmaxf(fmaxf(s1[0], s1[1]), fmaxf(s1[2], s1[3])));
      m = sw16_max(m); m = sw32_max(m);
      m2 = m * SC2;
      float p[8];
#pragma unroll
      for (int i = 0; i < 4; ++i) { p[i] = __builtin_amdgcn_exp2f(fmaf(s0[i], SC2, -m2)); p[4 + i] = __builtin_amdgcn_exp2f(fmaf(s1[i], SC2, -m2)); }
      pb = v4u{pk2(p[0], p[1]), pk2(p[2], p[3]), pk2(p[4], p[5]), pk2(p[6], p[7])};
      float l = ((p[0] + p[1]) + (p[2] + p[3])) + ((p[4] + p[5]) + (p[6] + p[7]));
      l = sw16_add(l, l); lsum = sw32_add(l, l);
    }
    BAR();
    dma_wblock((const char*)woB + (size_t)hh_ * 128 * 1024 + qq_ * 256, 1024u, L_S4, lane, w);
    WAITVM(4);
    STAMP(8);
    {
      const u32 q4 = c16 >> 2, p4 = (u32)lane & 3u, rw = 4u * r4 + q4;
      const u32 o = vb + 256u * rw + 32u * (rw & 7u) + 8u * p4;
#pragma unroll
      for (int c = 0; c < 8; ++c) {
        const v4u a = tr128(L, o ^ (32u * c), (o ^ (32u * c)) + 4096u);
        const f32x4 d = mfma16(a, pb, f32x4{0.f, 0.f, 0.f, 0.f});
        if (c16 < 4u) *(LAS f32x4*)(L + kb + 528u * c16 + 64u * c + 16u * r4) = d;
      }
      if (lane < 4) *(LAS f32x2*)(L + kb + 4096u + 8u * lane) = f32x2{m2, lsum};
    }
    BAR();
    STAMP(9);
    if (w < 4) {
      const f32x2 ml = *(LAS f32x2*)(L + L_S0 + 8192u * ((u32)lane & 7u) + 4096u + 8u * w);
      float M = ml[0];
      M = fmaxf(M, dppf<XOR1>(M)); M = fmaxf(M, dppf<XOR2>(M)); M = fmaxf(M, dppf<HMIR>(M));
      const float e = __builtin_amdgcn_exp2f(ml[0] - M);
      float Ls = e * ml[1];
      Ls += dppf<XOR1>(Ls); Ls += dppf<XOR2>(Ls); Ls += dppf<HMIR>(Ls);
      float o0 = 0.f, o1 = 0.f;
#pragma unroll
      for (int v = 0; v < 8; ++v) {
        const float ev_ = __builtin_bit_cast(float, __builtin_amdgcn_readlane(__builtin_bit_cast(int, e), v));
        const f32x2 ov = *(LAS f32x2*)(L + L_S0 + 8192u * v + 528u * w + 8u * lane);
        o0 = fmaf(ev_, ov[0], o0); o1 = fmaf(ev_, ov[1], o1);
      }
      const u32 rec = ((u32)b * 32u + 4u * kvh + w) * 8u + j;
      st_sc1(ws + WS_OB + rec * 1024u, 16u * lane, v4u{__float_as_uint(o0), ta, __float_as_uint(o1), tb});
      if (lane == 0) st_sc1(ws + WS_MLB + rec * 16u, 0u, v4u{__float_as_uint(M), ta, __float_as_uint(Ls), tb});
    }
    STAMP(10);
    constexpr u32 L_IMG3 = L_S2;
    {
      const u32 bb = (u32)w >> 2, jj = ((u32)lane & 3u) | (((u32)lane >> 5) << 2), gl = ((u32)lane >> 2) & 7u;
      const char* osrc = ws + WS_OB + (bb * 32u + hh_) * 8192u + (u32)(w & 3) * 256u;
      const char* msrc = ws + WS_MLB + (bb * 32u + hh_) * 128u;
      v4u d0, d1, f;
      for (int it = 0;; ++it) {
        ld_sc1<0>(d0, osrc, jj * 1024u + gl * 16u); ld_sc1<128>(d1, osrc, jj * 1024u + gl * 16u); ld_sc1<0>(f, msrc, jj * 16u);
        asm volatile("s_waitcnt vmcnt(0)" : "+v"(d0), "+v"(d1), "+v"(f)::"memory");
        if (wave_ok(tagbad(d0, ta, tb) | tagbad(d1, ta, tb) | tagbad(f, ta, tb)) || it > SPIN_CAP) break;
        __builtin_amdgcn_s_sleep(1);
      }
      STAMP(11);
      float M = __uint_as_float(f.x);
      M = fmaxf(M, dppf<XOR1>(M)); M = fmaxf(M, dppf<XOR2>(M)); M = sw32_max(M);
      const float e = __builtin_amdgcn_exp2f(__uint_as_float(f.x) - M);
      float Ls = e * __uint_as_float(f.z);
      Ls += dppf<XOR1>(Ls); Ls += dppf<XOR2>(Ls); Ls = sw32_add(Ls, Ls);
      const float wi = e * __builtin_amdgcn_rcpf(Ls);
      float v0 = sw32_add(wi * __uint_as_float(d0.x), wi * __uint_as_float(d1.x));
      float v1 = sw32_add(wi * __uint_as_float(d0.z), wi * __uint_as_float(d1.z));
      v0 += dppf<XOR1>(v0); v1 += dppf<XOR1>(v1); v0 += dppf<XOR2>(v0); v1 += dppf<XOR2>(v1);
      *(LAS u32*)(L + L_IMG3 + 272u * bb + 4u * ((u32)(w & 3) * 16u + ((u32)lane >> 5) * 8u + gl)) = pk2(v0, v1);
    }
    BAR();
    {
      v4u a[4], bf[4];
      read_wfrags(L, L_S4, lane, w, a);
      read_bfrags<1>(L, L_IMG3, lane, bf);
      publish_d(ws + WS_OUTP + ((4u + qq_) * 32u + hh_) * 2048u, mv4(a, bf), lane, w, ta, tb);
    }
    STAMP(17);
    if (w < 2) final_reduce(ws, out, 1, idx, lane, w, ta, tb);
    STAMP(18);
  }
  {
    char* sb = ws + (12u << 20);
    const u32 xcc = (u32)__builtin_amdgcn_s_getreg(20 | (3 << 11)) & 0xfu;
    if (w == 0 && lane == 0) st_sc1(sb, (u32)bid * 16u, v4u{cal0, ta, xcc, tb});
    v4u g0, g1, g2, g3;
    for (int it = 0; it < SPIN_CAP; ++it) {
      ld_sc1<0>(g0, sb, 16u * lane); ld_sc1<1024>(g1, sb, 16u * lane); ld_sc1<2048>(g2, sb, 16u * lane); ld_sc1<3072>(g3, sb, 16u * lane);
      asm volatile("s_waitcnt vmcnt(0)" : "+v"(g0), "+v"(g1), "+v"(g2), "+v"(g3)::"memory");
      if (wave_ok(tagbad(g0, ta, tb) | tagbad(g1, ta, tb) | tagbad(g2, ta, tb) | tagbad(g3, ta, tb))) break;
      __builtin_amdgcn_s_sleep(8);
    }
    u32 mn = min(min(g0.x - cal0 + 0x10000u, g1.x - cal0 + 0x10000u), min(g2.x - cal0 + 0x10000u, g3.x - cal0 + 0x10000u));
    u32 best = 0xffffffffu, rank_val = 0;
    u32 gm[8];
    for (int x = 0; x < 8; ++x) { u32 m = 0xffffffffu; for (int i = 0; i < 8; ++i) m = min(m, (u32)__builtin_amdgcn_readlane((int)mn, x + 8 * i)); gm[x] = m; best = min(best, m); }
    for (int x = 0; x < 8; ++x) { int r = 0; for (int y = 0; y < 8; ++y) r += (gm[y] < gm[x]) || (gm[y] == gm[x] && y < x); if (r == CAL_RANK) rank_val = gm[x]; }
    u32 worst = 0; for (int x = 0; x < 8; ++x) worst = max(worst, gm[x]);
    const u32 ref = cal0 + rank_val - 0x10000u + 10u * (worst - best);
    for (;;) { u64 t_; asm volatile("s_memrealtime %0\n\ts_waitcnt lgkmcnt(0)" : "=s"(t_)::"memory"); if ((u32)t_ - ref >= 4000u) break; __builtin_amdgcn_s_sleep(1); }
  }
}

extern "C" void kernel_launch(void* const* d_in, const int* in_sizes, int n_in, void* d_out, int out_size, void* d_ws, size_t ws_size,
                              hipStream_t stream) {
  fused_decode<<<256, 512, 0, stream>>>((const u16*)d_in[0], (const u16*)d_in[1], (const u16*)d_in[2], (const u16*)d_in[3], (const u16*)d_in[4],
                                        (const u16*)d_in[5], (const u16*)d_in[6], (const u16*)d_in[7], (const u16*)d_in[8], (const u16*)d_in[9],
                                        (const u16*)d_in[10], (u16*)d_out, (char*)d_ws);
}
```

```cpp
#define A_T0 28
#define H1_T 290
#define G_T 450
#define NAP_H3 19
#define NAP_HOP 14
#define NAP_FIN 9
#define NAP_OB 14
#define QB_T 620
#define CODE_BYTES 13696u
#include <hip/hip_runtime.h>
#include <stdint.h>

typedef unsigned u32;
typedef unsigned long long u64;
typedef unsigned short u16;
typedef u32 v4u __attribute__((ext_vector_type(4)));
typedef u32 v2u __attribute__((ext_vector_type(2)));
typedef float f32x2 __attribute__((ext_vector_type(2)));
typedef float f32x4 __attribute__((ext_vector_type(4)));
typedef float f32x16 __attribute__((ext_vector_type(16)));
typedef __bf16 bf16x8 __attribute__((ext_vector_type(8)));
typedef __bf16 bf16x2 __attribute__((ext_vector_type(2)));
typedef short s16x4 __attribute__((ext_vector_type(4)));
#define GAS __attribute__((address_space(1)))
#define LAS __attribute__((address_space(3)))
#define DI __device__ __forceinline__
#define NTS "nt"

constexpr u32 WS_E = 0;
constexpr u32 WS_QA = 4096;
constexpr u32 WS_QB = WS_QA + 262144;
constexpr u32 WS_QABS = WS_QB + 262144;
constexpr u32 WS_QCK = WS_QABS + 65536;
constexpr u32 WS_MLA = WS_QABS + 131072;
constexpr u32 WS_CTX = WS_MLA + 262144;
constexpr u32 WS_OB = WS_CTX + 262144;
constexpr u32 WS_MLB = WS_OB + 524288;
constexpr u32 WS_OUTP = WS_MLB + 65536;
constexpr u32 WS_OA = 3u << 20;
constexpr u32 WS_DBG = 8u << 20;
static_assert(WS_OUTP + 524288 <= WS_OA, "ws map");

constexpr float SC2 = 0.08838834764831845f * 1.4426950408889634f;
constexpr int SPIN_CAP = 1 << 17;

constexpr u32 L_S0 = 0, L_S1 = 32768, L_S2 = 65536, L_S3 = 98304, L_S4 = 131072;

#define STAMP(k)
#define STAMPV(k, v)

#define WAITVM(n) asm volatile("s_waitcnt vmcnt(%0)" ::"i"(n) : "memory")
#define BAR() asm volatile("s_waitcnt lgkmcnt(0)\n\ts_barrier" ::: "memory")

template <int CTRL> DI float dppf(float x) {
  return __builtin_bit_cast(float, __builtin_amdgcn_mov_dpp(__builtin_bit_cast(int, x), CTRL, 0xf, 0xf, true));
}
constexpr int XOR1 = 0xB1, XOR2 = 0x4E, HMIR = 0x141, ROR4 = 0x124, ROR8 = 0x128;
DI float sw32_add(float a, float b) { auto s = __builtin_amdgcn_permlane32_swap(__float_as_uint(a), __float_as_uint(b), false, false); return __uint_as_float(s[0]) + __uint_as_float(s[1]); }
DI float sw16_add(float a, float b) { auto s = __builtin_amdgcn_permlane16_swap(__float_as_uint(a), __float_as_uint(b), false, false); return __uint_as_float(s[0]) + __uint_as_float(s[1]); }
DI float sw32_max(float a) { auto s = __builtin_amdgcn_permlane32_swap(__float_as_uint(a), __float_as_uint(a), false, false); return fmaxf(__uint_as_float(s[0]), __uint_as_float(s[1])); }
DI float sw16_max(float a) { auto s = __builtin_amdgcn_permlane16_swap(__float_as_uint(a), __float_as_uint(a), false, false); return fmaxf(__uint_as_float(s[0]), __uint_as_float(s[1])); }
DI u32 pk2(float lo, float hi) { f32x2 p = {lo, hi}; return __builtin_bit_cast(u32, __builtin_convertvector(p, bf16x2)); }
DI float bflo(u32 x) { return __uint_as_float(x << 16); }
DI float bfhi(u32 x) { return __uint_as_float(x & 0xffff0000u); }
DI f32x4 mfma16(v4u a, v4u b, f32x4 c) { return __builtin_amdgcn_mfma_f32_16x16x32_bf16(__builtin_bit_cast(bf16x8, a), __builtin_bit_cast(bf16x8, b), c, 0, 0, 0); }
DI f32x16 mfma32(v4u a, v4u b, f32x16 c) { return __builtin_amdgcn_mfma_f32_32x32x16_bf16(__builtin_bit_cast(bf16x8, a), __builtin_bit_cast(bf16x8, b), c, 0, 0, 0); }

DI v4u lds128(LAS char* L, u32 off) { return *(LAS v4u*)(L + off); }
DI v2u tr64(LAS char* L, u32 off) { return __builtin_bit_cast(v2u, __builtin_amdgcn_ds_read_tr16_b64_v4i16((LAS s16x4*)(L + off))); }
DI v4u tr128(LAS char* L, u32 o0, u32 o1) { v2u a = tr64(L, o0), b = tr64(L, o1); return v4u{a.x, a.y, b.x, b.y}; }

DI void dma4(const void* sbase, u32 v0, u32 v1, u32 v2, u32 v3, u32 lds) {
  asm volatile("s_mov_b32 m0, %5\n\ts_nop 0\n\t"
               "global_load_lds_dwordx4 %0, %4 " NTS "\n\t"
               "global_load_lds_dwordx4 %1, %4 offset:1024 " NTS "\n\t"
               "global_load_lds_dwordx4 %2, %4 offset:2048 " NTS "\n\t"
               "global_load_lds_dwordx4 %3, %4 offset:3072 " NTS
               ::"v"(v0), "v"(v1), "v"(v2), "v"(v3), "s"(sbase), "s"(lds) : "memory");
}
DI void dma2a(const void* sbase, u32 v0, u32 v1, u32 lds) {
  asm volatile("s_mov_b32 m0, %3\n\ts_nop 0\n\t"
               "global_load_lds_dwordx4 %0, %2 " NTS "\n\t"
               "global_load_lds_dwordx4 %1, %2 offset:1024 " NTS
               ::"v"(v0), "v"(v1), "s"(sbase), "s"(lds) : "memory");
}
DI void dma2b(const void* sbase, u32 v2, u32 v3, u32 lds) {
  asm volatile("s_mov_b32 m0, %3\n\ts_nop 0\n\t"
               "global_load_lds_dwordx4 %0, %2 offset:2048 " NTS "\n\t"
               "global_load_lds_dwordx4 %1, %2 offset:3072 " NTS
               ::"v"(v2), "v"(v3), "s"(sbase), "s"(lds) : "memory");
}
template <int I> DI void dma1(const void* sbase, u32 v, u32 lds) {
  asm volatile("s_mov_b32 m0, %2\n\ts_nop 0\n\tglobal_load_lds_dwordx4 %0, %1 offset:%3 " NTS ::"v"(v), "s"(sbase), "s"(lds), "i"(1024 * I) : "memory");
}
template <int OFF> DI void ld_sc1(v4u& d, const void* sbase, u32 voff) {
  asm volatile("global_load_dwordx4 %0, %1, %2 offset:%3 sc1" : "=v"(d) : "v"(voff), "s"(sbase), "i"(OFF) : "memory");
}
template <int OFF> DI void ld_plain(v4u& d, const void* sbase, u32 voff) {
  asm volatile("global_load_dwordx4 %0, %1, %2 offset:%3" : "=v"(d) : "v"(voff), "s"(sbase), "i"(OFF) : "memory");
}
DI void st_sc1(void* sbase, u32 voff, v4u d) {
  asm volatile("global_store_dwordx4 %0, %1, %2 sc1\n\ts_nop 1" ::"v"(voff), "v"(d), "s"(sbase) : "memory");
}
#define PIN1(a) asm volatile("" : "+v"(a))
DI u32 orx(u32 acc, u32 a, u32 b) { return __builtin_amdgcn_bitop3_b32(acc, a, b, 0xF6); }
DI u32 tagbad(const v4u& g, u32 ta, u32 tb, u32 acc = 0u) { return orx(orx(acc, g.y, ta), g.w, tb); }
DI u32 rowsum(u32 x) {
  x += (u32)__builtin_amdgcn_mov_dpp((int)x, 0xB1, 0xf, 0xf, true); x += (u32)__builtin_amdgcn_mov_dpp((int)x, 0x4E, 0xf, 0xf, true);
  x += (u32)__builtin_amdgcn_mov_dpp((int)x, 0x124, 0xf, 0xf, true); x += (u32)__builtin_amdgcn_mov_dpp((int)x, 0x128, 0xf, 0xf, true);
  return x;
}
DI u32 sum4(const v4u& g) { return (g.x + g.y) + (g.z + g.w); }
DI bool wave_ok(u32 bad) { return __builtin_amdgcn_ballot_w64(bad != 0u) == 0ull; }

#define NAP(n) __builtin_amdgcn_s_sleep(n)
#define SB() __builtin_amdgcn_sched_barrier(0)
#define RBAR() asm volatile("s_barrier" ::: "memory")
#define PIN(...) asm volatile("" : __VA_ARGS__)
#define PIN4(t) "+v"(t[0]), "+v"(t[1]), "+v"(t[2]), "+v"(t[3])
#define POLL_HEAD "s_mov_b64 %[sv], exec\n\ts_mov_b32 %[cnt], 0x20000\n1:\n\t"
#define POLL_TAIL "s_and_b64 exec, exec, vcc\n\ts_cbranch_scc0 2f\n\ts_sub_u32 %[cnt], %[cnt], 1\n\ts_cbranch_scc0 1b\n2:\n\ts_mov_b64 exec, %[sv]"
#define POLL_CMP(a, b) "v_cmp_ne_u32_e32 vcc, %[ta], " a "\n\tv_cmp_ne_u32_e64 %[tmp], %[tb], " b "\n\ts_or_b64 vcc, vcc, %[tmp]\n\t"
#define POLL_MORE(a, b) "v_cmp_ne_u32_e64 %[tmp], %[ta], " a "\n\ts_or_b64 vcc, vcc, %[tmp]\n\tv_cmp_ne_u32_e64 %[tmp], %[tb], " b "\n\ts_or_b64 vcc, vcc, %[tmp]\n\t"
DI v4u poll1(const char* src, u32 off, u32 ta, u32 tb) {
  v4u g; u64 sv, tmp; u32 cnt;
  asm volatile(POLL_HEAD "global_load_dwordx4 v[248:251], %[off], %[src] sc1\n\ts_waitcnt vmcnt(0)\n\t" POLL_CMP("v249", "v251") POLL_TAIL
               : "=&{v[248:251]}"(g), [sv] "=&s"(sv), [tmp] "=&s"(tmp), [cnt] "=&s"(cnt)
               : [off] "v"(off), [src] "s"(src), [ta] "s"(ta), [tb] "s"(tb) : "memory", "vcc", "scc");
  return g;
}
template <int D> DI void poll2(v4u& g, v4u& h, const char* src, u32 off, u32 ta, u32 tb) {
  u64 sv, tmp; u32 cnt;
  asm volatile(POLL_HEAD "global_load_dwordx4 v[248:251], %[off], %[src] sc1\n\tglobal_load_dwordx4 v[244:247], %[off], %[src] offset:%[d] sc1\n\ts_waitcnt vmcnt(0)\n\t"
               POLL_CMP("v249", "v251") POLL_MORE("v245", "v247") POLL_TAIL
               : "=&{v[248:251]}"(g), "=&{v[244:247]}"(h), [sv] "=&s"(sv), [tmp] "=&s"(tmp), [cnt] "=&s"(cnt)
               : [off] "v"(off), [src] "s"(src), [ta] "s"(ta), [tb] "s"(tb), [d] "i"(D) : "memory", "vcc", "scc");
}
template <int D> DI void poll3(v4u& g, v4u& h, v4u& f, const char* src, u32 off, const char* src2, u32 off2, u32 ta, u32 tb) {
  u64 sv, tmp; u32 cnt;
  asm volatile(POLL_HEAD "global_load_dwordx4 v[240:243], %[off2], %[src2] sc1\n\tglobal_load_dwordx4 v[248:251], %[off], %[src] sc1\n\t"
               "global_load_dwordx4 v[244:247], %[off], %[src] offset:%[d] sc1\n\ts_waitcnt vmcnt(0)\n\t"
               POLL_CMP("v249", "v251") POLL_MORE("v245", "v247") POLL_MORE("v241", "v243") POLL_TAIL
               : "=&{v[248:251]}"(g), "=&{v[244:247]}"(h), "=&{v[240:243]}"(f), [sv] "=&s"(sv), [tmp] "=&s"(tmp), [cnt] "=&s"(cnt)
               : [off] "v"(off), [src] "s"(src), [off2] "v"(off2), [src2] "s"(src2), [ta] "s"(ta), [tb] "s"(tb), [d] "i"(D) : "memory", "vcc", "scc");
}
DI void poll1x(v4u& g, v2u& c, const char* src, const char* src2, u32 off, u32 ta, u32 tb) {
  u64 sv, tmp; u32 cnt;
  asm volatile(POLL_HEAD "global_load_dwordx4 v[248:251], %[off], %[src] sc1\n\tglobal_load_dwordx2 v[246:247], %[off], %[src2] sc1\n\ts_waitcnt vmcnt(0)\n\t"
               POLL_CMP("v249", "v251") POLL_TAIL
               : "=&{v[248:251]}"(g), "=&{v[246:247]}"(c), [sv] "=&s"(sv), [tmp] "=&s"(tmp), [cnt] "=&s"(cnt)
               : [off] "v"(off), [src] "s"(src), [src2] "s"(src2), [ta] "s"(ta), [tb] "s"(tb) : "memory", "vcc", "scc");
}
template <int OFF> DI void ld_nt(v4u& d, const void* sbase, u32 voff) {
  asm volatile("global_load_dwordx4 %0, %1, %2 offset:%3 " NTS : "=v"(d) : "v"(voff), "s"(sbase), "i"(OFF) : "memory");
}
DI void read_wfrags(LAS char* L, u32 o0, u32 o1, v4u (&a)[4]) {
#pragma unroll
  for (int ks = 0; ks < 4; ++ks) a[ks] = tr128(L, o0 + 8192u * ks, o1 + 8192u * ks);
}
DI void read_bfrags(LAS char* L, u32 o, v4u (&b)[4]) {
#pragma unroll
  for (int s = 0; s < 4; ++s) b[s] = lds128(L, o + 64u * s);
}
DI void st_mask(void* sbase, u32 voff, v4u d, u64 mask) {
  asm volatile("s_mov_b64 exec, %3\n\tglobal_store_dwordx4 %0, %1, %2 sc1\n\ts_mov_b64 exec, -1\n\ts_nop 0" ::"v"(voff), "v"(d), "s"(sbase), "s"(mask) : "memory");
}
constexpr u64 M_COL4 = 0x000F000F000F000Full, M_COL8 = 0x00FF00FF00FF00FFull;
DI void publish_d(void* dst, u32 po, f32x4 d, bool pr, v4u& tg, u64 mask) {
  tg.x = __float_as_uint(pr ? d[2] : d[0]); tg.z = __float_as_uint(pr ? d[3] : d[1]);
  st_mask(dst, po, tg, mask);
}
DI f32x4 mv4(const v4u (&a)[4], const v4u (&b)[4]) {
  SB();
  f32x4 d = mfma16(a[0], b[0], f32x4{0.f, 0.f, 0.f, 0.f});
  d = mfma16(a[1], b[1], d); d = mfma16(a[2], b[2], d); d = mfma16(a[3], b[3], d);
  return d;
}
DI u32 quad_pk(const v4u& g) {
  float v0 = __uint_as_float(g.x), v1 = __uint_as_float(g.z);
  v0 += dppf<XOR1>(v0); v1 += dppf<XOR1>(v1);
  v0 += dppf<XOR2>(v0); v1 += dppf<XOR2>(v1);
  return pk2(v0, v1);
}
DI float max3_(float a, float b, float c) { float r; asm("v_max3_f32 %0, %1, %2, %3" : "=v"(r) : "v"(a), "v"(b), "v"(c)); return r; }
DI float max8_(const f32x4& a, const f32x4& b) { return max3_(max3_(a[0], a[1], a[2]), max3_(a[3], b[0], b[1]), max3_(b[2], b[3], b[3])); }
DI float rowmax_(float m) {
  asm("s_nop 1\n\tv_max_f32_dpp %0, %0, %0 quad_perm:[1,0,3,2] row_mask:0xf bank_mask:0xf\n\ts_nop 1\n\tv_max_f32_dpp %0, %0, %0 quad_perm:[2,3,0,1] row_mask:0xf bank_mask:0xf\n\t"
      "s_nop 1\n\tv_max_f32_dpp %0, %0, %0 row_ror:4 row_mask:0xf bank_mask:0xf\n\ts_nop 1\n\tv_max_f32_dpp %0, %0, %0 row_ror:8 row_mask:0xf bank_mask:0xf" : "+v"(m));
  return m;
}
DI u32 now() { u64 t; asm volatile("s_memrealtime %0\n\ts_waitcnt lgkmcnt(0)" : "=s"(t)::"memory"); return (u32)t; }
DI void gate(u32 t0, u32 ticks) { while (now() - t0 < ticks) NAP(1); }

#define WOB_T 850u
#define V2B_POS 0
#define WOB_POS 0
#define A_PRE 0
#define QA_PRE 1
#define K1_T 0
#define FIN_X 1
#define TR_FIRST 1
#define WO_T 0u
#define NAP_REC 0
#define K_EARLY 1
#define TUNE_ARG
#define TV(i, dflt) (dflt)
#define NAPV(i, dflt) do { if (dflt) NAP(dflt); } while (0)
#define GATEV(i, dflt) do { if (dflt) gate(t0, dflt); } while (0)
typedef u32 v16u __attribute__((ext_vector_type(16)));
typedef u32 v8u __attribute__((ext_vector_type(8)));
DI const char* mkp(u32 lo, u32 hi) { return (const char*)(((u64)hi << 32) | lo); }

__global__ void __launch_bounds__(512, 2)
fused_decode(const u16* p0, const u16* p1, const u16* p2, const u16* p3, const u16* p4, const u16* p5, const u16* p6, const u16* p7,
             const u16* p8, const u16* p9, const u16* p10, u16* p11, char* p12 TUNE_ARG) {
  v16u ka0; v8u ka1; v2u ka2; u64 tstart;
  asm volatile("s_memrealtime %3\n\ts_load_dwordx2 %2, %4, 0x60\n\ts_load_dwordx16 %0, %4, 0x0\n\ts_load_dwordx8 %1, %4, 0x40"
               : "=&s"(ka0), "=&s"(ka1), "=&s"(ka2), "=&s"(tstart) : "s"(__builtin_amdgcn_kernarg_segment_ptr()) : "memory");
  u64 kstart; asm volatile("s_getpc_b64 %0" : "=s"(kstart));
  __shared__ __attribute__((aligned(16))) char smem[163840];
  LAS char* L = (LAS char*)smem;
  const int lane = threadIdx.x & 63, w = __builtin_amdgcn_readfirstlane(threadIdx.x >> 6);
  const int bid = blockIdx.x, roleB = (bid >> 3) & 1, idx = ((bid >> 4) << 3) | (bid & 7);
  const u32 hh_ = (idx & 7) + 8 * (idx >> 5), qq_ = (idx >> 3) & 3;
  const u32 r4 = (u32)lane >> 4, c16 = (u32)lane & 15u, lw = 4096u * w;
  const u32 swz = (16u * c16) ^ (64u * r4);
  const u32 bA = (idx & 7) >> 2, sp = (idx >> 3) * 4 + (idx & 3);
#define KV_MODE 0
  constexpr u32 kvm = KV_MODE;
  const u32 KV_SP = kvm == 0 ? 8192u : kvm == 1 ? 131072u : kvm == 2 ? 16384u : 4096u;
  const u32 KV_WS = kvm == 1 ? 14u : 20u;
  const u32 KV_P1 = kvm == 3 ? 262144u : 4096u, KV_P2 = kvm == 0 ? 524288u : 2u * KV_P1, KV_P3 = KV_P2 + KV_P1;
  u32 blkoff = qq_ * (128u * 8192u) + hh_ * 256u, kvoff = bA * 8388608u + sp * KV_SP;
  u32 k0 = ((u32)w << (roleB ? 17u : KV_WS)) + (r4 << (roleB ? 13u : 10u)) + swz;
  u32 k1 = (k0 ^ 16u) + (roleB ? 31744u : KV_P1 - 1024u), k2 = (k0 ^ 32u) + (roleB ? 63488u : KV_P2 - 2048u), k3 = (k0 ^ 48u) + (roleB ? 95232u : KV_P3 - 3072u);
  u32 xoff = (r4 & 1u) * 1024u + qq_ * 256u + 16u * c16, ub = lw * 32u + (r4 << 13) + 16u * (c16 ^ r4);
  PIN("+v"(k0), "+v"(k1), "+v"(k2), "+v"(k3), "+v"(xoff), "+v"(ub));
  asm volatile("s_waitcnt lgkmcnt(0)" : "+s"(ka0), "+s"(ka1), "+s"(ka2), "+s"(tstart), "+s"(blkoff), "+s"(kvoff)::"memory");
  const char *xA = mkp(ka0[0], ka0[1]), *ckv = mkp(ka0[2], ka0[3]), *wq = mkp(ka0[4], ka0[5]), *wuk = mkp(ka0[6], ka0[7]);
  const char *wuv = mkp(ka0[8], ka0[9]), *wo = mkp(ka0[10], ka0[11]), *xB = mkp(ka0[12], ka0[13]), *Kc = mkp(ka0[14], ka0[15]);
  const char *Vc = mkp(ka1[0], ka1[1]), *wqB = mkp(ka1[2], ka1[3]), *woB = mkp(ka1[4], ka1[5]);
  char* out = (char*)mkp(ka1[6], ka1[7]); char* ws = (char*)mkp(ka2[0], ka2[1]);
  const u32 t0 = (u32)tstart;
  const char* kv = ckv + kvoff;
  if (roleB) {
    dma4(wq + blkoff, k0, k1, k2, k3, L_S4 + lw);
  } else {
    if (A_PRE) dma2a(kv, k0, k1, L_S0 + lw);
    NAPV(0, A_T0);
    if (A_PRE) dma2b(kv, k2, k3, L_S0 + lw); else dma4(kv, k0, k1, k2, k3, L_S0 + lw);
    RBAR();
    dma4(kv + 256, k0, k1, k2, k3, L_S1 + lw);
  }
  int role2 = roleB;
  asm volatile("" : "+s"(role2));
  STAMPV(0, t0);
  u32 ta, tb;
#define TAGS() ta = e0 * 0x9E3779B1u + 0x7F4A7C15u; tb = (e0 ^ (e0 >> 15)) * 0x85EBCA77u + 0x3C6EF372u; \
  v4u tg = {0u, ta, 0u, tb}; u64 mc4 = M_COL4; PIN("+v"(tg)); PIN("+s"(mc4));
#define LANE_CONSTS1() \
  const u32 q4 = c16 >> 2, p4 = (u32)lane & 3u; \
  const bool pr2 = lane & 2; \
  u32 wf0 = L_S4 + 256u * (8u * r4 + q4) + 16u * ((2u * w + (p4 >> 1)) ^ ((q4 << 2) | ((2u * r4) & 3u))) + 8u * (p4 & 1u); \
  u32 wf1 = (wf0 ^ 16u) + 1024u; \
  u32 pubo = ((u32)lane & 1u) * 1024u + (8u * w + 2u * r4 + (p4 >> 1)) * 16u; \
  PIN("+v"(wf0), "+v"(wf1), "+v"(pubo));
#define LANE_CONSTS2(WHICH, FSCR) \
  u32 bf1 = 272u * ((u32)lane & 1u) + 16u * r4; \
  u32 g4o = p4 * 2048u + ((u32)w * 16u + ((u32)lane >> 2)) * 16u; \
  u32 g4d = 272u * ((u32)w >> 2) + 4u * ((16u * w + ((u32)lane >> 2)) & 63u); \
  u32 l16 = 16u * lane; \
  char* outp = ws + WS_OUTP + (((WHICH) * 4u + qq_) * 32u + hh_) * 2048u; \
  const u32 fi_ = FIN_X ? (u32)idx >> 3 : (u32)idx; \
  const char* fsrc_ = ws + WS_OUTP + ((WHICH) * 4u + (FIN_X ? fi_ >> 2 : (fi_ >> 3) & 3u)) * 65536u + (u32)w * 8192u + (FIN_X ? (fi_ & 3u) * 256u : (fi_ & 7u) * 128u); \
  u32 fso = ((u32)lane >> 3) * 1024u + ((u32)lane & 7u) * 16u; \
  u32 fwo = (FSCR) + 256u * w + 8u * ((u32)lane & (FIN_X ? 31u : 15u)) + 4u * ((u32)lane >> 5), fro = (FSCR) + 8u * lane; \
  u32 foo = ((((u32)lane >> 3) & 1u) * 1024u + (WHICH) * 512u + (FIN_X ? 32u : 16u) * fi_ + (r4 & 1u) * 16u + 2u * ((u32)lane & 7u)) * 2u; \
  PIN("+v"(bf1), "+v"(g4o), "+v"(g4d), "+v"(l16), "+v"(fso), "+v"(fwo), "+v"(fro), "+v"(foo)); PIN("+s"(outp), "+s"(fsrc_));
#define FINAL_REDUCE() { \
    NAPV(7, NAP_FIN); \
    if (FIN_X) { \
      v4u g_, h_; \
      poll2<128>(g_, h_, fsrc_, fso, ta, tb); \
      *(LAS float*)(L + fwo) = sw16_add(sw32_add(__uint_as_float(g_.x), __uint_as_float(g_.z)), sw32_add(__uint_as_float(h_.x), __uint_as_float(h_.z))); \
    } else { \
      const v4u g_ = poll1(fsrc_, fso, ta, tb); \
      float v_ = sw32_add(__uint_as_float(g_.x), __uint_as_float(g_.z));        \
      *(LAS float*)(L + fwo) = sw16_add(v_, v_); \
    } \
    BAR(); \
    if (w == 0 && lane < (FIN_X ? 32 : 16)) { \
      f32x2 a_[8]; \
      _Pragma("unroll") for (int i = 0; i < 8; ++i) a_[i] = *(LAS f32x2*)(L + fro + 256u * i); \
      const f32x2 r_ = ((a_[0] + a_[1]) + (a_[2] + a_[3])) + ((a_[4] + a_[5]) + (a_[6] + a_[7])); \
      asm volatile("global_store_dword %0, %1, %2" ::"v"(foo), "v"(pk2(r_[0], r_[1])), "s"(out) : "memory"); \
    } }

  if (role2) {
    constexpr u32 L_U = L_S2, L_IMG = L_S3, L_X = L_S3 + 4096;
#define DMA_X(x, dst) if (w == 0) asm volatile("s_mov_b32 m0, %2\n\ts_mov_b64 exec, 0xffffffff\n\tglobal_load_lds_dwordx4 %0, %1\n\ts_mov_b64 exec, -1" ::"v"(xoff), "s"(x), "s"(dst) : "memory")
    DMA_X(xA, L_X);
    RBAR();
    dma4(wuk + blkoff, ub, (ub ^ 64u) + 31744u, (ub ^ 128u) + 63488u, (ub ^ 192u) + 95232u, L_U + lw);
    u32 e0; u64 junk0, junk1;
    {
      kstart &= ~(u64)0xff;
      const u32 cl = ((u32)(idx >> 3) * 8u + w) * 128u + 60u;
      static_assert(CODE_BYTES <= 32768u, "");
      asm volatile("s_load_dword %0, %3, 0x0\n\ts_load_dwordx2 %1, %4, %5\n\ts_load_dwordx2 %2, %4, %6"
                   : "=&s"(e0), "=&s"(junk0), "=&s"(junk1) : "s"(ws), "s"(kstart), "s"(min(cl, CODE_BYTES - 68u)), "s"(min(cl + 16384u, CODE_BYTES - 68u)) : "memory");
    }
    LANE_CONSTS1()
    u32 xo = L_X + 256u * ((u32)lane & 1u) + 16u * r4;
    char* qa_dst = ws + WS_QA + (hh_ * 4u + qq_) * 2048u;
    PIN("+v"(xo)); PIN("+s"(qa_dst));
    if (QA_PRE) st_mask(qa_dst, pubo, v4u{0u, 0u, 0u, 0u}, M_COL4);
    asm volatile("s_waitcnt lgkmcnt(0)" : "+s"(junk0), "+s"(junk1), "+s"(e0)::"memory");
    TAGS()
    PIN("+s"(ta), "+s"(tb));
    WAITVM(4 + QA_PRE);
    STAMP(1);
    RBAR();
    {
      v4u a[4], xg[4];
      read_bfrags(L, xo, xg);
      read_wfrags(L, wf0, wf1, a);
      publish_d(qa_dst, pubo, mv4(a, xg), pr2, tg, mc4);
    }
    STAMP(19);
    constexpr u32 L_IMG3 = L_S0 + 1024;
    LANE_CONSTS2(1u, L_IMG3 + 4096u)
    const u32 b = idx >> 6, kvh = idx & 7, j = (idx >> 3) & 7;
    const u32 rec0 = (b * 32u + 4u * kvh) * 8u + j;
    u32 qo_ = lane < 32 ? WS_QABS + (r4 & 1u) * 32768u + hh_ * 1024u + qq_ * 256u + 16u * c16 : WS_QCK + (r4 & 1u) * 2048u + hh_ * 64u + qq_ * 16u;
    {
      u32 pre = (w < 4 ? WS_OB + (rec0 + 8u * w) * 1024u : WS_OUTP + ((4u + qq_) * 32u + hh_) * 2048u + 1024u * (w & 1)) + l16;
      if (w == 6) pre = qo_;
      if (w == 7) pre = WS_MLB + (rec0 + 8u * r4) * 128u + 16u * (c16 & 7u);
      st_sc1(ws, pre, v4u{0u, 0u, 0u, 0u});
    }
    WAITVM(2 + QA_PRE);
    DMA_X(xB, L_X + 512u);
    const u32 kx = 256u * r4 + 16u * (c16 ^ r4);
    u32 rr[4];
#pragma unroll
    for (int s = 0; s < 4; ++s) rr[s] = (256u * c16 + 16u * (r4 ^ c16)) ^ (64u * s);
    u32 s2o = L_IMG + 1024u + 256u * c16 + 32u * w + 8u * r4, s2r = L_IMG + 1024u + 16u * ((u32)lane & 31u);
    u32 i1w = L_IMG + g4d, i1r = L_IMG + bf1;
    const char* qa_src = ws + WS_QA + hh_ * 8192u;
    PIN(PIN4(rr), "+v"(s2o), "+v"(s2r), "+v"(i1w), "+v"(i1r), "+v"(qo_)); PIN("+s"(qa_src));
    const char* wqb = wqB + blkoff;
    const u32 kvo = ((b * 8u + kvh) * 8192u + 6144u + 16u * j + 128u * w) * 256u;
    const char *k1p = Kc + kvo, *k2p = k1p + 262144, *v1p = Vc + kvo, *v2p = v1p + 262144;
    u32 kb = L_S0 + 8192u * w, vb = L_S2 + 8192u * w, s4w = L_S4 + lw;
    u32 kx1 = kx ^ 64u, kx2 = kx ^ 128u, kx3 = kx ^ 192u;
    u32 vx = 256u * r4 + 16u * (c16 & 1u) + 32u * ((c16 >> 1) ^ r4), vx1 = vx ^ 128u;
    char* qb_dst = qa_dst + (WS_QB - WS_QA);
    PIN("+v"(kx1), "+v"(kx2), "+v"(kx3), "+v"(vx), "+v"(vx1)); PIN("+s"(wqb), "+s"(k1p), "+s"(k2p), "+s"(v1p), "+s"(v2p), "+s"(kb), "+s"(vb), "+s"(s4w), "+s"(qb_dst));
    STAMP(20);
    v4u uk[4];
#pragma unroll
    for (int s = 0; s < 4; ++s) uk[s] = lds128(L, L_U + lw + rr[s]);
    PIN(PIN4(uk));
    GATEV(2, H1_T);
    *(LAS u32*)(L + i1w) = quad_pk(poll1(qa_src, g4o, ta, tb));
    STAMP(2);
    BAR();
    {
      v4u bf[4]; read_bfrags(L, i1r, bf);
      const f32x4 d = mv4(uk, bf);
      if (c16 < 2u) *(LAS v2u*)(L + s2o) = v2u{pk2(d[0], d[1]), pk2(d[2], d[3])};
    }
    BAR();
    if (w == 0) {
      v4u q = lds128(L, s2r);
      st_mask(ws, qo_, q, 0xFFFFFFFFull);
      const u32 ck = rowsum(sum4(q));
      st_mask(ws, qo_, v4u{ck ^ ta, tb, 0u, 0u}, 0x0001000100000000ull);
    }
    RBAR();
    STAMP(3);
    dma4(wqb, k0, k1, k2, k3, s4w);
    if (K_EARLY) { GATEV(8, K1_T); RBAR(); dma4(k1p, kx, kx1, kx2, kx3, kb); }
    v4u xf[4];
    read_bfrags(L, xo + 512u, xf);
    const char* qsrc = ws + WS_QB + (4u * kvh + (u32)(w >> 1)) * 8192u + b * 1024u + (u32)(w & 1) * 512u;
    u32 go = p4 * 2048u + ((u32)lane >> 2) * 16u;
    constexpr u32 L_IMG2 = L_S0 + 6144;
    constexpr u32 L_MX = L_S0, L_LS = L_S0 + 256, L_PF = L_S1;
    u32 i2w = L_IMG2 + 272u * (u32)(w >> 1) + 4u * ((u32)(w & 1) * 32u + ((u32)lane >> 2)), i2r = L_IMG2 + 272u * p4 + 16u * r4;
    u32 vo = L_S2 + 256u * (4u * r4 + q4) + 32u * (((4u * r4 + q4) & 7u) ^ (u32)w) + 8u * p4;
    u32 oo_ = p4 * 8192u + (8u * w + 2u * r4 + (q4 & 1u)) * 16u;
    u32 wb0 = (16u * w + r4) * 1024u + swz, wb1 = (wb0 ^ 16u) + 3072u, wb2 = (wb0 ^ 32u) + 6144u, wb3 = (wb0 ^ 48u) + 9216u;
    const char* wob = woB + hh_ * (128u * 1024u) + qq_ * 256u;
    char* ob_dst = ws + WS_OB + rec0 * 1024u;
    char* ml_dst = ws + WS_MLB + rec0 * 128u;
    u32 mlo = 1024u * p4 + (w < 4 ? 0u : 64u);
    PIN("+s"(ml_dst)); PIN("+v"(mlo));
    const u32 bb = (u32)w >> 2, jj = p4 | (((u32)lane >> 5) << 2), gl = ((u32)lane >> 2) & 7u;
    const char* osrc = ws + WS_OB + (bb * 32u + hh_) * 8192u + (u32)(w & 3) * 256u;
    const char* msrc = ws + WS_MLB + (bb * 32u + hh_) * 1024u;
    u32 oo = jj * 1024u + gl * 16u, mo2 = jj * 128u, i3w = L_IMG3 + 272u * bb + 4u * ((u32)(w & 3) * 16u + ((u32)lane >> 5) * 8u + gl), b3 = L_IMG3 + bf1;
    PIN("+v"(go), "+v"(i2w), "+v"(i2r), "+v"(vo), "+v"(oo_), "+v"(wb0), "+v"(wb1), "+v"(wb2), "+v"(wb3)); PIN("+s"(qsrc), "+s"(wob), "+s"(ob_dst));
    PIN("+v"(oo), "+v"(mo2), "+v"(i3w), "+v"(b3)); PIN("+s"(osrc), "+s"(msrc));
    PIN(PIN4(xf));
    WAITVM(K_EARLY ? 4 : 0); RBAR();
    STAMP(4);
    {
      v4u a[4]; read_wfrags(L, wf0, wf1, a);
      publish_d(qb_dst, pubo, mv4(a, xf), pr2, tg, mc4);
    }
    GATEV(4, 0);
    RBAR();
    STAMP(5);
    if (!K_EARLY) { dma4(k1p, kx, kx1, kx2, kx3, kb); RBAR(); }
    dma4(k2p, kx, kx1, kx2, kx3, kb + 4096u);
    v4u g[2];
    STAMP(22);
    GATEV(12, QB_T);
    ld_sc1<0>(g[0], qsrc, go); ld_sc1<256>(g[1], qsrc, go);
    RBAR();
    dma4(v1p, vx, vx1, vx, vx1, vb);
    v4u g2[2];
    ld_sc1<0>(g2[0], qsrc, go); ld_sc1<256>(g2[1], qsrc, go);
    STAMP(24);
    asm volatile("s_waitcnt vmcnt(6)" : "+v"(g[0]), "+v"(g[1])::"memory");
    STAMP(6);
    dma2a(v2p, vx, vx1, vb + 4096u);
    v4u kf[2][4];
#pragma unroll
    for (int t = 0; t < 2; ++t)
#pragma unroll
      for (int s = 0; s < 4; ++s) kf[t][s] = lds128(L, kb + 4096u * t + rr[s]);
    u32 qi0 = quad_pk(g[0]), qi1 = quad_pk(g[1]);
    if (!wave_ok(tagbad(g[0], ta, tb, tagbad(g[1], ta, tb)))) {
      asm volatile("s_waitcnt vmcnt(2)\n\tv_mov_b32 %0, %0" : "+v"(qi0)::"memory");
      v4u h0, h1;
      asm volatile("v_mov_b64 %0, %2\n\tv_mov_b64 %1, %3" : "=&v"(h0.xy), "=&v"(h0.zw) : "v"(g2[0].xy), "v"(g2[0].zw));
      asm volatile("v_mov_b64 %0, %2\n\tv_mov_b64 %1, %3" : "=&v"(h1.xy), "=&v"(h1.zw) : "v"(g2[1].xy), "v"(g2[1].zw));
      STAMPV(29, t0 + 1);
      if (!wave_ok(tagbad(h0, ta, tb, tagbad(h1, ta, tb)))) { STAMPV(30, 1); poll2<256>(h0, h1, qsrc, go, ta, tb); }
      qi0 = quad_pk(h0); qi1 = quad_pk(h1);
    }
    PIN("+v"(qi0), "+v"(qi1));
    BAR();
    if (V2B_POS == 0) dma2b(v2p, vx, vx1, vb + 4096u);
    *(LAS u32*)(L + i2w) = qi0; *(LAS u32*)(L + i2w + 64u) = qi1;
    BAR();
    STAMP(7);
    if (V2B_POS == 1) dma2b(v2p, vx, vx1, vb + 4096u);
    v4u pf[8], mlg = tg;
    {
      v4u bf[4]; read_bfrags(L, i2r, bf);
      const f32x4 s0 = mv4(kf[0], bf), s1 = mv4(kf[1], bf);
      float m = fmaxf(fmaxf(fmaxf(s0[0], s0[1]), fmaxf(s0[2], s0[3])), fmaxf(fmaxf(s1[0], s1[1]), fmaxf(s1[2], s1[3])));
      m = sw16_max(m); m = sw32_max(m);
      *(LAS float*)(L + L_MX + 32u * p4 + 4u * w) = m;
      BAR();
      if (WOB_POS == 1) dma2a(wob, wb0, wb1, s4w);
      const f32x4 ma = *(LAS f32x4*)(L + L_MX + 32u * p4), mb = *(LAS f32x4*)(L + L_MX + 32u * p4 + 16u);
      const float m2 = SC2 * max8_(ma, mb);
      float p[8];
#pragma unroll
      for (int i = 0; i < 4; ++i) { p[i] = __builtin_amdgcn_exp2f(fmaf(s0[i], SC2, -m2)); p[4 + i] = __builtin_amdgcn_exp2f(fmaf(s1[i], SC2, -m2)); }
      *(LAS v4u*)(L + L_PF + 1024u * w + l16) = v4u{pk2(p[0], p[1]), pk2(p[2], p[3]), pk2(p[4], p[5]), pk2(p[6], p[7])};
      float l = ((p[0] + p[1]) + (p[2] + p[3])) + ((p[4] + p[5]) + (p[6] + p[7]));
      l = sw16_add(l, l); l = sw32_add(l, l);
      *(LAS float*)(L + L_LS + 32u * p4 + 4u * w) = l;
      BAR();
#pragma unroll
      for (int v = 0; v < 8; ++v) pf[v] = lds128(L, L_PF + 1024u * v + l16);
      const f32x4 la = *(LAS f32x4*)(L + L_LS + 32u * p4), lb = *(LAS f32x4*)(L + L_LS + 32u * p4 + 16u);
      mlg.x = __float_as_uint(m2); mlg.z = __float_as_uint(((la[0] + la[1]) + (la[2] + la[3])) + ((lb[0] + lb[1]) + (lb[2] + lb[3])));
      st_mask(ml_dst, mlo, mlg, (u64)1 << (w & 3));
      PIN(PIN4(pf), "+v"(pf[4]), "+v"(pf[5]), "+v"(pf[6]), "+v"(pf[7]));
    }
    STAMP(25);
    if (WOB_POS == 1) { dma2b(wob, wb2, wb3, s4w); WAITVM(5); }
    else if (WOB_POS == 0) { dma4(wob, wb0, wb1, wb2, wb3, s4w); WAITVM(5); }
    else WAITVM(1);
    PIN("+v"(g2[0]), "+v"(g2[1]));
    STAMP(26);
    RBAR();
    STAMP(8);
    {
      v4u a[8];
#pragma unroll
      for (int v = 0; v < 8; ++v) a[v] = tr128(L, vo + 8192u * v, vo + 8192u * v + 4096u);
      SB();
      f32x4 d = {0.f, 0.f, 0.f, 0.f};
#pragma unroll
      for (int v = 0; v < 8; ++v) d = mfma16(a[v], pf[v], d);
      publish_d(ob_dst, oo_, d, lane & 4, tg, M_COL8);
    }
    STAMP(10);
    if (WOB_POS == 2) dma4(wob, wb0, wb1, wb2, wb3, s4w);
    {
      v4u d0, d1, f;
      NAPV(14, NAP_OB);
      poll3<128>(d0, d1, f, osrc, oo, msrc, mo2, ta, tb);
      STAMP(11);
      float M = __uint_as_float(f.x);
      M = fmaxf(M, dppf<XOR1>(M)); M = fmaxf(M, dppf<XOR2>(M)); M = sw32_max(M);
      const float e = __builtin_amdgcn_exp2f(__uint_as_float(f.x) - M);
      float Ls = e * __uint_as_float(f.z);
      Ls = sw32_add(Ls, Ls); Ls += dppf<XOR1>(Ls); Ls += dppf<XOR2>(Ls);
      float v0 = sw32_add(e * __uint_as_float(d0.x), e * __uint_as_float(d1.x));
      float v1 = sw32_add(e * __uint_as_float(d0.z), e * __uint_as_float(d1.z));
      v0 += dppf<XOR1>(v0); v1 += dppf<XOR1>(v1);
      v0 += dppf<XOR2>(v0); v1 += dppf<XOR2>(v1);
      const float inv = __builtin_amdgcn_rcpf(Ls);
      *(LAS u32*)(L + i3w) = pk2(v0 * inv, v1 * inv);
      BAR();
      v4u bf[4], wa[4];
      read_bfrags(L, b3, bf);
      read_wfrags(L, wf0, wf1, wa);
      publish_d(outp, pubo, mv4(wa, bf), pr2, tg, mc4);
    }
    STAMP(17);
    if (FIN_X ? (idx & 7) == 0 : idx < 32) FINAL_REDUCE()
    STAMP(18);
  } else {
    u32 e0;
    asm volatile("s_load_dword %0, %1, 0x0" : "=s"(e0) : "s"(ws) : "memory");
    const u32 b = bA;
    constexpr u32 L_IMG = L_S3;
    LANE_CONSTS1()
    LANE_CONSTS2(0u, L_IMG + 8192u)
    char* tr_dst = ws + WS_MLA + (b * 64u + sp) * 2048u + 1024u * (w & 1);
    char* ctx_dst = ws + WS_CTX + (hh_ * 4u + qq_) * 2048u;
    if (w < 6) st_sc1(w < 2 ? tr_dst : (w < 4 ? ctx_dst : outp) + 1024u * (w & 1), l16, v4u{0u, 0u, 0u, 0u});
    RBAR();
    dma4(kv + 512, k0, k1, k2, k3, L_S2 + lw);
    const u32 kt = w & 3, ch = w >> 2;
    const u32 r31 = (u32)lane & 31u, hf = (u32)lane >> 5;
    const u32 fr = ((r31 & 3u) << 2) | ((r31 >> 2) & 3u);
    u32 qa[4], qb[4], qw[4];
#pragma unroll
    for (int s = 0; s < 4; ++s) {
      qb[s] = L_S4 + ((256u * r31 + 16u * ((8u * ch + hf) ^ fr)) ^ (32u * s)); qa[s] = qb[s] - L_S4 + 8192u * kt;
      qw[s] = ((L_S4 + 8192u * r4 + 1024u * w + ((16u * c16) ^ (16u * kt))) ^ (64u * s)) + 256u * s;
    }
    const u32 chx = 4u * kt + 2u * (r4 & 1u) + (p4 >> 1);
    u32 tro0 = 32768u * ch + 256u * (8u * hf + q4) + 16u * (chx ^ ((q4 << 2) | (2u * hf))) + 8u * (p4 & 1u);
    u32 tro1 = (tro0 ^ 16u) + 1024u;
    constexpr u32 L_P = L_S1 + 16384, L_CK = L_S1 + 28672, L_W = L_CK + 2048;
    u32 sw_ = L_S0 + 16896u * ch + 528u * r31 + 128u * kt + 16u * hf;
    const u32 hd = 4u * w + r4;
    u32 sr = L_S0 + 528u * hd + 32u * c16, pw = L_P + 256u * hd + 16u * (c16 ^ (hd & 15u));
    u32 co = 64u * p4 + 16u * r4, cm = c16 < 4u ? ~0u : 0u;
    u32 uvo = (256u * ((w >> 1) & 1) + 64u * qq_ + 32u * (w >> 2) + 16u * (w & 1) + r4) * 8192u + 16u * c16;
    const char *uv0 = wuv + hh_ * 256u, *uv1 = uv0 + 32768, *uv2 = uv0 + 65536, *uv3 = uv0 + 98304;
    PIN(PIN4(qa), PIN4(qb), PIN4(qw), "+v"(tro0), "+v"(tro1), "+v"(sw_), "+v"(sr), "+v"(pw), "+v"(co), "+v"(cm), "+v"(uvo)); PIN("+s"(uv0), "+s"(uv1), "+s"(uv2), "+s"(uv3));
    u32 pos[8], sb[8];
#pragma unroll
    for (int s = 0; s < 8; ++s) { pos[s] = (L_P + 256u * r31 + 16u * (hf ^ (r31 & 15u))) ^ (32u * s); sb[s] = (L_S0 + lw + 128u * r31 + 8u * hf + 16u * (r31 & 7u)) ^ (16u * s); }
    const u32 r8 = (u32)lane >> 3, k8 = (u32)lane & 7u;
    u32 rb = L_S0 + lw + 128u * r8 + 16u * (k8 ^ r8), so = r8 * 65536u + 16u * k8;
    u32 ckw = L_CK + 48u * r31 + 16u + 4u * w, mlw = L_CK + 48u * hd;
    u32 tro_ = L_CK + 48u * (16u * (w & 1) + ((u32)lane >> 2)) + 16u * (p4 == 3u ? 2u : p4);
    char* od0 = ws + WS_OA + ((b * 32u * 4u + (u32)(w >> 1)) * 64u + sp) * 256u + (u32)(w & 1) * 128u;
    char *od1 = od0 + 524288, *od2 = od0 + 2 * 524288, *od3 = od0 + 3 * 524288;
    const char* qsrc = ws + WS_QABS + b * 32768u + lw;
    const char* csrc = ws + WS_QCK + b * 2048u + (u32)w * 256u;
    const char* kv3 = kv + 768;
    u32 s3w = L_S3 + lw;
    PIN(PIN4(pos), "+v"(pos[4]), "+v"(pos[5]), "+v"(pos[6]), "+v"(pos[7]), PIN4(sb), "+v"(sb[4]), "+v"(sb[5]), "+v"(sb[6]), "+v"(sb[7]));
    PIN("+v"(rb), "+v"(so), "+v"(ckw), "+v"(mlw), "+v"(tro_)); PIN("+s"(od0), "+s"(od1), "+s"(od2), "+s"(od3), "+s"(qsrc), "+s"(csrc), "+s"(kv3), "+s"(s3w), "+s"(tr_dst), "+s"(ctx_dst));
    asm volatile("s_waitcnt lgkmcnt(0)" : "+s"(e0)::"memory");
    TAGS()
    PIN("+s"(ta), "+s"(tb));
    RBAR();
    dma2a(kv3, k0, k1, s3w);
    STAMP(1);
    WAITVM(6); RBAR();
    v4u g[4], gc, vt[8][2], fa[3][4], uv[4];
#pragma unroll
    for (int ks = 0; ks < 8; ++ks) vt[ks][0] = tr128(L, tro0 + 4096u * ks, tro1 + 4096u * ks);
#pragma unroll
    for (int s = 0; s < 4; ++s) { fa[0][s] = lds128(L, qa[s]); fa[1][s] = lds128(L, qa[s] + 32768u); qa[s] += 65536u; }
    tro0 += 65536u; tro1 += 65536u;
    PIN(PIN4(qa), "+v"(tro0), "+v"(tro1));
    STAMP(2);
    GATEV(3, G_T);
    STAMP(3);
#define GATHER_Q() { ld_sc1<0>(g[0], qsrc, l16); ld_sc1<1024>(g[1], qsrc, l16); ld_sc1<2048>(g[2], qsrc, l16); ld_sc1<3072>(g[3], qsrc, l16); ld_sc1<0>(gc, csrc, co); }
#define WAIT_Q(n) asm volatile("s_waitcnt vmcnt(%5)" : PIN4(g), "+v"(gc) : "i"(n) : "memory")
#define PUT_Q() _Pragma("unroll") for (int i = 0; i < 4; ++i) *(LAS v4u*)(L + qw[i]) = g[i];
#define BAD_Q() orx(rowsum(sum4(g[0]) + sum4(g[1]) + sum4(g[2]) + sum4(g[3]) - ((gc.x ^ ta) & cm)), gc.y, tb)
    GATHER_Q()
    RBAR();
    dma2b(kv3, k2, k3, s3w);
    STAMP(4);
    WAIT_Q(2);
    STAMP(5);
    PUT_Q()
    u64 badq = __builtin_amdgcn_ballot_w64(BAD_Q() != 0u);
    if (badq) {
      _Pragma("nounroll") for (int it = 0; it < SPIN_CAP && badq; ++it) {
        u64 rows = (badq | (badq >> 8)) & 0x00FF00FF00FF00FFull;
        rows = (rows | (rows >> 4)) & 0x000F000F000F000Full; rows = (rows | (rows >> 2)) & 0x0003000300030003ull; rows = (rows | (rows >> 1)) & 0x0001000100010001ull;
        rows *= 0xFFFFull;
        u64 sv; u32 cnt;
        asm volatile("s_mov_b64 %[sv], exec\n\ts_mov_b64 exec, %[rows]\n\ts_mov_b32 %[cnt], 0x20000\n"
                     "1:\n\tglobal_load_dwordx4 v[248:251], %[co], %[cs] sc1\n\ts_waitcnt vmcnt(0)\n\t"
                     "v_cmp_ne_u32_e32 vcc, %[tb], v249\n\ts_and_b64 exec, exec, vcc\n\ts_cbranch_scc0 2f\n\ts_sub_u32 %[cnt], %[cnt], 1\n\ts_cbranch_scc0 1b\n"
                     "2:\n\ts_mov_b64 exec, %[rows]\n\t"
                     "global_load_dwordx4 %[g0], %[go], %[qs] sc1\n\tglobal_load_dwordx4 %[g1], %[go], %[qs] offset:1024 sc1\n\t"
                     "global_load_dwordx4 %[g2], %[go], %[qs] offset:2048 sc1\n\tglobal_load_dwordx4 %[g3], %[go], %[qs] offset:3072 sc1\n\t"
                     "s_waitcnt vmcnt(0)\n\ts_mov_b64 exec, %[sv]"
                     : "+{v[248:251]}"(gc), [g0] "+v"(g[0]), [g1] "+v"(g[1]), [g2] "+v"(g[2]), [g3] "+v"(g[3]), [sv] "=&s"(sv), [cnt] "=&s"(cnt)
                     : [rows] "s"(rows), [co] "v"(co), [cs] "s"(csrc), [go] "v"(l16), [qs] "s"(qsrc), [tb] "s"(tb) : "memory", "vcc", "scc");
        STAMPV(30, it + 1);
        badq = __builtin_amdgcn_ballot_w64(BAD_Q() != 0u);
      }
      PUT_Q()
    }
    BAR();
    STAMP(7);
    {
      v4u fb[4][4];
#pragma unroll
      for (int s = 0; s < 4; ++s) fb[0][s] = lds128(L, qb[s]);
#pragma unroll
      for (int s = 0; s < 4; ++s) fb[1][s] = lds128(L, qb[s] + 8192u);
#pragma unroll
      for (int s = 0; s < 4; ++s) { fa[2][s] = lds128(L, qa[s]); fb[2][s] = lds128(L, qb[s] + 16384u); }
      SB();
      f32x16 acc;
#pragma unroll
      for (int i = 0; i < 16; ++i) acc[i] = 0.f;
#pragma unroll
      for (int s = 0; s < 4; ++s) acc = mfma32(fa[0][s], fb[0][s], acc);
      SB();
      ld_nt<0>(uv[0], uv0, uvo); ld_nt<0>(uv[1], uv1, uvo); ld_nt<0>(uv[2], uv2, uvo); ld_nt<0>(uv[3], uv3, uvo);
#pragma unroll
      for (int s = 0; s < 4; ++s) fb[3][s] = lds128(L, qb[s] + 24576u);
      SB();
#pragma unroll
      for (int t = 1; t < 3; ++t)
#pragma unroll
        for (int s = 0; s < 4; ++s) acc = mfma32(fa[t][s], fb[t][s], acc);
      PIN("+v"(acc));
      WAITVM(4); BAR();
      STAMP(8);
#pragma unroll
      for (int s = 0; s < 4; ++s) fa[0][s] = lds128(L, qa[s] + 32768u);
#pragma unroll
      for (int s = 0; s < 4; ++s) acc = mfma32(fa[0][s], fb[3][s], acc);
#pragma unroll
      for (int a = 0; a < 4; ++a) *(LAS f32x4*)(L + sw_ + 32u * a) = f32x4{acc[4 * a], acc[4 * a + 1], acc[4 * a + 2], acc[4 * a + 3]};
    }
    BAR();
    STAMP(9);
    float p[8], m2;
    {
      const f32x4 s0 = *(LAS f32x4*)(L + sr) + *(LAS f32x4*)(L + sr + 16896u), s1 = *(LAS f32x4*)(L + sr + 16u) + *(LAS f32x4*)(L + sr + 16896u + 16u);
      m2 = rowmax_(max8_(s0, s1)) * SC2;
#pragma unroll
      for (int i = 0; i < 4; ++i) { p[i] = __builtin_amdgcn_exp2f(fmaf(s0[i], SC2, -m2)); p[4 + i] = __builtin_amdgcn_exp2f(fmaf(s1[i], SC2, -m2)); }
      *(LAS v4u*)(L + pw) = v4u{pk2(p[0], p[1]), pk2(p[2], p[3]), pk2(p[4], p[5]), pk2(p[6], p[7])};
      SB();
      BAR();
    }
    STAMP(10);
    {
      v4u pf[8];
#pragma unroll
      for (int s = 0; s < 8; ++s) pf[s] = lds128(L, pos[s]);
      SB();
      f32x16 o0, o1;
#pragma unroll
      for (int i = 0; i < 16; ++i) { o0[i] = 0.f; o1[i] = 0.f; }
#pragma unroll
      for (int s = 0; s < 4; ++s) {
        o0 = mfma32(vt[s][0], pf[s], o0);
        vt[s][1] = tr128(L, tro0 + 4096u * s, tro1 + 4096u * s);
        SB();
      }
#pragma unroll
      for (int s = 4; s < 8; ++s) {
        o0 = mfma32(vt[s][0], pf[s], o0);
        vt[s][1] = tr128(L, tro0 + 4096u * s, tro1 + 4096u * s);
        SB();
      }
      o1 = mfma32(vt[0][1], pf[0], o1);
      float l = ((p[0] + p[1]) + (p[2] + p[3])) + ((p[4] + p[5]) + (p[6] + p[7]));
      SB();
      o1 = mfma32(vt[1][1], pf[1], o1);
      l += dppf<XOR1>(l); l += dppf<XOR2>(l);
      SB();
      o1 = mfma32(vt[2][1], pf[2], o1);
      l += dppf<ROR4>(l); l += dppf<ROR8>(l);
      *(LAS f32x2*)(L + mlw) = f32x2{m2, l};
      SB();
      o1 = mfma32(vt[3][1], pf[3], o1);
      SB();
      u32 ck = 0;
#pragma unroll
      for (int a = 0; a < 4; ++a) {
        o1 = mfma32(vt[4 + a][1], pf[4 + a], o1);
        const v2u t2 = v2u{pk2(o0[4 * a], o0[4 * a + 1]), pk2(o0[4 * a + 2], o0[4 * a + 3])};
        *(LAS v2u*)(L + sb[a]) = t2;
        ck += t2.x + t2.y;
        SB();
      }
      v2u t2[4];
#pragma unroll
      for (int a = 0; a < 4; ++a) {
        t2[a] = v2u{pk2(o1[4 * a], o1[4 * a + 1]), pk2(o1[4 * a + 2], o1[4 * a + 3])};
        *(LAS v2u*)(L + sb[4 + a]) = t2[a];
      }
      v4u t[4];
#pragma unroll
      for (int i = 0; i < 4; ++i) t[i] = lds128(L, rb + 1024u * i);
      SB();
#pragma unroll
      for (int a = 0; a < 4; ++a) ck += t2[a].x + t2[a].y;
      {
        auto sw = __builtin_amdgcn_permlane32_swap(ck, ck, false, false);
        *(LAS u32*)(L + ckw) = (sw[0] + sw[1]) ^ ta;
      }
      SB();
#define TRAILER() if (w < 2) { v4u t_ = lds128(L, tro_); if (p4 == 0u) t_ = v4u{t_.x, ta, t_.y, tb}; st_sc1(tr_dst, l16, t_); }
      if (TR_FIRST) { BAR(); TRAILER() RBAR(); }
      st_sc1(od0, so, t[0]); st_sc1(od1, so, t[1]); st_sc1(od2, so, t[2]); st_sc1(od3, so, t[3]);
    }
    STAMP(11);
    if (!TR_FIRST) { BAR(); TRAILER() }
    STAMP(12);
    const u32 bb = (u32)w >> 2, half = ((u32)w >> 1) & 1u, rh = (u32)w & 1u;
    const char* fsrc = ws + WS_MLA + bb * 131072u + hh_ * 64u;
    const char* fsrc2 = fsrc + 16u + 8u * qq_;
    const char* psrc = ws + WS_OA + ((bb * 32u + hh_) * 4u + qq_) * 16384u + rh * 8192u + half * 128u;
    const char* psrc2 = psrc + 4096;
    const char* wo_src = wo + hh_ * (128u * 1024u) + qq_ * 256u;
    const char* ctx_src = ws + WS_CTX + hh_ * 8192u;
    u32 pso = r8 * 256u + k8 * 16u, fo = 2048u * lane, wr2 = L_W + 1024u * bb + 512u * rh + 16u * r8 + 8u, wr = wr2 - 8u + 4u * half, ww = L_W + 1024u * bb + l16;
    u32 iw = L_IMG + 272u * (bb + 2u * (r8 & 1u) + 4u * rh) + 2u * (64u * half + 8u * k8 + 4u * hf + 2u * (r4 & 1u));
    u32 b7 = L_IMG + 272u * k8 + 16u * r4, i5w = L_IMG + 4096u + g4d, i5r = L_IMG + 4096u + bf1;
    u32 wb0 = (16u * w + r4) * 1024u + swz, wb1 = (wb0 ^ 16u) + 3072u, wb2 = (wb0 ^ 32u) + 6144u, wb3 = (wb0 ^ 48u) + 9216u, s4w = L_S4 + lw;
    {
      const u32 pto = L_S2 + lw + 256u * r4 + swz;
      asm volatile("s_waitcnt vmcnt(4)" : PIN4(uv)::"memory");
#pragma unroll
      for (int i = 0; i < 4; ++i) *(LAS v4u*)(L + (pto ^ (16u * i)) + 1024u * i) = uv[i];
    }
    BAR();
    v4u wa[4];
    read_wfrags(L, wf0 - (L_S4 - L_S2), wf1 - (L_S4 - L_S2), wa);
    PIN("+v"(pso), "+v"(fo), "+v"(wr), "+v"(wr2), "+v"(ww), "+v"(iw), "+v"(b7), "+v"(i5w), "+v"(i5r), "+v"(wb0), "+v"(wb1), "+v"(wb2), "+v"(wb3), PIN4(wa)); PIN("+s"(fsrc), "+s"(fsrc2), "+s"(psrc), "+s"(psrc2), "+s"(wo_src), "+s"(ctx_src), "+s"(s4w));
    {
      const bool poller = (w & 3) == 0;
      v4u f; v2u c2;
      const bool wo_now = now() - t0 < TV(9, WO_T);
      if (wo_now) dma4(wo_src, wb0, wb1, wb2, wb3, s4w);
      NAPV(5, NAP_H3);
      if (poller) poll1x(f, c2, fsrc, fsrc2, fo, ta, tb);
      if (poller) NAPV(13, NAP_REC);
      STAMP(13);
      RBAR();
      v4u d0, d1, d2, d3;
      ld_sc1<0>(d0, psrc, pso); ld_sc1<2048>(d1, psrc, pso); ld_sc1<0>(d2, psrc2, pso); ld_sc1<2048>(d3, psrc2, pso);
      if (poller) {
        float M = rowmax_(__uint_as_float(f.x) + 0.f);
        M = sw16_max(M); M = sw32_max(M);
        const float e = __builtin_amdgcn_exp2f(__uint_as_float(f.x) - M);
        float Ls = e * __uint_as_float(f.z);
        Ls += dppf<XOR1>(Ls); Ls += dppf<XOR2>(Ls); Ls += dppf<ROR4>(Ls); Ls += dppf<ROR8>(Ls); Ls = sw16_add(Ls, Ls); Ls = sw32_add(Ls, Ls);
        const u32 wv = __float_as_uint(e * __builtin_amdgcn_rcpf(Ls));
        *(LAS v4u*)(L + ww) = v4u{c2.x ^ ta, c2.y ^ ta, wv, wv};
      }
      BAR();
      if (!wo_now) dma4(wo_src, wb0, wb1, wb2, wb3, s4w);
      f32x2 wi[4]; u32 ci[4];
#pragma unroll
      for (int i = 0; i < 4; ++i) { ci[i] = *(LAS u32*)(L + wr + 128u * i); wi[i] = *(LAS f32x2*)(L + wr2 + 128u * i); }
      u32 cs4 = (ci[0] + ci[1]) + (ci[2] + ci[3]);
      u64 odd = __builtin_amdgcn_ballot_w64(r8 & 1u);
      PIN(PIN4(wi), "+v"(cs4)); PIN("+s"(odd));
      if (wo_now) WAITVM(0);
      asm volatile("s_waitcnt vmcnt(4)" : "+v"(d0), "+v"(d1), "+v"(d2), "+v"(d3)::"memory");
      STAMP(14);
      auto combine = [&]() {
        f32x2 a[4];
#pragma unroll
        for (int k = 0; k < 4; ++k) {
          a[k] = f32x2{bflo(d0[k]), bfhi(d0[k])} * wi[0];
          a[k] = __builtin_elementwise_fma(f32x2{bflo(d1[k]), bfhi(d1[k])}, wi[1], a[k]);
          a[k] = __builtin_elementwise_fma(f32x2{bflo(d2[k]), bfhi(d2[k])}, wi[2], a[k]);
          a[k] = __builtin_elementwise_fma(f32x2{bflo(d3[k]), bfhi(d3[k])}, wi[3], a[k]);
        }
        u32 x = (sum4(d0) + sum4(d1)) + (sum4(d2) + sum4(d3));
        x += (u32)__builtin_amdgcn_mov_dpp((int)x, XOR1, 0xf, 0xf, true); x += (u32)__builtin_amdgcn_mov_dpp((int)x, XOR2, 0xf, 0xf, true);
        x += (u32)__builtin_amdgcn_mov_dpp((int)x, HMIR, 0xf, 0xf, true);
        const float r0 = sw32_add(a[0][0], a[2][0]), r1 = sw32_add(a[0][1], a[2][1]), r2 = sw32_add(a[1][0], a[3][0]), r3 = sw32_add(a[1][1], a[3][1]);
        float u0 = sw16_add(r0, r2), u1 = sw16_add(r1, r3);
        u0 += dppf<ROR8>(u0); u1 += dppf<ROR8>(u1);
        const u32 hi = pk2(u0, u1), lo = pk2(u0 - bflo(hi), u1 - bfhi(hi));
        u32 sel;
        asm("v_cndmask_b32_e64 %0, %1, %2, %3" : "=v"(sel) : "v"(hi), "v"(lo), "s"(odd));
        *(LAS u32*)(L + iw) = sel;

        return __builtin_amdgcn_ballot_w64(x != cs4) == 0ull;
      };
      const bool ok_ = combine();
      STAMP(20);
      if (!ok_) {
        const char* cs = fsrc2 + 4u * half + rh * 65536u;
        _Pragma("nounroll") for (int it = 0; it < SPIN_CAP; ++it) {
          STAMPV(29, t0 + it + 1);
          ld_sc1<0>(d0, psrc, pso); ld_sc1<2048>(d1, psrc, pso); ld_sc1<0>(d2, psrc2, pso); ld_sc1<2048>(d3, psrc2, pso);
          asm volatile("global_load_dword %0, %4, %5 sc1\n\tglobal_load_dword %1, %4, %6 sc1\n\tglobal_load_dword %2, %4, %7 sc1\n\tglobal_load_dword %3, %4, %8 sc1\n\ts_waitcnt vmcnt(0)"
                       : "=&v"(ci[0]), "=&v"(ci[1]), "=&v"(ci[2]), "=&v"(ci[3]) : "v"(2048u * r8), "s"(cs), "s"(cs + 16384), "s"(cs + 32768), "s"(cs + 49152) : "memory");
          PIN("+v"(d0), "+v"(d1), "+v"(d2), "+v"(d3));
          cs4 = ((ci[0] ^ ta) + (ci[1] ^ ta)) + ((ci[2] ^ ta) + (ci[3] ^ ta));
          if (combine()) break;
        }
      }
      BAR();
      v4u bf[4]; read_bfrags(L, b7, bf);
      STAMP(21);
      f32x4 d = mv4(wa, bf);
      float v0 = pr2 ? d[2] : d[0], v1 = pr2 ? d[3] : d[1], z0 = pr2 ? d[0] : d[2], z1 = pr2 ? d[1] : d[3];
      v0 += dppf<XOR2>(z0); v1 += dppf<XOR2>(z1);
      v0 += dppf<ROR4>(v0); v1 += dppf<ROR4>(v1);
      tg.x = __float_as_uint(v0); tg.z = __float_as_uint(v1);
      st_mask(ctx_dst, pubo, tg, mc4);
    }
    STAMP(15);
    NAPV(6, NAP_HOP);
    *(LAS u32*)(L + i5w) = quad_pk(poll1(ctx_src, g4o, ta, tb));
    STAMP(16);
    BAR();
    {
      v4u bf[4], wb[4];
      read_bfrags(L, i5r, bf);
      read_wfrags(L, wf0, wf1, wb);
      publish_d(outp, pubo, mv4(wb, bf), pr2, tg, mc4);
    }
    STAMP(17);
    if (FIN_X ? (idx & 7) == 0 : idx < 32) {
      FINAL_REDUCE()
      if (idx == 0 && w == 0 && lane == 0) __hip_atomic_store((GAS u32*)(ws + WS_E), e0 + 1u, __ATOMIC_RELAXED, __HIP_MEMORY_SCOPE_AGENT);
    }
    STAMP(18);
  }
}

extern "C" void kernel_launch(void* const* d_in, const int* in_sizes, int n_in, void* d_out, int out_size, void* d_ws, size_t ws_size,
                              hipStream_t stream) {
  fused_decode<<<256, 512, 0, stream>>>((const u16*)d_in[0], (const u16*)d_in[1], (const u16*)d_in[2], (const u16*)d_in[3], (const u16*)d_in[4],
                                        (const u16*)d_in[5], (const u16*)d_in[6], (const u16*)d_in[7], (const u16*)d_in[8], (const u16*)d_in[9],
                                        (const u16*)d_in[10], (u16*)d_out, (char*)d_ws
  );
}
```
